```python
import torch

def _softmax(x, axis=-1):
    return torch.softmax(x, dim=axis)
def _gelu(x):
    return torch.nn.functional.gelu(x, approximate="tanh")
def _silu(x):
    return torch.nn.functional.silu(x)
def _sigmoid(x):
    return torch.sigmoid(x)
def _layernorm(x, axis=-1, eps=1e-5):
    m = x.mean(dim=axis, keepdim=True)
    v = x.var(dim=axis, keepdim=True, unbiased=False)
    return (x - m) / torch.sqrt(v + eps)
def _scatter_add(tgt, idx, src, axis=0):
    return tgt.clone().index_add_(axis, idx, src)
def _segment_sum(src, seg, n):
    out = torch.zeros((n,) + src.shape[1:], dtype=src.dtype)
    return out.index_add_(0, seg, src)
def _pad(x, pw):
    flat = tuple(v for pair in reversed(pw) for v in pair)
    return torch.nn.functional.pad(x, flat)

kernel_name = 'standalone__attention_interact_autoint__recsys'

def setup_inputs():

    _w = (lambda a: (lambda t: t.to(torch.bfloat16) if t.is_floating_point() else t)(torch.as_tensor(a)))
    B, F = 4096, 26
    D = max(2, (128 // 2) * 2)
    H = 2
    Dh = D // H
    return {
        "e": _w((lambda *s: torch.randn(*s, dtype=torch.bfloat16) * 0.05)(B, F, D).to(float)),
        "wq": _w((lambda *s: torch.randn(*s, dtype=torch.bfloat16) * 1.6)(H, D, Dh).to(float)),
        "wk": _w((lambda *s: torch.randn(*s, dtype=torch.bfloat16) * 1.6)(H, D, Dh).to(float)),
        "wv": _w((lambda *s: torch.randn(*s, dtype=torch.bfloat16) * 0.05)(H, D, Dh).to(float)),
        "wres": _w((lambda *s: torch.randn(*s, dtype=torch.bfloat16) * 0.05)(D, D).to(float)),
    }

def reference(e, wq, wk, wv, wres):
    H, D, Dh = wq.shape[0], wq.shape[1], wq.shape[2]
    B, F = e.shape[0], e.shape[1]
    q = torch.einsum('bfd,hdk->bhfk', e, wq)
    k = torch.einsum('bfd,hdk->bhfk', e, wk)
    v = torch.einsum('bfd,hdk->bhfk', e, wv)
    scale = float(Dh) ** -0.5
    scores = torch.einsum('bhfk,bhgk->bhfg', q, k) * scale
    attn = _softmax(scores, axis=-1)
    attn = (lambda x, dt: x.to(dt))(attn, v.dtype)
    ctx = torch.einsum('bhfg,bhgk->bhfk', attn, v)
    ctx = (lambda x, *d: x.permute(*d))(ctx, 0, 2, 1, 3).reshape(B, F, H * Dh)
    res = torch.einsum('bfd,de->bfe', e, wres)
    out = ctx + res
    mask = (lambda x, dt: x.to(dt))(out > 0, out.dtype)
    return out * mask
```

```cpp
#include <hip/hip_runtime.h>
#include <hip/hip_bf16.h>


typedef __attribute__((ext_vector_type(8))) __bf16 bf16x8;
typedef __attribute__((ext_vector_type(2))) __bf16 bf16x2;
typedef __attribute__((ext_vector_type(4))) short s16x4;
typedef __attribute__((ext_vector_type(8))) short s16x8;
typedef __attribute__((ext_vector_type(16))) float f32x16;
typedef __attribute__((ext_vector_type(4))) unsigned u32x4;
typedef __attribute__((address_space(3))) s16x4* lds_s16x4_ptr;
typedef __attribute__((address_space(3))) void* lds_void_ptr;

#define DI __device__ __forceinline__
#define MFMA(a, b, c) __builtin_amdgcn_mfma_f32_32x32x16_bf16((a), (b), (c), 0, 0, 0)

constexpr int NB_TOTAL = 4096;
constexpr int NF = 26;
constexpr int ND = 128;
constexpr int WAVES = 8;
constexpr int BPW = 2;

DI unsigned pack2(float lo, float hi) {
  bf16x2 v;
  v[0] = (__bf16)lo;
  v[1] = (__bf16)hi;
  return __builtin_bit_cast(unsigned, v);
}

DI bf16x8 pack_step(const f32x16& x, int s) {
  u32x4 p;
  p[0] = pack2(x[8 * s + 0], x[8 * s + 1]);
  p[1] = pack2(x[8 * s + 2], x[8 * s + 3]);
  p[2] = pack2(x[8 * s + 4], x[8 * s + 5]);
  p[3] = pack2(x[8 * s + 6], x[8 * s + 7]);
  return __builtin_bit_cast(bf16x8, p);
}

DI bf16x8 wfrag(const char* lbase, int panel, int t, int s) {
  const char* p = lbase + panel * 16384 + s * 2048 + t * 256;
  s16x4 lo = __builtin_amdgcn_ds_read_tr16_b64_v4i16((lds_s16x4_ptr)(p));
  s16x4 hi = __builtin_amdgcn_ds_read_tr16_b64_v4i16((lds_s16x4_ptr)(p + 512));
  return __builtin_bit_cast(bf16x8, __builtin_shufflevector(lo, hi, 0, 1, 2, 3, 4, 5, 6, 7));
}

DI float bf16_round(float x) {
  return (float)(__bf16)x;
}

__global__ __launch_bounds__(WAVES * 64, 2) void autoint_kernel(
    const __bf16* __restrict__ e, const __bf16* __restrict__ wq, const __bf16* __restrict__ wk,
    const __bf16* __restrict__ wv, const __bf16* __restrict__ wres, __bf16* __restrict__ out) {
  extern __shared__ __attribute__((aligned(16))) char smem[];
  const int tid = threadIdx.x;
  const int lane = tid & 63;
  const int wave = __builtin_amdgcn_readfirstlane(tid >> 6);
  const int r = lane & 31, h = lane >> 5;

  {
#pragma unroll
    for (int panel = 0; panel < 8; ++panel) {
      const int m = panel >> 1, hd = panel & 1;
#pragma unroll
      for (int i = 0; i < 2; ++i) {
        const int pos = (wave * 2 + i) * 64 + lane;
        const int blk2 = pos >> 4;
        const int dq = blk2 >> 1, t = blk2 & 1;
        const int q = (pos >> 2) & 3, pb = (pos >> 1) & 1, blk = pos & 1;
        const int d = 4 * dq + q;
        const int c8 = 4 * pb + 2 * t + blk;
        const __bf16* src;
        if (m == 0) src = wq + hd * 8192 + d * 64 + c8 * 8;
        else if (m == 1) src = wk + hd * 8192 + d * 64 + c8 * 8;
        else if (m == 2) src = wv + hd * 8192 + d * 64 + c8 * 8;
        else src = wres + d * 128 + hd * 64 + c8 * 8;
        __builtin_amdgcn_global_load_lds((const void*)src,
                                         (lds_void_ptr)(smem + panel * 16384 + (wave * 2 + i) * 1024), 16, 0, 0);
      }
    }
  }

  const int q4 = (lane & 15) >> 2, p4 = lane & 3, blk = (lane >> 4) & 1;
  const char* lbase = smem + h * 1024 + 64 * q4 + 32 * (p4 & 1) + 16 * blk + 8 * (p4 >> 1);

  const int rr = r < NF ? r : NF - 1;
  const float cexp = 0.125f * 1.4426950408889634f;

  bf16x8 E[8];
  {
    const int b = blockIdx.x * (WAVES * BPW) + wave;
    const __bf16* ep = e + ((size_t)b * NF + rr) * ND + 8 * h;
#pragma unroll
    for (int s = 0; s < 8; ++s) E[s] = *(const bf16x8*)(ep + 16 * s);
  }

  __builtin_amdgcn_s_waitcnt(0);
  __syncthreads();

#pragma unroll 1
  for (int it = 0; it < BPW; ++it) {
    const int b = blockIdx.x * (WAVES * BPW) + it * WAVES + wave;
    __bf16* op = out + ((size_t)b * NF + r) * ND + 32 * h;

#pragma unroll
    for (int hd = 0; hd < 2; ++hd) {
      bf16x8 Qp[2][2], Kp[2][2];
#pragma unroll
      for (int t = 0; t < 2; ++t) {
        f32x16 acc = {};
#pragma unroll
        for (int s = 0; s < 8; ++s) acc = MFMA(wfrag(lbase, 0 + hd, t, s), E[s], acc);
        Qp[t][0] = pack_step(acc, 0);
        Qp[t][1] = pack_step(acc, 1);
      }
#pragma unroll
      for (int t = 0; t < 2; ++t) {
        f32x16 acc = {};
#pragma unroll
        for (int s = 0; s < 8; ++s) acc = MFMA(wfrag(lbase, 2 + hd, t, s), E[s], acc);
        Kp[t][0] = pack_step(acc, 0);
        Kp[t][1] = pack_step(acc, 1);
      }
      f32x16 S = {};
#pragma unroll
      for (int t = 0; t < 2; ++t)
#pragma unroll
        for (int ks = 0; ks < 2; ++ks) S = MFMA(Kp[t][ks], Qp[t][ks], S);

      float sv[14];
#pragma unroll
      for (int i = 0; i < 14; ++i) sv[i] = bf16_round(S[i]);
      float m = sv[0];
#pragma unroll
      for (int i = 1; i < 12; ++i) m = fmaxf(m, sv[i]);
      float m2 = fmaxf(m, fmaxf(sv[12], sv[13]));
      m = h == 0 ? m2 : m;
      {
        auto sw = __builtin_amdgcn_permlane32_swap(__float_as_uint(m), __float_as_uint(m), false, false);
        m = fmaxf(__uint_as_float(sw[0]), __uint_as_float(sw[1]));
      }
      f32x16 P;
      float sum = 0.f;
#pragma unroll
      for (int i = 0; i < 12; ++i) {
        P[i] = __builtin_amdgcn_exp2f((sv[i] - m) * cexp);
        sum += P[i];
      }
      {
        float p12 = __builtin_amdgcn_exp2f((sv[12] - m) * cexp);
        float p13 = __builtin_amdgcn_exp2f((sv[13] - m) * cexp);
        P[12] = h == 0 ? p12 : 0.f;
        P[13] = h == 0 ? p13 : 0.f;
        sum += P[12] + P[13];
      }
      {
        auto sw = __builtin_amdgcn_permlane32_swap(__float_as_uint(sum), __float_as_uint(sum), false, false);
        sum = __uint_as_float(sw[0]) + __uint_as_float(sw[1]);
      }
      const float inv = __builtin_amdgcn_rcpf(sum);
#pragma unroll
      for (int i = 0; i < 14; ++i) P[i] *= inv;
      P[14] = 0.f;
      P[15] = 0.f;
      bf16x8 Pp[2];
      Pp[0] = pack_step(P, 0);
      Pp[1] = pack_step(P, 1);

#pragma unroll
      for (int t = 0; t < 2; ++t) {
        f32x16 acc = {};
#pragma unroll
        for (int s = 0; s < 8; ++s) acc = MFMA(E[s], wfrag(lbase, 4 + hd, t, s), acc);
        bf16x8 V0 = pack_step(acc, 0), V1 = pack_step(acc, 1);
        f32x16 o = {};
#pragma unroll
        for (int s = 0; s < 8; ++s) o = MFMA(wfrag(lbase, 6 + hd, t, s), E[s], o);
        o = MFMA(V0, Pp[0], o);
        o = MFMA(V1, Pp[1], o);
#pragma unroll
        for (int i = 0; i < 16; ++i) o[i] = fmaxf(o[i], 0.f);
        bf16x8 o0 = pack_step(o, 0), o1 = pack_step(o, 1);
        if (r < NF) {
          *(bf16x8*)(op + 64 * hd + 16 * t) = o0;
          *(bf16x8*)(op + 64 * hd + 16 * t + 8) = o1;
        }
      }
    }

    if (it + 1 < BPW) {
      const int bn = blockIdx.x * (WAVES * BPW) + (it + 1) * WAVES + wave;
      const __bf16* ep = e + ((size_t)bn * NF + rr) * ND + 8 * h;
#pragma unroll
      for (int s = 0; s < 8; ++s) E[s] = *(const bf16x8*)(ep + 16 * s);
    }
  }
}

extern "C" void kernel_launch(void* const* d_in, const int* in_sizes, int n_in, void* d_out, int out_size,
                              void* d_ws, size_t ws_size, hipStream_t stream) {
  const __bf16* e = (const __bf16*)d_in[0];
  const __bf16* wq = (const __bf16*)d_in[1];
  const __bf16* wk = (const __bf16*)d_in[2];
  const __bf16* wv = (const __bf16*)d_in[3];
  const __bf16* wres = (const __bf16*)d_in[4];
  __bf16* out = (__bf16*)d_out;
  const int lds = 131072;
  hipFuncSetAttribute((const void*)autoint_kernel, hipFuncAttributeMaxDynamicSharedMemorySize, lds);
  autoint_kernel<<<NB_TOTAL / (WAVES * BPW), WAVES * 64, lds, stream>>>(e, wq, wk, wv, wres, out);
}
```

```cpp
#include <hip/hip_runtime.h>
#include <hip/hip_bf16.h>

typedef __attribute__((ext_vector_type(8))) __bf16 bf16x8;
typedef __attribute__((ext_vector_type(2))) __bf16 bf16x2;
typedef __attribute__((ext_vector_type(4))) short s16x4;
typedef __attribute__((ext_vector_type(8))) short s16x8;
typedef __attribute__((ext_vector_type(16))) float f32x16;
typedef __attribute__((ext_vector_type(4))) unsigned u32x4;
typedef __attribute__((address_space(3))) s16x4* lds_s16x4_ptr;
typedef __attribute__((address_space(3))) u32x4* lds_u32x4_ptr;
typedef __attribute__((address_space(3))) void* lds_void_ptr;

#define DI __device__ __forceinline__
#define MFMA(a, b, c) __builtin_amdgcn_mfma_f32_32x32x16_bf16((a), (b), (c), 0, 0, 0)

#define ST_AUX 2

constexpr int NB_TOTAL = 4096;
constexpr int NF = 26;
constexpr int ND = 128;
constexpr int WAVES = 8;
constexpr int IPW = 4;
constexpr int BPG = WAVES * IPW;
constexpr int EBYTES = NF * ND * 2;
constexpr int LDS_BYTES = 65536 + WAVES * 12288;

DI unsigned pack2(float lo, float hi) {
  bf16x2 v;
  v[0] = (__bf16)lo;
  v[1] = (__bf16)hi;
  return __builtin_bit_cast(unsigned, v);
}

DI u32x4 pack_u(const f32x16& x, int s) {
  u32x4 p;
  p[0] = pack2(x[8 * s + 0], x[8 * s + 1]);
  p[1] = pack2(x[8 * s + 2], x[8 * s + 3]);
  p[2] = pack2(x[8 * s + 4], x[8 * s + 5]);
  p[3] = pack2(x[8 * s + 6], x[8 * s + 7]);
  return p;
}
DI bf16x8 pack_step(const f32x16& x, int s) { return __builtin_bit_cast(bf16x8, pack_u(x, s)); }

DI u32x4 relu_pack(const f32x16& x, int s) {
  s16x8 z = {};
  return __builtin_bit_cast(u32x4, __builtin_elementwise_max(__builtin_bit_cast(s16x8, pack_u(x, s)), z));
}

DI bf16x8 wfrag(unsigned lbase, int panel, int t, int s) {
  const unsigned p = lbase + panel * 16384 + s * 2048 + t * 256;
  s16x4 lo = __builtin_amdgcn_ds_read_tr16_b64_v4i16((lds_s16x4_ptr)(p));
  s16x4 hi = __builtin_amdgcn_ds_read_tr16_b64_v4i16((lds_s16x4_ptr)(p + 512));
  return __builtin_bit_cast(bf16x8, __builtin_shufflevector(lo, hi, 0, 1, 2, 3, 4, 5, 6, 7));
}

template <int IMM>
DI void glds16s(unsigned voff, const void* sbase, unsigned lds_dst) {
  unsigned keep;
  asm volatile(
      "s_mov_b32 %0, m0\n\ts_mov_b32 m0, %3\n\ts_nop 0\n\tglobal_load_lds_dwordx4 %1, %2 offset:%c4\n\ts_mov_b32 m0, %0"
      : "=&s"(keep)
      : "v"(voff), "s"(sbase), "s"(lds_dst), "i"(IMM)
      : "memory");
}

#define SGB(mask, n) __builtin_amdgcn_sched_group_barrier(mask, n, 0)
#define PATTERN(nv)                               \
  _Pragma("unroll") for (int s = 0; s < 8; ++s) { \
    SGB(0x8, 1);                                  \
    SGB(0x100, 2);                                \
    SGB(0x2, nv);                                 \
  }                                               \
  __builtin_amdgcn_sched_barrier(0);
#define PATTERN3(nv)                              \
  _Pragma("unroll") for (int s = 0; s < 8; ++s) { \
    SGB(0x8, 1);                                  \
    SGB(0x100, 3);                                \
    SGB(0x2, nv);                                 \
  }                                               \
  __builtin_amdgcn_sched_barrier(0);

#define WAITV(n) asm volatile("s_waitcnt vmcnt(" #n ")" ::: "memory");
#define WAITW(n)                  \
  WAITV(n)                        \
  __builtin_amdgcn_s_barrier();   \
  __builtin_amdgcn_sched_barrier(0);

#define STAMP(k)
#define LBAR()                                      \
  if (c.leader) __builtin_amdgcn_s_barrier();       \
  __builtin_amdgcn_sched_barrier(0);
struct Ctx {
  bool leader;
  unsigned long long* prof;
  unsigned lbase;
  unsigned eread;
  unsigned ebuf;
  unsigned owr;
  unsigned ord;
  unsigned evoff[4];
  int gvoff, gvoff3;
  int h, lane;
  const char* ebase;
  int obase;
  __amdgpu_buffer_rsrc_t rsrc;
};

#define LOADW(buf, panel, t) \
  _Pragma("unroll") for (int s = 0; s < 8; ++s) W[buf][s] = wfrag(c.lbase, (panel), (t), s);
#define EB(s) __builtin_bit_cast(bf16x8, E[s])

DI void dma_e(const Ctx& c, int item) {
  const char* p = c.ebase + (size_t)item * (WAVES * EBYTES);
  glds16s<0>(c.evoff[0], p, c.ebuf);
  glds16s<1024>(c.evoff[1], p, c.ebuf);
  glds16s<2048>(c.evoff[2], p, c.ebuf);
  glds16s<3072>(c.evoff[3], p, c.ebuf);
  const char* p2 = p + 4096;
  glds16s<0>(c.evoff[0], p2, c.ebuf + 4096);
  glds16s<1024>(c.evoff[1], p2, c.ebuf + 4096);
  if (c.lane < 32) glds16s<2048>(c.evoff[2], p2, c.ebuf + 4096);
}

DI void read_e(const Ctx& c, u32x4 (&E)[8]) {
#pragma unroll
  for (int s = 0; s < 8; ++s) E[s] = *(lds_u32x4_ptr)(c.eread ^ (s << 5));
}

template <int IT>
DI void item_body(const Ctx& c, const u32x4 (&E)[8], u32x4 (&E2)[8], bf16x8 (&W)[2][8], f32x16& o0, f32x16& o1) {
  constexpr bool FIRST = IT == 0;
  const float cexp = 0.125f * 1.4426950408889634f;
  const int h = c.h;
  bf16x8 Qp[2][2], Kp[2][2], Vp[2][2], Pp[2];
  f32x16 a0 = {}, a1 = {}, a2 = {}, a3 = {}, a4 = {}, a5 = {}, S = {};
  u32x4 ob[4];
  LOADW(1, 0, 1);
#pragma unroll
  for (int s = 0; s < 8; ++s) a0 = MFMA(W[0][s], EB(s), a0);
  if (!FIRST) {
    *(lds_u32x4_ptr)(c.owr ^ 0) = relu_pack(o0, 0);
    *(lds_u32x4_ptr)(c.owr ^ 16) = relu_pack(o0, 1);
    *(lds_u32x4_ptr)(c.owr ^ 32) = relu_pack(o1, 0);
    *(lds_u32x4_ptr)(c.owr ^ 48) = relu_pack(o1, 1);
  }
  PATTERN(5);
  if (FIRST) { LBAR(); }
  LOADW(0, 1, 0);
  if (!FIRST) {
#pragma unroll
    for (int j = 0; j < 4; ++j) ob[j] = *(lds_u32x4_ptr)(c.ord + j * 1024);
  }
#pragma unroll
  for (int s = 0; s < 8; ++s) a1 = MFMA(W[1][s], EB(s), a1);
  Qp[0][0] = pack_step(a0, 0);
  Qp[0][1] = pack_step(a0, 1);
  PATTERN3(2);
  LOADW(1, 1, 1);
#pragma unroll
  for (int s = 0; s < 8; ++s) a2 = MFMA(W[0][s], EB(s), a2);
  Qp[1][0] = pack_step(a1, 0);
  Qp[1][1] = pack_step(a1, 1);
  if (!FIRST) {
    const int so = c.obase + (IT - 1) * (WAVES * EBYTES);
    __builtin_amdgcn_raw_buffer_store_b128(ob[0], c.rsrc, c.gvoff, so, ST_AUX);
    __builtin_amdgcn_raw_buffer_store_b128(ob[1], c.rsrc, c.gvoff + 2048, so, ST_AUX);
    __builtin_amdgcn_raw_buffer_store_b128(ob[2], c.rsrc, c.gvoff, so + 4096, ST_AUX);
    __builtin_amdgcn_raw_buffer_store_b128(ob[3], c.rsrc, c.gvoff3, so + 4096, ST_AUX);
  }
  PATTERN(2);
  if (FIRST) { LBAR(); }
  LOADW(0, 2, 0);
#pragma unroll
  for (int s = 0; s < 8; ++s) a3 = MFMA(W[1][s], EB(s), a3);
  Kp[0][0] = pack_step(a2, 0);
  Kp[0][1] = pack_step(a2, 1);
  PATTERN(2);
  if (FIRST) { LBAR(); }
  if (IT + 1 < IPW) {
    if (FIRST) { WAITV(0); } else { WAITV(4); }
    __builtin_amdgcn_sched_barrier(0);
  }
  LOADW(1, 2, 1);
  if (IT + 1 < IPW) read_e(c, E2);
#pragma unroll
  for (int s = 0; s < 8; ++s) a4 = MFMA(EB(s), W[0][s], a4);
  Kp[1][0] = pack_step(a3, 0);
  Kp[1][1] = pack_step(a3, 1);
  PATTERN3(2);
  S = MFMA(Kp[0][0], Qp[0][0], S);
  S = MFMA(Kp[0][1], Qp[0][1], S);
  S = MFMA(Kp[1][0], Qp[1][0], S);
  S = MFMA(Kp[1][1], Qp[1][1], S);
  __builtin_amdgcn_sched_barrier(0);
  if (IT + 2 < IPW) {
    asm volatile("s_waitcnt lgkmcnt(0)" ::: "memory");
    dma_e(c, IT + 2);
    __builtin_amdgcn_sched_barrier(0);
  }
  LOADW(0, 3, 0);
#pragma unroll
  for (int s = 0; s < 8; ++s) a5 = MFMA(EB(s), W[1][s], a5);
  Vp[0][0] = pack_step(a4, 0);
  Vp[0][1] = pack_step(a4, 1);
  float sv[14];
#pragma unroll
  for (int i = 0; i < 7; ++i) {
    unsigned u = pack2(S[2 * i], S[2 * i + 1]);
    sv[2 * i] = __uint_as_float(u << 16);
    sv[2 * i + 1] = __uint_as_float(u & 0xffff0000u);
  }
  float m = sv[0];
#pragma unroll
  for (int i = 1; i < 12; ++i) m = fmaxf(m, sv[i]);
  float m2 = fmaxf(m, fmaxf(sv[12], sv[13]));
  m = h == 0 ? m2 : m;
  {
    auto sw = __builtin_amdgcn_permlane32_swap(__float_as_uint(m), __float_as_uint(m), false, false);
    m = fmaxf(__uint_as_float(sw[0]), __uint_as_float(sw[1]));
  }
  PATTERN(6);
  LOADW(1, 3, 1);
  o0 = f32x16{};
#pragma unroll
  for (int s = 0; s < 8; ++s) o0 = MFMA(W[0][s], EB(s), o0);
  Vp[1][0] = pack_step(a5, 0);
  Vp[1][1] = pack_step(a5, 1);
  f32x16 P;
  const float mc = m * cexp;
  float sum = 0.f;
#pragma unroll
  for (int i = 0; i < 12; ++i) {
    P[i] = __builtin_amdgcn_exp2f(__builtin_fmaf(sv[i], cexp, -mc));
    sum += P[i];
  }
  {
    float p12 = __builtin_amdgcn_exp2f(__builtin_fmaf(sv[12], cexp, -mc));
    float p13 = __builtin_amdgcn_exp2f(__builtin_fmaf(sv[13], cexp, -mc));
    P[12] = h == 0 ? p12 : 0.f;
    P[13] = h == 0 ? p13 : 0.f;
    sum += P[12] + P[13];
  }
  {
    auto sw = __builtin_amdgcn_permlane32_swap(__float_as_uint(sum), __float_as_uint(sum), false, false);
    sum = __uint_as_float(sw[0]) + __uint_as_float(sw[1]);
  }
  PATTERN(7);
  LOADW(0, 0, 0);
  o1 = f32x16{};
#pragma unroll
  for (int s = 0; s < 8; ++s) o1 = MFMA(W[1][s], EB(s), o1);
  const float inv = __builtin_amdgcn_rcpf(sum);
#pragma unroll
  for (int i = 0; i < 14; ++i) P[i] *= inv;
  P[14] = 0.f;
  P[15] = 0.f;
  Pp[0] = pack_step(P, 0);
  Pp[1] = pack_step(P, 1);
  PATTERN(3);
  o0 = MFMA(Vp[0][0], Pp[0], o0);
  o0 = MFMA(Vp[0][1], Pp[1], o0);
  o1 = MFMA(Vp[1][0], Pp[0], o1);
  o1 = MFMA(Vp[1][1], Pp[1], o1);
  __builtin_amdgcn_sched_barrier(0);
}

__global__ __launch_bounds__(WAVES * 64, 2) void autoint_kernel(
    const __bf16* __restrict__ e, const __bf16* __restrict__ wq, const __bf16* __restrict__ wk,
    const __bf16* __restrict__ wv, const __bf16* __restrict__ wres, __bf16* __restrict__ out, unsigned long long* prof) {
  extern __shared__ __attribute__((aligned(16))) char smem[];
  const int tid = threadIdx.x;
  const int lane = tid & 63;
  const int wave = __builtin_amdgcn_readfirstlane(tid >> 6);
  const int r = lane & 31, h = lane >> 5;
  const unsigned lds0 = __builtin_amdgcn_readfirstlane((unsigned)(size_t)(lds_void_ptr)smem);
  const int bid = blockIdx.x;
  const int hd = (bid >> 3) & 1;
  const int grp = (bid & 7) | ((bid >> 4) << 3);
  const int b0 = grp * BPG + wave;

  Ctx c;
  c.h = h;
  c.lane = lane;
  c.prof = prof + (size_t)(blockIdx.x * WAVES + wave) * 32;
  STAMP(0);
  c.ebuf = lds0 + 65536 + wave * 12288;
  c.ebase = (const char*)e + (size_t)b0 * EBYTES;
  c.obase = b0 * EBYTES;
  {
    const int lq = lane >> 4, c0 = (lane & 15) ^ lq;
#pragma unroll
    for (int j = 0; j < 4; ++j) c.evoff[j] = (unsigned)((lq * 256 + (c0 << 4)) ^ (j << 6));
  }
  const bool leader = wave < 4;
  c.leader = leader;
  if (leader) {
    dma_e(c, 0);
  } else {
    unsigned o64[4], o128[4];
#pragma unroll
    for (int i = 0; i < 4; ++i) {
      const int pos = ((wave - 4) * 4 + i) * 64 + lane;
      const int blk2 = pos >> 4;
      const int dq = blk2 >> 1, t = blk2 & 1;
      const int q = (pos >> 2) & 3, pb = (pos >> 1) & 1, blk = pos & 1;
      const int d = 4 * dq + q, c8 = 4 * pb + 2 * t + blk;
      o64[i] = (unsigned)(d * 128 + c8 * 16);
      o128[i] = (unsigned)(d * 256 + c8 * 16);
    }
    const char* sq = (const char*)wq + hd * 16384;
    const char* sk = (const char*)wk + hd * 16384;
    const char* sv = (const char*)wv + hd * 16384;
    const char* sr = (const char*)wres + hd * 128;
    const unsigned dst = lds0 + (wave - 4) * 4096;
#pragma unroll
    for (int i = 0; i < 4; ++i) glds16s<0>(o64[i], sq, dst + i * 1024);
#pragma unroll
    for (int i = 0; i < 4; ++i) glds16s<0>(o64[i], sk, dst + 16384 + i * 1024);
#pragma unroll
    for (int i = 0; i < 4; ++i) glds16s<0>(o64[i], sv, dst + 32768 + i * 1024);
#pragma unroll
    for (int i = 0; i < 4; ++i) glds16s<0>(o128[i], sr, dst + 49152 + i * 1024);
    dma_e(c, 0);
  }
  __builtin_amdgcn_sched_barrier(0);

  {
    const int q4 = (lane & 15) >> 2, p4 = lane & 3, blk = (lane >> 4) & 1;
    c.lbase = lds0 + h * 1024 + 64 * q4 + 32 * (p4 & 1) + 16 * blk + 8 * (p4 >> 1);
    asm volatile("" : "+v"(c.lbase));
    c.eread = c.ebuf + r * 256 + ((h ^ (r & 15)) << 4);
    const unsigned obuf = c.ebuf + 8192;
    c.owr = obuf + r * 128 + (((4 * h) ^ (r & 7)) << 4);
    c.ord = obuf + lane * 16;
    const int orow = lane >> 3, oc = (lane & 7) ^ (orow & 7);
    c.gvoff = orow * 256 + hd * 128 + oc * 16;
    c.gvoff3 = lane < 16 ? c.gvoff + 2048 : 0x40000000;
    c.rsrc = __builtin_amdgcn_make_buffer_rsrc((void*)out, 0, NB_TOTAL * EBYTES, 0x00020000);
  }
  {
    u32x4 z = {};
    *(lds_u32x4_ptr)(c.ebuf + NF * 256 + lane * 16) = z;
    if (lane < 32) *(lds_u32x4_ptr)(c.ebuf + NF * 256 + 1024 + lane * 16) = z;
  }

  u32x4 EA[8], EB2[8];
  STAMP(1);
  if (leader) {
    __builtin_amdgcn_s_barrier();
  } else {
    WAITW(19);
    WAITW(15);
    WAITW(11);
    WAITW(7);
  }
  WAITV(0);
  __builtin_amdgcn_sched_barrier(0);
  STAMP(2);
  read_e(c, EA);
  bf16x8 W[2][8];
  LOADW(0, 0, 0);
  asm volatile("s_waitcnt lgkmcnt(0)" ::: "memory");
  __builtin_amdgcn_sched_barrier(0);
  dma_e(c, 1);
  __builtin_amdgcn_sched_barrier(0);
  STAMP(3);

  f32x16 o0 = {}, o1 = {};
  item_body<0>(c, EA, EB2, W, o0, o1);
  STAMP(4);
  item_body<1>(c, EB2, EA, W, o0, o1);
  STAMP(5);
  item_body<2>(c, EA, EB2, W, o0, o1);
  STAMP(6);
  item_body<3>(c, EB2, EA, W, o0, o1);
  STAMP(7);
  {
    *(lds_u32x4_ptr)(c.owr ^ 0) = relu_pack(o0, 0);
    *(lds_u32x4_ptr)(c.owr ^ 16) = relu_pack(o0, 1);
    *(lds_u32x4_ptr)(c.owr ^ 32) = relu_pack(o1, 0);
    *(lds_u32x4_ptr)(c.owr ^ 48) = relu_pack(o1, 1);
    u32x4 ob[4];
#pragma unroll
    for (int j = 0; j < 4; ++j) ob[j] = *(lds_u32x4_ptr)(c.ord + j * 1024);
    const int so = c.obase + (IPW - 1) * (WAVES * EBYTES);
    __builtin_amdgcn_raw_buffer_store_b128(ob[0], c.rsrc, c.gvoff, so, ST_AUX);
    __builtin_amdgcn_raw_buffer_store_b128(ob[1], c.rsrc, c.gvoff + 2048, so, ST_AUX);
    __builtin_amdgcn_raw_buffer_store_b128(ob[2], c.rsrc, c.gvoff, so + 4096, ST_AUX);
    __builtin_amdgcn_raw_buffer_store_b128(ob[3], c.rsrc, c.gvoff3, so + 4096, ST_AUX);
  }
  STAMP(8);
}

extern "C" void kernel_launch(void* const* d_in, const int* in_sizes, int n_in, void* d_out, int out_size,
                              void* d_ws, size_t ws_size, hipStream_t stream) {
  const __bf16* e = (const __bf16*)d_in[0];
  const __bf16* wq = (const __bf16*)d_in[1];
  const __bf16* wk = (const __bf16*)d_in[2];
  const __bf16* wv = (const __bf16*)d_in[3];
  const __bf16* wres = (const __bf16*)d_in[4];
  __bf16* out = (__bf16*)d_out;
  (void)hipFuncSetAttribute((const void*)autoint_kernel, hipFuncAttributeMaxDynamicSharedMemorySize, LDS_BYTES);
  autoint_kernel<<<2 * NB_TOTAL / BPG, WAVES * 64, LDS_BYTES, stream>>>(e, wq, wk, wv, wres, out, (unsigned long long*)d_ws);
}
```

```cpp
#include <hip/hip_runtime.h>
#include <hip/hip_bf16.h>

typedef __attribute__((ext_vector_type(8))) __bf16 bf16x8;
typedef __attribute__((ext_vector_type(2))) __bf16 bf16x2;
typedef __attribute__((ext_vector_type(4))) short s16x4;
typedef __attribute__((ext_vector_type(8))) short s16x8;
typedef __attribute__((ext_vector_type(16))) float f32x16;
typedef __attribute__((ext_vector_type(4))) unsigned u32x4;
typedef __attribute__((address_space(3))) s16x4* lds_s16x4_ptr;
typedef __attribute__((address_space(3))) u32x4* lds_u32x4_ptr;
typedef __attribute__((address_space(3))) void* lds_void_ptr;

#define DI __device__ __forceinline__
#define MFMA(a, b, c) __builtin_amdgcn_mfma_f32_32x32x16_bf16((a), (b), (c), 0, 0, 0)

#define ST_AUX 2

constexpr int NB_TOTAL = 4096;
constexpr int NF = 26;
constexpr int ND = 128;
constexpr int WAVES = 8;
constexpr int IPW = 4;
constexpr int BPG = WAVES * IPW;
constexpr int EBYTES = NF * ND * 2;
constexpr int LDS_BYTES = 65536 + WAVES * 12288;

DI unsigned pack2(float lo, float hi) {
  bf16x2 v;
  v[0] = (__bf16)lo;
  v[1] = (__bf16)hi;
  return __builtin_bit_cast(unsigned, v);
}

DI u32x4 pack_u(const f32x16& x, int s) {
  u32x4 p;
  p[0] = pack2(x[8 * s + 0], x[8 * s + 1]);
  p[1] = pack2(x[8 * s + 2], x[8 * s + 3]);
  p[2] = pack2(x[8 * s + 4], x[8 * s + 5]);
  p[3] = pack2(x[8 * s + 6], x[8 * s + 7]);
  return p;
}
DI bf16x8 pack_step(const f32x16& x, int s) { return __builtin_bit_cast(bf16x8, pack_u(x, s)); }

DI u32x4 relu_pack(const f32x16& x, int s) {
  s16x8 z = {};
  return __builtin_bit_cast(u32x4, __builtin_elementwise_max(__builtin_bit_cast(s16x8, pack_u(x, s)), z));
}

DI bf16x8 wfrag(unsigned lbase, int panel, int t, int s) {
  const unsigned p = lbase + panel * 16384 + s * 2048 + t * 256;
  s16x4 lo = __builtin_amdgcn_ds_read_tr16_b64_v4i16((lds_s16x4_ptr)(p));
  s16x4 hi = __builtin_amdgcn_ds_read_tr16_b64_v4i16((lds_s16x4_ptr)(p + 512));
  return __builtin_bit_cast(bf16x8, __builtin_shufflevector(lo, hi, 0, 1, 2, 3, 4, 5, 6, 7));
}

template <int IMM>
DI void glds16s(unsigned voff, const void* sbase, unsigned lds_dst) {
  unsigned keep;
  asm volatile(
      "s_mov_b32 %0, m0\n\ts_mov_b32 m0, %3\n\ts_nop 0\n\tglobal_load_lds_dwordx4 %1, %2 offset:%c4\n\ts_mov_b32 m0, %0"
      : "=&s"(keep)
      : "v"(voff), "s"(sbase), "s"(lds_dst), "i"(IMM)
      : "memory");
}

#define SGB(mask, n) __builtin_amdgcn_sched_group_barrier(mask, n, 0)
#define PATTERN(nv)                               \
  _Pragma("unroll") for (int s = 0; s < 8; ++s) { \
    SGB(0x8, 1);                                  \
    SGB(0x100, 2);                                \
    SGB(0x2, nv);                                 \
  }                                               \
  __builtin_amdgcn_sched_barrier(0);
#define PATTERN3(nv)                              \
  _Pragma("unroll") for (int s = 0; s < 8; ++s) { \
    SGB(0x8, 1);                                  \
    SGB(0x100, 3);                                \
    SGB(0x2, nv);                                 \
  }                                               \
  __builtin_amdgcn_sched_barrier(0);

#define WAITV(n) asm volatile("s_waitcnt vmcnt(" #n ")" ::: "memory");
#define WAITW(n)                  \
  WAITV(n)                        \
  __builtin_amdgcn_s_barrier();   \
  __builtin_amdgcn_sched_barrier(0);

#define STAMP(k)
#define LBAR()                                      \
  if (c.leader) __builtin_amdgcn_s_barrier();       \
  __builtin_amdgcn_sched_barrier(0);
struct Ctx {
  bool leader;
  unsigned long long* prof;
  unsigned lbase;
  unsigned eread;
  unsigned ebuf;
  unsigned owr;
  unsigned ord;
  unsigned evoff[4];
  int gvoff, gvoff3;
  int h, lane;
  const char* ebase;
  int obase;
  __amdgpu_buffer_rsrc_t rsrc;
};

#define LOADW(buf, panel, t) \
  _Pragma("unroll") for (int s = 0; s < 8; ++s) W[buf][s] = wfrag(c.lbase, (panel), (t), s);
#define EB(s) __builtin_bit_cast(bf16x8, E[s])

DI void dma_e(const Ctx& c, int item) {
  const char* p = c.ebase + (size_t)item * (WAVES * EBYTES);
  glds16s<0>(c.evoff[0], p, c.ebuf);
  glds16s<1024>(c.evoff[1], p, c.ebuf);
  glds16s<2048>(c.evoff[2], p, c.ebuf);
  glds16s<3072>(c.evoff[3], p, c.ebuf);
  const char* p2 = p + 4096;
  glds16s<0>(c.evoff[0], p2, c.ebuf + 4096);
  glds16s<1024>(c.evoff[1], p2, c.ebuf + 4096);
  if (c.lane < 32) glds16s<2048>(c.evoff[2], p2, c.ebuf + 4096);
}

DI void read_e(const Ctx& c, u32x4 (&E)[8]) {
#pragma unroll
  for (int s = 0; s < 8; ++s) E[s] = *(lds_u32x4_ptr)(c.eread ^ (s << 5));
}

template <int IT>
DI void item_body(const Ctx& c, const u32x4 (&E)[8], u32x4 (&E2)[8], bf16x8 (&W)[2][8], f32x16& o0, f32x16& o1) {
  constexpr bool FIRST = IT == 0;
  const float cexp = 0.125f * 1.4426950408889634f;
  const int h = c.h;
  bf16x8 Qp[2][2], Kp[2][2], Vp[2][2], Pp[2];
  f32x16 a0 = {}, a1 = {}, a2 = {}, a3 = {}, a4 = {}, a5 = {}, S = {};
  u32x4 ob[4];
  LOADW(1, 0, 1);
#pragma unroll
  for (int s = 0; s < 8; ++s) a0 = MFMA(W[0][s], EB(s), a0);
  if (!FIRST) {
    *(lds_u32x4_ptr)(c.owr ^ 0) = relu_pack(o0, 0);
    *(lds_u32x4_ptr)(c.owr ^ 16) = relu_pack(o0, 1);
    *(lds_u32x4_ptr)(c.owr ^ 32) = relu_pack(o1, 0);
    *(lds_u32x4_ptr)(c.owr ^ 48) = relu_pack(o1, 1);
  }
  PATTERN(5);
  if (FIRST) { LBAR(); }
  LOADW(0, 1, 0);
  if (!FIRST) {
#pragma unroll
    for (int j = 0; j < 4; ++j) ob[j] = *(lds_u32x4_ptr)(c.ord + j * 1024);
  }
#pragma unroll
  for (int s = 0; s < 8; ++s) a1 = MFMA(W[1][s], EB(s), a1);
  Qp[0][0] = pack_step(a0, 0);
  Qp[0][1] = pack_step(a0, 1);
  PATTERN3(2);
  LOADW(1, 1, 1);
#pragma unroll
  for (int s = 0; s < 8; ++s) a2 = MFMA(W[0][s], EB(s), a2);
  Qp[1][0] = pack_step(a1, 0);
  Qp[1][1] = pack_step(a1, 1);
  if (!FIRST) {
    const int so = c.obase + (IT - 1) * (WAVES * EBYTES);
    __builtin_amdgcn_raw_buffer_store_b128(ob[0], c.rsrc, c.gvoff, so, ST_AUX);
    __builtin_amdgcn_raw_buffer_store_b128(ob[1], c.rsrc, c.gvoff + 2048, so, ST_AUX);
    __builtin_amdgcn_raw_buffer_store_b128(ob[2], c.rsrc, c.gvoff, so + 4096, ST_AUX);
    __builtin_amdgcn_raw_buffer_store_b128(ob[3], c.rsrc, c.gvoff3, so + 4096, ST_AUX);
  }
  PATTERN(2);
  if (FIRST) { LBAR(); }
  LOADW(0, 2, 0);
#pragma unroll
  for (int s = 0; s < 8; ++s) a3 = MFMA(W[1][s], EB(s), a3);
  Kp[0][0] = pack_step(a2, 0);
  Kp[0][1] = pack_step(a2, 1);
  PATTERN(2);
  if (FIRST) { LBAR(); }
  if (IT + 1 < IPW) {
    if (FIRST) { WAITV(0); } else { WAITV(4); }
    __builtin_amdgcn_sched_barrier(0);
  }
  LOADW(1, 2, 1);
  if (IT + 1 < IPW) read_e(c, E2);
#pragma unroll
  for (int s = 0; s < 8; ++s) a4 = MFMA(EB(s), W[0][s], a4);
  Kp[1][0] = pack_step(a3, 0);
  Kp[1][1] = pack_step(a3, 1);
  PATTERN3(2);
  S = MFMA(Kp[0][0], Qp[0][0], S);
  S = MFMA(Kp[0][1], Qp[0][1], S);
  S = MFMA(Kp[1][0], Qp[1][0], S);
  S = MFMA(Kp[1][1], Qp[1][1], S);
  __builtin_amdgcn_sched_barrier(0);
  if (IT + 2 < IPW) {
    asm volatile("s_waitcnt lgkmcnt(0)" ::: "memory");
    dma_e(c, IT + 2);
    __builtin_amdgcn_sched_barrier(0);
  }
  LOADW(0, 3, 0);
#pragma unroll
  for (int s = 0; s < 8; ++s) a5 = MFMA(EB(s), W[1][s], a5);
  Vp[0][0] = pack_step(a4, 0);
  Vp[0][1] = pack_step(a4, 1);
  float sv[14];
#pragma unroll
  for (int i = 0; i < 7; ++i) {
    unsigned u = pack2(S[2 * i], S[2 * i + 1]);
    sv[2 * i] = __uint_as_float(u << 16);
    sv[2 * i + 1] = __uint_as_float(u & 0xffff0000u);
  }
  float m = sv[0];
#pragma unroll
  for (int i = 1; i < 12; ++i) m = fmaxf(m, sv[i]);
  float m2 = fmaxf(m, fmaxf(sv[12], sv[13]));
  m = h == 0 ? m2 : m;
  {
    auto sw = __builtin_amdgcn_permlane32_swap(__float_as_uint(m), __float_as_uint(m), false, false);
    m = fmaxf(__uint_as_float(sw[0]), __uint_as_float(sw[1]));
  }
  PATTERN(6);
  LOADW(1, 3, 1);
  o0 = f32x16{};
#pragma unroll
  for (int s = 0; s < 8; ++s) o0 = MFMA(W[0][s], EB(s), o0);
  Vp[1][0] = pack_step(a5, 0);
  Vp[1][1] = pack_step(a5, 1);
  f32x16 P;
  const float mc = m * cexp;
  float sum = 0.f;
#pragma unroll
  for (int i = 0; i < 12; ++i) {
    P[i] = __builtin_amdgcn_exp2f(__builtin_fmaf(sv[i], cexp, -mc));
    sum += P[i];
  }
  {
    float p12 = __builtin_amdgcn_exp2f(__builtin_fmaf(sv[12], cexp, -mc));
    float p13 = __builtin_amdgcn_exp2f(__builtin_fmaf(sv[13], cexp, -mc));
    P[12] = h == 0 ? p12 : 0.f;
    P[13] = h == 0 ? p13 : 0.f;
    sum += P[12] + P[13];
  }
  {
    auto sw = __builtin_amdgcn_permlane32_swap(__float_as_uint(sum), __float_as_uint(sum), false, false);
    sum = __uint_as_float(sw[0]) + __uint_as_float(sw[1]);
  }
  PATTERN(7);
  LOADW(0, 0, 0);
  o1 = f32x16{};
#pragma unroll
  for (int s = 0; s < 8; ++s) o1 = MFMA(W[1][s], EB(s), o1);
  const float inv = __builtin_amdgcn_rcpf(sum);
#pragma unroll
  for (int i = 0; i < 14; ++i) P[i] *= inv;
  P[14] = 0.f;
  P[15] = 0.f;
  Pp[0] = pack_step(P, 0);
  Pp[1] = pack_step(P, 1);
  PATTERN(3);
  o0 = MFMA(Vp[0][0], Pp[0], o0);
  o0 = MFMA(Vp[0][1], Pp[1], o0);
  o1 = MFMA(Vp[1][0], Pp[0], o1);
  o1 = MFMA(Vp[1][1], Pp[1], o1);
  __builtin_amdgcn_sched_barrier(0);
}

__global__ __launch_bounds__(WAVES * 64, 2) void autoint_kernel(
    const __bf16* __restrict__ e, const __bf16* __restrict__ wq, const __bf16* __restrict__ wk,
    const __bf16* __restrict__ wv, const __bf16* __restrict__ wres, __bf16* __restrict__ out, unsigned long long* prof) {
  extern __shared__ __attribute__((aligned(16))) char smem[];
  const int tid = threadIdx.x;
  const int lane = tid & 63;
  const int wave = __builtin_amdgcn_readfirstlane(tid >> 6);
  const int r = lane & 31, h = lane >> 5;
  const unsigned lds0 = __builtin_amdgcn_readfirstlane((unsigned)(size_t)(lds_void_ptr)smem);
  const int bid = blockIdx.x;
  const int hd = (bid >> 3) & 1;
  const int grp = (bid & 7) | ((bid >> 4) << 3);
  const int b0 = grp * BPG + wave;

  unsigned long long T0c, T0r;
  asm volatile("s_memtime %0\n\ts_memrealtime %1\n\ts_waitcnt lgkmcnt(0)" : "=s"(T0c), "=s"(T0r) :: "memory");
  Ctx c;
  c.h = h;
  c.lane = lane;
  c.prof = prof + (size_t)(blockIdx.x * WAVES + wave) * 32;
  STAMP(0);
  c.ebuf = lds0 + 65536 + wave * 12288;
  c.ebase = (const char*)e + (size_t)b0 * EBYTES;
  c.obase = b0 * EBYTES;
  {
    const int lq = lane >> 4, c0 = (lane & 15) ^ lq;
#pragma unroll
    for (int j = 0; j < 4; ++j) c.evoff[j] = (unsigned)((lq * 256 + (c0 << 4)) ^ (j << 6));
  }
  const bool leader = wave < 4;
  c.leader = leader;
  if (leader) {
    dma_e(c, 0);
  } else {
    unsigned o64[4], o128[4];
#pragma unroll
    for (int i = 0; i < 4; ++i) {
      const int pos = ((wave - 4) * 4 + i) * 64 + lane;
      const int blk2 = pos >> 4;
      const int dq = blk2 >> 1, t = blk2 & 1;
      const int q = (pos >> 2) & 3, pb = (pos >> 1) & 1, blk = pos & 1;
      const int d = 4 * dq + q, c8 = 4 * pb + 2 * t + blk;
      o64[i] = (unsigned)(d * 128 + c8 * 16);
      o128[i] = (unsigned)(d * 256 + c8 * 16);
    }
    const char* sq = (const char*)wq + hd * 16384;
    const char* sk = (const char*)wk + hd * 16384;
    const char* sv = (const char*)wv + hd * 16384;
    const char* sr = (const char*)wres + hd * 128;
    const unsigned dst = lds0 + (wave - 4) * 4096;
#pragma unroll
    for (int i = 0; i < 4; ++i) glds16s<0>(o64[i], sq, dst + i * 1024);
#pragma unroll
    for (int i = 0; i < 4; ++i) glds16s<0>(o64[i], sk, dst + 16384 + i * 1024);
#pragma unroll
    for (int i = 0; i < 4; ++i) glds16s<0>(o64[i], sv, dst + 32768 + i * 1024);
#pragma unroll
    for (int i = 0; i < 4; ++i) glds16s<0>(o128[i], sr, dst + 49152 + i * 1024);
    dma_e(c, 0);
  }
  __builtin_amdgcn_sched_barrier(0);

  {
    const int q4 = (lane & 15) >> 2, p4 = lane & 3, blk = (lane >> 4) & 1;
    c.lbase = lds0 + h * 1024 + 64 * q4 + 32 * (p4 & 1) + 16 * blk + 8 * (p4 >> 1);
    asm volatile("" : "+v"(c.lbase));
    c.eread = c.ebuf + r * 256 + ((h ^ (r & 15)) << 4);
    const unsigned obuf = c.ebuf + 8192;
    c.owr = obuf + r * 128 + (((4 * h) ^ (r & 7)) << 4);
    c.ord = obuf + lane * 16;
    const int orow = lane >> 3, oc = (lane & 7) ^ (orow & 7);
    c.gvoff = orow * 256 + hd * 128 + oc * 16;
    c.gvoff3 = lane < 16 ? c.gvoff + 2048 : 0x40000000;
    c.rsrc = __builtin_amdgcn_make_buffer_rsrc((void*)out, 0, NB_TOTAL * EBYTES, 0x00020000);
  }
  {
    u32x4 z = {};
    *(lds_u32x4_ptr)(c.ebuf + NF * 256 + lane * 16) = z;
    if (lane < 32) *(lds_u32x4_ptr)(c.ebuf + NF * 256 + 1024 + lane * 16) = z;
  }

  u32x4 EA[8], EB2[8];
  STAMP(1);
  if (leader) {
    __builtin_amdgcn_s_barrier();
  } else {
    WAITW(19);
    WAITW(15);
    WAITW(11);
    WAITW(7);
  }
  WAITV(0);
  __builtin_amdgcn_sched_barrier(0);
  STAMP(2);
  read_e(c, EA);
  bf16x8 W[2][8];
  LOADW(0, 0, 0);
  asm volatile("s_waitcnt lgkmcnt(0)" ::: "memory");
  __builtin_amdgcn_sched_barrier(0);
  dma_e(c, 1);
  __builtin_amdgcn_sched_barrier(0);
  STAMP(3);

  f32x16 o0 = {}, o1 = {};
  item_body<0>(c, EA, EB2, W, o0, o1);
  STAMP(4);
  item_body<1>(c, EB2, EA, W, o0, o1);
  STAMP(5);
  item_body<2>(c, EA, EB2, W, o0, o1);
  STAMP(6);
  item_body<3>(c, EB2, EA, W, o0, o1);
  STAMP(7);
  {
    *(lds_u32x4_ptr)(c.owr ^ 0) = relu_pack(o0, 0);
    *(lds_u32x4_ptr)(c.owr ^ 16) = relu_pack(o0, 1);
    *(lds_u32x4_ptr)(c.owr ^ 32) = relu_pack(o1, 0);
    *(lds_u32x4_ptr)(c.owr ^ 48) = relu_pack(o1, 1);
    u32x4 ob[4];
#pragma unroll
    for (int j = 0; j < 4; ++j) ob[j] = *(lds_u32x4_ptr)(c.ord + j * 1024);
    const int so = c.obase + (IPW - 1) * (WAVES * EBYTES);
    __builtin_amdgcn_raw_buffer_store_b128(ob[0], c.rsrc, c.gvoff, so, ST_AUX);
    __builtin_amdgcn_raw_buffer_store_b128(ob[1], c.rsrc, c.gvoff + 2048, so, ST_AUX);
    __builtin_amdgcn_raw_buffer_store_b128(ob[2], c.rsrc, c.gvoff, so + 4096, ST_AUX);
    __builtin_amdgcn_raw_buffer_store_b128(ob[3], c.rsrc, c.gvoff3, so + 4096, ST_AUX);
  }
  STAMP(8);
  asm volatile("s_waitcnt vmcnt(0)" ::: "memory");
  unsigned long long T1c, T1r;
  asm volatile("s_memtime %0\n\ts_memrealtime %1\n\ts_waitcnt lgkmcnt(0)" : "=s"(T1c), "=s"(T1r) :: "memory");
  if ((blockIdx.x == 0 || blockIdx.x == 133) && tid == 0) printf("blk %d cyc %llu rt %llu\n", (int)blockIdx.x, T1c - T0c, T1r - T0r);
}

extern "C" void kernel_launch(void* const* d_in, const int* in_sizes, int n_in, void* d_out, int out_size,
                              void* d_ws, size_t ws_size, hipStream_t stream) {
  const __bf16* e = (const __bf16*)d_in[0];
  const __bf16* wq = (const __bf16*)d_in[1];
  const __bf16* wk = (const __bf16*)d_in[2];
  const __bf16* wv = (const __bf16*)d_in[3];
  const __bf16* wres = (const __bf16*)d_in[4];
  __bf16* out = (__bf16*)d_out;
  (void)hipFuncSetAttribute((const void*)autoint_kernel, hipFuncAttributeMaxDynamicSharedMemorySize, LDS_BYTES);
  autoint_kernel<<<2 * NB_TOTAL / BPG, WAVES * 64, LDS_BYTES, stream>>>(e, wq, wk, wv, wres, out, (unsigned long long*)d_ws);
}
```

```cpp
#define DIAG 1
#include <hip/hip_runtime.h>
#include <hip/hip_bf16.h>

typedef __attribute__((ext_vector_type(8))) __bf16 bf16x8;
typedef __attribute__((ext_vector_type(2))) __bf16 bf16x2;
typedef __attribute__((ext_vector_type(4))) short s16x4;
typedef __attribute__((ext_vector_type(8))) short s16x8;
typedef __attribute__((ext_vector_type(16))) float f32x16;
typedef __attribute__((ext_vector_type(4))) unsigned u32x4;
typedef __attribute__((address_space(3))) s16x4* lds_s16x4_ptr;
typedef __attribute__((address_space(3))) u32x4* lds_u32x4_ptr;
typedef __attribute__((address_space(3))) void* lds_void_ptr;

#define DI __device__ __forceinline__
#define MFMA(a, b, c) __builtin_amdgcn_mfma_f32_32x32x16_bf16((a), (b), (c), 0, 0, 0)

#define ST_AUX 2

constexpr int NB_TOTAL = 4096;
constexpr int NF = 26;
constexpr int ND = 128;
constexpr int WAVES = 8;
constexpr int IPW = 4;
constexpr int BPG = WAVES * IPW;
constexpr int EBYTES = NF * ND * 2;
constexpr int LDS_BYTES = 65536 + WAVES * 12288;

DI unsigned pack2(float lo, float hi) {
  bf16x2 v;
  v[0] = (__bf16)lo;
  v[1] = (__bf16)hi;
  return __builtin_bit_cast(unsigned, v);
}

DI u32x4 pack_u(const f32x16& x, int s) {
  u32x4 p;
  p[0] = pack2(x[8 * s + 0], x[8 * s + 1]);
  p[1] = pack2(x[8 * s + 2], x[8 * s + 3]);
  p[2] = pack2(x[8 * s + 4], x[8 * s + 5]);
  p[3] = pack2(x[8 * s + 6], x[8 * s + 7]);
  return p;
}
DI bf16x8 pack_step(const f32x16& x, int s) { return __builtin_bit_cast(bf16x8, pack_u(x, s)); }

DI u32x4 relu_pack(const f32x16& x, int s) {
  s16x8 z = {};
  return __builtin_bit_cast(u32x4, __builtin_elementwise_max(__builtin_bit_cast(s16x8, pack_u(x, s)), z));
}

DI bf16x8 wfrag(unsigned lbase, int panel, int t, int s) {
  const unsigned p = lbase + panel * 16384 + s * 2048 + t * 256;
  s16x4 lo = __builtin_amdgcn_ds_read_tr16_b64_v4i16((lds_s16x4_ptr)(p));
  s16x4 hi = __builtin_amdgcn_ds_read_tr16_b64_v4i16((lds_s16x4_ptr)(p + 512));
  return __builtin_bit_cast(bf16x8, __builtin_shufflevector(lo, hi, 0, 1, 2, 3, 4, 5, 6, 7));
}

template <int IMM>
DI void glds16s(unsigned voff, const void* sbase, unsigned lds_dst) {
  unsigned keep;
  asm volatile(
      "s_mov_b32 %0, m0\n\ts_mov_b32 m0, %3\n\ts_nop 0\n\tglobal_load_lds_dwordx4 %1, %2 offset:%c4\n\ts_mov_b32 m0, %0"
      : "=&s"(keep)
      : "v"(voff), "s"(sbase), "s"(lds_dst), "i"(IMM)
      : "memory");
}

#define SGB(mask, n) __builtin_amdgcn_sched_group_barrier(mask, n, 0)
#define PATTERN(nv)                               \
  _Pragma("unroll") for (int s = 0; s < 8; ++s) { \
    SGB(0x8, 1);                                  \
    SGB(0x100, 2);                                \
    SGB(0x2, nv);                                 \
  }                                               \
  __builtin_amdgcn_sched_barrier(0);
#define PATTERN3(nv)                              \
  _Pragma("unroll") for (int s = 0; s < 8; ++s) { \
    SGB(0x8, 1);                                  \
    SGB(0x100, 3);                                \
    SGB(0x2, nv);                                 \
  }                                               \
  __builtin_amdgcn_sched_barrier(0);

#define WAITV(n) asm volatile("s_waitcnt vmcnt(" #n ")" ::: "memory");
#define WAITW(n)                  \
  WAITV(n)                        \
  __builtin_amdgcn_s_barrier();   \
  __builtin_amdgcn_sched_barrier(0);

#define STAMP(k)
#define LBAR()                                      \
  if (c.leader) __builtin_amdgcn_s_barrier();       \
  __builtin_amdgcn_sched_barrier(0);
struct Ctx {
  bool leader;
  unsigned long long* prof;
  unsigned lbase;
  unsigned eread;
  unsigned ebuf;
  unsigned owr;
  unsigned ord;
  unsigned evoff[4];
  int gvoff, gvoff3, gvoff0;
  int h, lane;
  const char* ebase;
  int obase;
  __amdgpu_buffer_rsrc_t rsrc;
};

#define LOADW(buf, panel, t) \
  _Pragma("unroll") for (int s = 0; s < 8; ++s) W[buf][s] = wfrag(c.lbase, (panel), (t), s);
#define EB(s) __builtin_bit_cast(bf16x8, E[s])

DI void dma_e(const Ctx& c, int item) {
  const char* p = c.ebase + (size_t)item * (WAVES * EBYTES);
  glds16s<0>(c.evoff[0], p, c.ebuf);
  glds16s<1024>(c.evoff[1], p, c.ebuf);
  glds16s<2048>(c.evoff[2], p, c.ebuf);
  glds16s<3072>(c.evoff[3], p, c.ebuf);
  const char* p2 = p + 4096;
  glds16s<0>(c.evoff[0], p2, c.ebuf + 4096);
  glds16s<1024>(c.evoff[1], p2, c.ebuf + 4096);
  if (c.lane < 32) glds16s<2048>(c.evoff[2], p2, c.ebuf + 4096);
}

DI void read_e(const Ctx& c, u32x4 (&E)[8]) {
#pragma unroll
  for (int s = 0; s < 8; ++s) E[s] = *(lds_u32x4_ptr)(c.eread ^ (s << 5));
}

template <int IT>
DI void item_body(const Ctx& c, const u32x4 (&E)[8], u32x4 (&E2)[8], bf16x8 (&W)[2][8], f32x16& o0, f32x16& o1) {
  constexpr bool FIRST = IT == 0;
  const float cexp = 0.125f * 1.4426950408889634f;
  const int h = c.h;
  bf16x8 Qp[2][2], Kp[2][2], Vp[2][2], Pp[2];
  f32x16 a0 = {}, a1 = {}, a2 = {}, a3 = {}, a4 = {}, a5 = {}, S = {};
  u32x4 ob[4];
  LOADW(1, 0, 1);
#pragma unroll
  for (int s = 0; s < 8; ++s) a0 = MFMA(W[0][s], EB(s), a0);
  if (!FIRST) {
    *(lds_u32x4_ptr)(c.owr ^ 0) = relu_pack(o0, 0);
    *(lds_u32x4_ptr)(c.owr ^ 16) = relu_pack(o0, 1);
    *(lds_u32x4_ptr)(c.owr ^ 32) = relu_pack(o1, 0);
    *(lds_u32x4_ptr)(c.owr ^ 48) = relu_pack(o1, 1);
  }
  PATTERN(5);
  if (FIRST) { LBAR(); }
  LOADW(0, 1, 0);
  if (!FIRST) {
#pragma unroll
    for (int j = 0; j < 4; ++j) ob[j] = *(lds_u32x4_ptr)(c.ord + j * 1024);
  }
#pragma unroll
  for (int s = 0; s < 8; ++s) a1 = MFMA(W[1][s], EB(s), a1);
  Qp[0][0] = pack_step(a0, 0);
  Qp[0][1] = pack_step(a0, 1);
  PATTERN3(2);
  LOADW(1, 1, 1);
#pragma unroll
  for (int s = 0; s < 8; ++s) a2 = MFMA(W[0][s], EB(s), a2);
  Qp[1][0] = pack_step(a1, 0);
  Qp[1][1] = pack_step(a1, 1);
  if (!FIRST) {
    const int so = c.obase + (IT - 1) * (WAVES * EBYTES);
    __builtin_amdgcn_raw_buffer_store_b128(ob[0], c.rsrc, IT == 1 ? c.gvoff0 : c.gvoff, so, ST_AUX);
    __builtin_amdgcn_raw_buffer_store_b128(ob[1], c.rsrc, c.gvoff + 2048, so, ST_AUX);
    __builtin_amdgcn_raw_buffer_store_b128(ob[2], c.rsrc, c.gvoff, so + 4096, ST_AUX);
    __builtin_amdgcn_raw_buffer_store_b128(ob[3], c.rsrc, c.gvoff3, so + 4096, ST_AUX);
  }
  PATTERN(2);
  if (FIRST) { LBAR(); }
  LOADW(0, 2, 0);
#pragma unroll
  for (int s = 0; s < 8; ++s) a3 = MFMA(W[1][s], EB(s), a3);
  Kp[0][0] = pack_step(a2, 0);
  Kp[0][1] = pack_step(a2, 1);
  PATTERN(2);
  if (FIRST) { LBAR(); }
  if (IT + 1 < IPW) {
    if (FIRST) { WAITV(0); } else { WAITV(4); }
    __builtin_amdgcn_sched_barrier(0);
  }
  LOADW(1, 2, 1);
  if (IT + 1 < IPW) read_e(c, E2);
#pragma unroll
  for (int s = 0; s < 8; ++s) a4 = MFMA(EB(s), W[0][s], a4);
  Kp[1][0] = pack_step(a3, 0);
  Kp[1][1] = pack_step(a3, 1);
  PATTERN3(2);
  S = MFMA(Kp[0][0], Qp[0][0], S);
  S = MFMA(Kp[0][1], Qp[0][1], S);
  S = MFMA(Kp[1][0], Qp[1][0], S);
  S = MFMA(Kp[1][1], Qp[1][1], S);
  __builtin_amdgcn_sched_barrier(0);
  if (IT + 2 < IPW) {
    asm volatile("s_waitcnt lgkmcnt(0)" ::: "memory");
    dma_e(c, IT + 2);
    __builtin_amdgcn_sched_barrier(0);
  }
  LOADW(0, 3, 0);
#pragma unroll
  for (int s = 0; s < 8; ++s) a5 = MFMA(EB(s), W[1][s], a5);
  Vp[0][0] = pack_step(a4, 0);
  Vp[0][1] = pack_step(a4, 1);
  float sv[14];
#pragma unroll
  for (int i = 0; i < 7; ++i) {
    unsigned u = pack2(S[2 * i], S[2 * i + 1]);
    sv[2 * i] = __uint_as_float(u << 16);
    sv[2 * i + 1] = __uint_as_float(u & 0xffff0000u);
  }
  float m = sv[0];
#pragma unroll
  for (int i = 1; i < 12; ++i) m = fmaxf(m, sv[i]);
  float m2 = fmaxf(m, fmaxf(sv[12], sv[13]));
  m = h == 0 ? m2 : m;
  {
    auto sw = __builtin_amdgcn_permlane32_swap(__float_as_uint(m), __float_as_uint(m), false, false);
    m = fmaxf(__uint_as_float(sw[0]), __uint_as_float(sw[1]));
  }
  PATTERN(6);
  LOADW(1, 3, 1);
  o0 = f32x16{};
#pragma unroll
  for (int s = 0; s < 8; ++s) o0 = MFMA(W[0][s], EB(s), o0);
  Vp[1][0] = pack_step(a5, 0);
  Vp[1][1] = pack_step(a5, 1);
  f32x16 P;
  const float mc = m * cexp;
  float sum = 0.f;
#pragma unroll
  for (int i = 0; i < 12; ++i) {
    P[i] = __builtin_amdgcn_exp2f(__builtin_fmaf(sv[i], cexp, -mc));
    sum += P[i];
  }
  {
    float p12 = __builtin_amdgcn_exp2f(__builtin_fmaf(sv[12], cexp, -mc));
    float p13 = __builtin_amdgcn_exp2f(__builtin_fmaf(sv[13], cexp, -mc));
    P[12] = h == 0 ? p12 : 0.f;
    P[13] = h == 0 ? p13 : 0.f;
    sum += P[12] + P[13];
  }
  {
    auto sw = __builtin_amdgcn_permlane32_swap(__float_as_uint(sum), __float_as_uint(sum), false, false);
    sum = __uint_as_float(sw[0]) + __uint_as_float(sw[1]);
  }
  PATTERN(7);
  LOADW(0, 0, 0);
  o1 = f32x16{};
#pragma unroll
  for (int s = 0; s < 8; ++s) o1 = MFMA(W[1][s], EB(s), o1);
  const float inv = __builtin_amdgcn_rcpf(sum);
#pragma unroll
  for (int i = 0; i < 14; ++i) P[i] *= inv;
  P[14] = 0.f;
  P[15] = 0.f;
  Pp[0] = pack_step(P, 0);
  Pp[1] = pack_step(P, 1);
  PATTERN(3);
  o0 = MFMA(Vp[0][0], Pp[0], o0);
  o0 = MFMA(Vp[0][1], Pp[1], o0);
  o1 = MFMA(Vp[1][0], Pp[0], o1);
  o1 = MFMA(Vp[1][1], Pp[1], o1);
  __builtin_amdgcn_sched_barrier(0);
}

__global__ __launch_bounds__(WAVES * 64, 2) void autoint_kernel(
    const __bf16* __restrict__ e, const __bf16* __restrict__ wq, const __bf16* __restrict__ wk,
    const __bf16* __restrict__ wv, const __bf16* __restrict__ wres, __bf16* __restrict__ out, unsigned long long* prof) {
  extern __shared__ __attribute__((aligned(16))) char smem[];
  const int tid = threadIdx.x;
  const int lane = tid & 63;
  const int wave = __builtin_amdgcn_readfirstlane(tid >> 6);
  const int r = lane & 31, h = lane >> 5;
  const unsigned lds0 = __builtin_amdgcn_readfirstlane((unsigned)(size_t)(lds_void_ptr)smem);
  const int bid = blockIdx.x;
  const int hd = (bid >> 3) & 1;
  const int grp = (bid & 7) | ((bid >> 4) << 3);
  const int b0 = grp * BPG + wave;

  unsigned long long T0c, T0r;
  asm volatile("s_memtime %0\n\ts_memrealtime %1\n\ts_waitcnt lgkmcnt(0)" : "=s"(T0c), "=s"(T0r) :: "memory");
  Ctx c;
  c.h = h;
  c.lane = lane;
  c.prof = prof + (size_t)(blockIdx.x * WAVES + wave) * 32;
  STAMP(0);
  c.ebuf = lds0 + 65536 + wave * 12288;
  c.ebase = (const char*)e + (size_t)b0 * EBYTES;
  c.obase = b0 * EBYTES;
  {
    const int lq = lane >> 4, c0 = (lane & 15) ^ lq;
#pragma unroll
    for (int j = 0; j < 4; ++j) c.evoff[j] = (unsigned)((lq * 256 + (c0 << 4)) ^ (j << 6));
  }
  const bool leader = wave < 4;
  c.leader = leader;
  if (leader) {
    dma_e(c, 0);
  } else {
    unsigned o64[4], o128[4];
#pragma unroll
    for (int i = 0; i < 4; ++i) {
      const int pos = ((wave - 4) * 4 + i) * 64 + lane;
      const int blk2 = pos >> 4;
      const int dq = blk2 >> 1, t = blk2 & 1;
      const int q = (pos >> 2) & 3, pb = (pos >> 1) & 1, blk = pos & 1;
      const int d = 4 * dq + q, c8 = 4 * pb + 2 * t + blk;
      o64[i] = (unsigned)(d * 128 + c8 * 16);
      o128[i] = (unsigned)(d * 256 + c8 * 16);
    }
    const char* sq = (const char*)wq + hd * 16384;
    const char* sk = (const char*)wk + hd * 16384;
    const char* sv = (const char*)wv + hd * 16384;
    const char* sr = (const char*)wres + hd * 128;
    const unsigned dst = lds0 + (wave - 4) * 4096;
#pragma unroll
    for (int i = 0; i < 4; ++i) glds16s<0>(o64[i], sq, dst + i * 1024);
#pragma unroll
    for (int i = 0; i < 4; ++i) glds16s<0>(o64[i], sk, dst + 16384 + i * 1024);
#pragma unroll
    for (int i = 0; i < 4; ++i) glds16s<0>(o64[i], sv, dst + 32768 + i * 1024);
#pragma unroll
    for (int i = 0; i < 4; ++i) glds16s<0>(o128[i], sr, dst + 49152 + i * 1024);
    dma_e(c, 0);
  }
  __builtin_amdgcn_sched_barrier(0);

  {
    const int q4 = (lane & 15) >> 2, p4 = lane & 3, blk = (lane >> 4) & 1;
    c.lbase = lds0 + h * 1024 + 64 * q4 + 32 * (p4 & 1) + 16 * blk + 8 * (p4 >> 1);
    asm volatile("" : "+v"(c.lbase));
    c.eread = c.ebuf + r * 256 + ((h ^ (r & 15)) << 4);
    const unsigned obuf = c.ebuf + 8192;
    c.owr = obuf + r * 128 + (((4 * h) ^ (r & 7)) << 4);
    c.ord = obuf + lane * 16;
    const int orow = lane >> 3, oc = (lane & 7) ^ (orow & 7);
    c.gvoff = orow * 256 + hd * 128 + oc * 16;
    c.gvoff3 = lane < 16 ? c.gvoff + 2048 : 0x40000000;
    c.gvoff0 = c.gvoff;
    c.rsrc = __builtin_amdgcn_make_buffer_rsrc((void*)out, 0, NB_TOTAL * EBYTES, 0x00020000);
  }
  {
    u32x4 z = {};
    *(lds_u32x4_ptr)(c.ebuf + NF * 256 + lane * 16) = z;
    if (lane < 32) *(lds_u32x4_ptr)(c.ebuf + NF * 256 + 1024 + lane * 16) = z;
  }

  u32x4 EA[8], EB2[8];
  STAMP(1);
  if (leader) {
    __builtin_amdgcn_s_barrier();
  } else {
    WAITW(19);
    WAITW(15);
    WAITW(11);
    WAITW(7);
  }
  WAITV(0);
  __builtin_amdgcn_sched_barrier(0);
  STAMP(2);
  read_e(c, EA);
  bf16x8 W[2][8];
  LOADW(0, 0, 0);
  asm volatile("s_waitcnt lgkmcnt(0)" ::: "memory");
  __builtin_amdgcn_sched_barrier(0);
  dma_e(c, 1);
  __builtin_amdgcn_sched_barrier(0);
  STAMP(3);

  f32x16 o0 = {}, o1 = {};
  item_body<0>(c, EA, EB2, W, o0, o1);
  STAMP(4);
  item_body<1>(c, EB2, EA, W, o0, o1);
  STAMP(5);
  item_body<2>(c, EA, EB2, W, o0, o1);
  STAMP(6);
  item_body<3>(c, EB2, EA, W, o0, o1);
  STAMP(7);
  {
    *(lds_u32x4_ptr)(c.owr ^ 0) = relu_pack(o0, 0);
    *(lds_u32x4_ptr)(c.owr ^ 16) = relu_pack(o0, 1);
    *(lds_u32x4_ptr)(c.owr ^ 32) = relu_pack(o1, 0);
    *(lds_u32x4_ptr)(c.owr ^ 48) = relu_pack(o1, 1);
    u32x4 ob[4];
#pragma unroll
    for (int j = 0; j < 4; ++j) ob[j] = *(lds_u32x4_ptr)(c.ord + j * 1024);
    const int so = c.obase + (IPW - 1) * (WAVES * EBYTES);
    __builtin_amdgcn_raw_buffer_store_b128(ob[0], c.rsrc, c.gvoff, so, ST_AUX);
    __builtin_amdgcn_raw_buffer_store_b128(ob[1], c.rsrc, c.gvoff + 2048, so, ST_AUX);
    __builtin_amdgcn_raw_buffer_store_b128(ob[2], c.rsrc, c.gvoff, so + 4096, ST_AUX);
    __builtin_amdgcn_raw_buffer_store_b128(ob[3], c.rsrc, c.gvoff3, so + 4096, ST_AUX);
  }
  STAMP(8);
  asm volatile("s_waitcnt vmcnt(0)" ::: "memory");
  unsigned long long T1c, T1r;
  asm volatile("s_memtime %0\n\ts_memrealtime %1\n\ts_waitcnt lgkmcnt(0)" : "=s"(T1c), "=s"(T1r) :: "memory");
  if (lane == 0) {
    atomicMax(&prof[0], ~T0r);
    atomicMax(&prof[1], T1r);
    atomicMax(&prof[2], T0r);
    atomicMax(&prof[3], T1c - T0c);
    atomicMax(&prof[4], T1r - T0r);
    atomicMax(&prof[5], ~T1r);
    __threadfence();
    unsigned long long cnt = atomicAdd(&prof[8], 1ull);
    if (cnt == 256 * 8 - 1) {
      __threadfence();
      unsigned long long a0 = ~atomicMax(&prof[0], 0ull), a1 = atomicMax(&prof[1], 0ull), a2 = atomicMax(&prof[2], 0ull), a3 = atomicMax(&prof[3], 0ull), a4 = atomicMax(&prof[4], 0ull), a5 = ~atomicMax(&prof[5], 0ull);
      float v = DIAG == 1 ? (float)(a1 - a0) : DIAG == 2 ? (float)(a2 - a0) : DIAG == 3 ? (float)a3 : DIAG == 4 ? (float)a4 : (float)(a5 - a0);
      u32x4 w; w[0] = pack2(v, v); w[1] = w[0]; w[2] = w[0]; w[3] = w[0];
      if (a1 - a0 < 10000) *(u32x4*)out = w;
    }
  }
}

extern "C" void kernel_launch(void* const* d_in, const int* in_sizes, int n_in, void* d_out, int out_size,
                              void* d_ws, size_t ws_size, hipStream_t stream) {
  const __bf16* e = (const __bf16*)d_in[0];
  const __bf16* wq = (const __bf16*)d_in[1];
  const __bf16* wk = (const __bf16*)d_in[2];
  const __bf16* wv = (const __bf16*)d_in[3];
  const __bf16* wres = (const __bf16*)d_in[4];
  __bf16* out = (__bf16*)d_out;
  (void)hipFuncSetAttribute((const void*)autoint_kernel, hipFuncAttributeMaxDynamicSharedMemorySize, LDS_BYTES);
  (void)hipMemsetAsync(d_ws, 0, 256, stream);
  autoint_kernel<<<2 * NB_TOTAL / BPG, WAVES * 64, LDS_BYTES, stream>>>(e, wq, wk, wv, wres, out, (unsigned long long*)d_ws);
}
```

```cpp
#include <hip/hip_runtime.h>
#include <hip/hip_bf16.h>

typedef __attribute__((ext_vector_type(8))) __bf16 bf16x8;
typedef __attribute__((ext_vector_type(2))) __bf16 bf16x2;
typedef __attribute__((ext_vector_type(4))) short s16x4;
typedef __attribute__((ext_vector_type(8))) short s16x8;
typedef __attribute__((ext_vector_type(16))) float f32x16;
typedef __attribute__((ext_vector_type(4))) unsigned u32x4;
typedef __attribute__((address_space(3))) s16x4* lds_s16x4_ptr;
typedef __attribute__((address_space(3))) u32x4* lds_u32x4_ptr;
typedef __attribute__((address_space(3))) void* lds_void_ptr;

#define DI __device__ __forceinline__
#define MFMA(a, b, c) __builtin_amdgcn_mfma_f32_32x32x16_bf16((a), (b), (c), 0, 0, 0)

#define ST_AUX 17

constexpr int NB_TOTAL = 4096;
constexpr int NF = 26;
constexpr int ND = 128;
constexpr int WAVES = 8;
constexpr int IPW = 4;
constexpr int BPG = WAVES * IPW;
constexpr int EBYTES = NF * ND * 2;
constexpr int LDS_BYTES = 65536 + WAVES * 12288;

DI unsigned pack2(float lo, float hi) {
  bf16x2 v;
  v[0] = (__bf16)lo;
  v[1] = (__bf16)hi;
  return __builtin_bit_cast(unsigned, v);
}

DI u32x4 pack_u(const f32x16& x, int s) {
  u32x4 p;
  p[0] = pack2(x[8 * s + 0], x[8 * s + 1]);
  p[1] = pack2(x[8 * s + 2], x[8 * s + 3]);
  p[2] = pack2(x[8 * s + 4], x[8 * s + 5]);
  p[3] = pack2(x[8 * s + 6], x[8 * s + 7]);
  return p;
}
DI bf16x8 pack_step(const f32x16& x, int s) { return __builtin_bit_cast(bf16x8, pack_u(x, s)); }

DI u32x4 relu_pack(const f32x16& x, int s) {
  s16x8 z = {};
  return __builtin_bit_cast(u32x4, __builtin_elementwise_max(__builtin_bit_cast(s16x8, pack_u(x, s)), z));
}

DI bf16x8 wfrag(unsigned lbase, int panel, int t, int s) {
  const unsigned p = lbase + panel * 16384 + s * 2048 + t * 256;
  s16x4 lo = __builtin_amdgcn_ds_read_tr16_b64_v4i16((lds_s16x4_ptr)(p));
  s16x4 hi = __builtin_amdgcn_ds_read_tr16_b64_v4i16((lds_s16x4_ptr)(p + 512));
  return __builtin_bit_cast(bf16x8, __builtin_shufflevector(lo, hi, 0, 1, 2, 3, 4, 5, 6, 7));
}

template <int IMM>
DI void glds16s(unsigned voff, const void* sbase, unsigned lds_dst) {
  unsigned keep;
  asm volatile(
      "s_mov_b32 %0, m0\n\ts_mov_b32 m0, %3\n\ts_nop 0\n\tglobal_load_lds_dwordx4 %1, %2 offset:%c4\n\ts_mov_b32 m0, %0"
      : "=&s"(keep)
      : "v"(voff), "s"(sbase), "s"(lds_dst), "i"(IMM)
      : "memory");
}

#define SGB(mask, n) __builtin_amdgcn_sched_group_barrier(mask, n, 0)
#define PATTERN(nv)                               \
  _Pragma("unroll") for (int s = 0; s < 8; ++s) { \
    SGB(0x8, 1);                                  \
    SGB(0x100, 2);                                \
    SGB(0x2, nv);                                 \
  }                                               \
  __builtin_amdgcn_sched_barrier(0);
#define PATTERN3(nv)                              \
  _Pragma("unroll") for (int s = 0; s < 8; ++s) { \
    SGB(0x8, 1);                                  \
    SGB(0x100, 3);                                \
    SGB(0x2, nv);                                 \
  }                                               \
  __builtin_amdgcn_sched_barrier(0);

#define WAITV(n) asm volatile("s_waitcnt vmcnt(" #n ")" ::: "memory");
#define WAITW(n)                  \
  WAITV(n)                        \
  __builtin_amdgcn_s_barrier();   \
  __builtin_amdgcn_sched_barrier(0);

#define STAMP(k)
#define LBAR()                                      \
  if (c.leader) __builtin_amdgcn_s_barrier();       \
  __builtin_amdgcn_sched_barrier(0);
struct Ctx {
  bool leader;
  unsigned long long* prof;
  unsigned lbase;
  unsigned eread;
  unsigned ebuf;
  unsigned owr;
  unsigned ord;
  unsigned evoff[4];
  int gvoff, gvoff3;
  int h, lane;
  const char* ebase;
  int obase;
  __amdgpu_buffer_rsrc_t rsrc;
};

#define LOADW(buf, panel, t) \
  _Pragma("unroll") for (int s = 0; s < 8; ++s) W[buf][s] = wfrag(c.lbase, (panel), (t), s);
#define EB(s) __builtin_bit_cast(bf16x8, E[s])

DI void dma_e(const Ctx& c, int item) {
  const char* p = c.ebase + (size_t)item * (WAVES * EBYTES);
  glds16s<0>(c.evoff[0], p, c.ebuf);
  glds16s<1024>(c.evoff[1], p, c.ebuf);
  glds16s<2048>(c.evoff[2], p, c.ebuf);
  glds16s<3072>(c.evoff[3], p, c.ebuf);
  const char* p2 = p + 4096;
  glds16s<0>(c.evoff[0], p2, c.ebuf + 4096);
  glds16s<1024>(c.evoff[1], p2, c.ebuf + 4096);
  if (c.lane < 32) glds16s<2048>(c.evoff[2], p2, c.ebuf + 4096);
}

DI void read_e(const Ctx& c, u32x4 (&E)[8]) {
#pragma unroll
  for (int s = 0; s < 8; ++s) E[s] = *(lds_u32x4_ptr)(c.eread ^ (s << 5));
}

template <int IT>
DI void item_body(const Ctx& c, const u32x4 (&E)[8], u32x4 (&E2)[8], bf16x8 (&W)[2][8], f32x16& o0, f32x16& o1) {
  constexpr bool FIRST = IT == 0;
  const float cexp = 0.125f * 1.4426950408889634f;
  const int h = c.h;
  bf16x8 Qp[2][2], Kp[2][2], Vp[2][2], Pp[2];
  f32x16 a0 = {}, a1 = {}, a2 = {}, a3 = {}, a4 = {}, a5 = {}, S = {};
  u32x4 ob[4];
  LOADW(1, 0, 1);
#pragma unroll
  for (int s = 0; s < 8; ++s) a0 = MFMA(W[0][s], EB(s), a0);
  if (!FIRST) {
    *(lds_u32x4_ptr)(c.owr ^ 0) = relu_pack(o0, 0);
    *(lds_u32x4_ptr)(c.owr ^ 16) = relu_pack(o0, 1);
    *(lds_u32x4_ptr)(c.owr ^ 32) = relu_pack(o1, 0);
    *(lds_u32x4_ptr)(c.owr ^ 48) = relu_pack(o1, 1);
  }
  PATTERN(5);
  if (FIRST) { LBAR(); }
  LOADW(0, 1, 0);
  if (!FIRST) {
#pragma unroll
    for (int j = 0; j < 4; ++j) ob[j] = *(lds_u32x4_ptr)(c.ord + j * 1024);
  }
#pragma unroll
  for (int s = 0; s < 8; ++s) a1 = MFMA(W[1][s], EB(s), a1);
  Qp[0][0] = pack_step(a0, 0);
  Qp[0][1] = pack_step(a0, 1);
  PATTERN3(2);
  LOADW(1, 1, 1);
#pragma unroll
  for (int s = 0; s < 8; ++s) a2 = MFMA(W[0][s], EB(s), a2);
  Qp[1][0] = pack_step(a1, 0);
  Qp[1][1] = pack_step(a1, 1);
  if (!FIRST) {
    const int so = c.obase + (IT - 1) * (WAVES * EBYTES);
    __builtin_amdgcn_raw_buffer_store_b128(ob[0], c.rsrc, c.gvoff, so, ST_AUX);
    __builtin_amdgcn_raw_buffer_store_b128(ob[1], c.rsrc, c.gvoff + 2048, so, ST_AUX);
    __builtin_amdgcn_raw_buffer_store_b128(ob[2], c.rsrc, c.gvoff, so + 4096, ST_AUX);
    __builtin_amdgcn_raw_buffer_store_b128(ob[3], c.rsrc, c.gvoff3, so + 4096, ST_AUX);
  }
  PATTERN(2);
  if (FIRST) { LBAR(); }
  LOADW(0, 2, 0);
#pragma unroll
  for (int s = 0; s < 8; ++s) a3 = MFMA(W[1][s], EB(s), a3);
  Kp[0][0] = pack_step(a2, 0);
  Kp[0][1] = pack_step(a2, 1);
  PATTERN(2);
  if (FIRST) { LBAR(); }
  if (IT + 1 < IPW) {
    if (FIRST) { WAITV(0); } else { WAITV(4); }
    __builtin_amdgcn_sched_barrier(0);
  }
  LOADW(1, 2, 1);
  if (IT + 1 < IPW) read_e(c, E2);
#pragma unroll
  for (int s = 0; s < 8; ++s) a4 = MFMA(EB(s), W[0][s], a4);
  Kp[1][0] = pack_step(a3, 0);
  Kp[1][1] = pack_step(a3, 1);
  PATTERN3(2);
  S = MFMA(Kp[0][0], Qp[0][0], S);
  S = MFMA(Kp[0][1], Qp[0][1], S);
  S = MFMA(Kp[1][0], Qp[1][0], S);
  S = MFMA(Kp[1][1], Qp[1][1], S);
  __builtin_amdgcn_sched_barrier(0);
  if (IT + 2 < IPW) {
    asm volatile("s_waitcnt lgkmcnt(0)" ::: "memory");
    dma_e(c, IT + 2);
    __builtin_amdgcn_sched_barrier(0);
  }
  LOADW(0, 3, 0);
#pragma unroll
  for (int s = 0; s < 8; ++s) a5 = MFMA(EB(s), W[1][s], a5);
  Vp[0][0] = pack_step(a4, 0);
  Vp[0][1] = pack_step(a4, 1);
  float sv[14];
#pragma unroll
  for (int i = 0; i < 7; ++i) {
    unsigned u = pack2(S[2 * i], S[2 * i + 1]);
    sv[2 * i] = __uint_as_float(u << 16);
    sv[2 * i + 1] = __uint_as_float(u & 0xffff0000u);
  }
  float m = sv[0];
#pragma unroll
  for (int i = 1; i < 12; ++i) m = fmaxf(m, sv[i]);
  float m2 = fmaxf(m, fmaxf(sv[12], sv[13]));
  m = h == 0 ? m2 : m;
  {
    auto sw = __builtin_amdgcn_permlane32_swap(__float_as_uint(m), __float_as_uint(m), false, false);
    m = fmaxf(__uint_as_float(sw[0]), __uint_as_float(sw[1]));
  }
  PATTERN(6);
  LOADW(1, 3, 1);
  o0 = f32x16{};
#pragma unroll
  for (int s = 0; s < 8; ++s) o0 = MFMA(W[0][s], EB(s), o0);
  Vp[1][0] = pack_step(a5, 0);
  Vp[1][1] = pack_step(a5, 1);
  f32x16 P;
  const float mc = m * cexp;
  float sum = 0.f;
#pragma unroll
  for (int i = 0; i < 12; ++i) {
    P[i] = __builtin_amdgcn_exp2f(__builtin_fmaf(sv[i], cexp, -mc));
    sum += P[i];
  }
  {
    float p12 = __builtin_amdgcn_exp2f(__builtin_fmaf(sv[12], cexp, -mc));
    float p13 = __builtin_amdgcn_exp2f(__builtin_fmaf(sv[13], cexp, -mc));
    P[12] = h == 0 ? p12 : 0.f;
    P[13] = h == 0 ? p13 : 0.f;
    sum += P[12] + P[13];
  }
  {
    auto sw = __builtin_amdgcn_permlane32_swap(__float_as_uint(sum), __float_as_uint(sum), false, false);
    sum = __uint_as_float(sw[0]) + __uint_as_float(sw[1]);
  }
  PATTERN(7);
  LOADW(0, 0, 0);
  o1 = f32x16{};
#pragma unroll
  for (int s = 0; s < 8; ++s) o1 = MFMA(W[1][s], EB(s), o1);
  const float inv = __builtin_amdgcn_rcpf(sum);
#pragma unroll
  for (int i = 0; i < 14; ++i) P[i] *= inv;
  P[14] = 0.f;
  P[15] = 0.f;
  Pp[0] = pack_step(P, 0);
  Pp[1] = pack_step(P, 1);
  PATTERN(3);
  o0 = MFMA(Vp[0][0], Pp[0], o0);
  o0 = MFMA(Vp[0][1], Pp[1], o0);
  o1 = MFMA(Vp[1][0], Pp[0], o1);
  o1 = MFMA(Vp[1][1], Pp[1], o1);
  __builtin_amdgcn_sched_barrier(0);
}

__global__ __launch_bounds__(WAVES * 64, 2) void autoint_kernel(
    const __bf16* __restrict__ e, const __bf16* __restrict__ wq, const __bf16* __restrict__ wk,
    const __bf16* __restrict__ wv, const __bf16* __restrict__ wres, __bf16* __restrict__ out, unsigned long long* prof) {
  extern __shared__ __attribute__((aligned(16))) char smem[];
  const int tid = threadIdx.x;
  const int lane = tid & 63;
  const int wave = __builtin_amdgcn_readfirstlane(tid >> 6);
  const int r = lane & 31, h = lane >> 5;
  const unsigned lds0 = __builtin_amdgcn_readfirstlane((unsigned)(size_t)(lds_void_ptr)smem);
  const int bid = blockIdx.x;
  const int hd = (bid >> 3) & 1;
  const int grp = (bid & 7) | ((bid >> 4) << 3);
  const int b0 = grp * BPG + wave;

  Ctx c;
  c.h = h;
  c.lane = lane;
  c.prof = prof + (size_t)(blockIdx.x * WAVES + wave) * 32;
  STAMP(0);
  c.ebuf = lds0 + 65536 + wave * 12288;
  c.ebase = (const char*)e + (size_t)b0 * EBYTES;
  c.obase = b0 * EBYTES;
  {
    const int lq = lane >> 4, c0 = (lane & 15) ^ lq;
#pragma unroll
    for (int j = 0; j < 4; ++j) c.evoff[j] = (unsigned)((lq * 256 + (c0 << 4)) ^ (j << 6));
  }
  const bool leader = wave < 4;
  c.leader = leader;
  if (leader) {
    dma_e(c, 0);
  } else {
    unsigned o64[4], o128[4];
#pragma unroll
    for (int i = 0; i < 4; ++i) {
      const int pos = ((wave - 4) * 4 + i) * 64 + lane;
      const int blk2 = pos >> 4;
      const int dq = blk2 >> 1, t = blk2 & 1;
      const int q = (pos >> 2) & 3, pb = (pos >> 1) & 1, blk = pos & 1;
      const int d = 4 * dq + q, c8 = 4 * pb + 2 * t + blk;
      o64[i] = (unsigned)(d * 128 + c8 * 16);
      o128[i] = (unsigned)(d * 256 + c8 * 16);
    }
    const char* sq = (const char*)wq + hd * 16384;
    const char* sk = (const char*)wk + hd * 16384;
    const char* sv = (const char*)wv + hd * 16384;
    const char* sr = (const char*)wres + hd * 128;
    const unsigned dst = lds0 + (wave - 4) * 4096;
#pragma unroll
    for (int i = 0; i < 4; ++i) glds16s<0>(o64[i], sq, dst + i * 1024);
#pragma unroll
    for (int i = 0; i < 4; ++i) glds16s<0>(o64[i], sk, dst + 16384 + i * 1024);
#pragma unroll
    for (int i = 0; i < 4; ++i) glds16s<0>(o64[i], sv, dst + 32768 + i * 1024);
#pragma unroll
    for (int i = 0; i < 4; ++i) glds16s<0>(o128[i], sr, dst + 49152 + i * 1024);
    dma_e(c, 0);
  }
  __builtin_amdgcn_sched_barrier(0);

  {
    const int q4 = (lane & 15) >> 2, p4 = lane & 3, blk = (lane >> 4) & 1;
    c.lbase = lds0 + h * 1024 + 64 * q4 + 32 * (p4 & 1) + 16 * blk + 8 * (p4 >> 1);
    asm volatile("" : "+v"(c.lbase));
    c.eread = c.ebuf + r * 256 + ((h ^ (r & 15)) << 4);
    const unsigned obuf = c.ebuf + 8192;
    c.owr = obuf + r * 128 + (((4 * h) ^ (r & 7)) << 4);
    c.ord = obuf + lane * 16;
    const int orow = lane >> 3, oc = (lane & 7) ^ (orow & 7);
    c.gvoff = orow * 256 + hd * 128 + oc * 16;
    c.gvoff3 = lane < 16 ? c.gvoff + 2048 : 0x40000000;
    c.rsrc = __builtin_amdgcn_make_buffer_rsrc((void*)out, 0, NB_TOTAL * EBYTES, 0x00020000);
  }
  {
    u32x4 z = {};
    *(lds_u32x4_ptr)(c.ebuf + NF * 256 + lane * 16) = z;
    if (lane < 32) *(lds_u32x4_ptr)(c.ebuf + NF * 256 + 1024 + lane * 16) = z;
  }

  u32x4 EA[8], EB2[8];
  STAMP(1);
  if (leader) {
    __builtin_amdgcn_s_barrier();
  } else {
    WAITW(19);
    WAITW(15);
    WAITW(11);
    WAITW(7);
  }
  WAITV(0);
  __builtin_amdgcn_sched_barrier(0);
  STAMP(2);
  read_e(c, EA);
  bf16x8 W[2][8];
  LOADW(0, 0, 0);
  asm volatile("s_waitcnt lgkmcnt(0)" ::: "memory");
  __builtin_amdgcn_sched_barrier(0);
  dma_e(c, 1);
  __builtin_amdgcn_sched_barrier(0);
  STAMP(3);

  f32x16 o0 = {}, o1 = {};
  item_body<0>(c, EA, EB2, W, o0, o1);
  STAMP(4);
  item_body<1>(c, EB2, EA, W, o0, o1);
  STAMP(5);
  item_body<2>(c, EA, EB2, W, o0, o1);
  STAMP(6);
  item_body<3>(c, EB2, EA, W, o0, o1);
  STAMP(7);
  {
    *(lds_u32x4_ptr)(c.owr ^ 0) = relu_pack(o0, 0);
    *(lds_u32x4_ptr)(c.owr ^ 16) = relu_pack(o0, 1);
    *(lds_u32x4_ptr)(c.owr ^ 32) = relu_pack(o1, 0);
    *(lds_u32x4_ptr)(c.owr ^ 48) = relu_pack(o1, 1);
    u32x4 ob[4];
#pragma unroll
    for (int j = 0; j < 4; ++j) ob[j] = *(lds_u32x4_ptr)(c.ord + j * 1024);
    const int so = c.obase + (IPW - 1) * (WAVES * EBYTES);
    __builtin_amdgcn_raw_buffer_store_b128(ob[0], c.rsrc, c.gvoff, so, ST_AUX);
    __builtin_amdgcn_raw_buffer_store_b128(ob[1], c.rsrc, c.gvoff + 2048, so, ST_AUX);
    __builtin_amdgcn_raw_buffer_store_b128(ob[2], c.rsrc, c.gvoff, so + 4096, ST_AUX);
    __builtin_amdgcn_raw_buffer_store_b128(ob[3], c.rsrc, c.gvoff3, so + 4096, ST_AUX);
  }
  STAMP(8);
}

extern "C" void kernel_launch(void* const* d_in, const int* in_sizes, int n_in, void* d_out, int out_size,
                              void* d_ws, size_t ws_size, hipStream_t stream) {
  const __bf16* e = (const __bf16*)d_in[0];
  const __bf16* wq = (const __bf16*)d_in[1];
  const __bf16* wk = (const __bf16*)d_in[2];
  const __bf16* wv = (const __bf16*)d_in[3];
  const __bf16* wres = (const __bf16*)d_in[4];
  __bf16* out = (__bf16*)d_out;
  (void)hipFuncSetAttribute((const void*)autoint_kernel, hipFuncAttributeMaxDynamicSharedMemorySize, LDS_BYTES);
  autoint_kernel<<<2 * NB_TOTAL / BPG, WAVES * 64, LDS_BYTES, stream>>>(e, wq, wk, wv, wres, out, (unsigned long long*)d_ws);
}
```

```cpp
#include <hip/hip_runtime.h>
#include <hip/hip_bf16.h>

typedef __attribute__((ext_vector_type(8))) __bf16 bf16x8;
typedef __attribute__((ext_vector_type(2))) __bf16 bf16x2;
typedef __attribute__((ext_vector_type(4))) short s16x4;
typedef __attribute__((ext_vector_type(2))) short s16x2;
typedef __attribute__((ext_vector_type(4))) float f32x4;
typedef __attribute__((ext_vector_type(4))) unsigned u32x4;
typedef __attribute__((ext_vector_type(2))) unsigned u32x2;
typedef __attribute__((address_space(3))) s16x4* lds_s16x4_ptr;
typedef __attribute__((address_space(3))) u32x4* lds_u32x4_ptr;
typedef __attribute__((address_space(3))) u32x2* lds_u32x2_ptr;
typedef __attribute__((address_space(3))) void* lds_void_ptr;

#define DI __device__ __forceinline__
#define BF(x) __builtin_bit_cast(bf16x8, (x))
#define MFMA(a, b, c) __builtin_amdgcn_mfma_f32_16x16x32_bf16(BF(a), BF(b), (c), 0, 0, 0)

#ifndef NVS
#define NVS 3
#endif
#define PRIO 3
#define ST_AUX 17

constexpr int NB_TOTAL = 4096;
constexpr int NF = 26;
constexpr int ND = 128;
constexpr int WAVES = 8;
constexpr int EPW = 4;
constexpr int BPG = WAVES * EPW;
constexpr int EBYTES = NF * ND * 2;
constexpr int LDS_BYTES = 65536 + WAVES * 12288;

DI unsigned pack2(float lo, float hi) {
  bf16x2 v;
  v[0] = (__bf16)lo;
  v[1] = (__bf16)hi;
  return __builtin_bit_cast(unsigned, v);
}
DI unsigned relu2(float lo, float hi) {
  unsigned u = pack2(lo, hi);
  asm volatile("" : "+v"(u));
  s16x2 z = {};
  return __builtin_bit_cast(unsigned, __builtin_elementwise_max(__builtin_bit_cast(s16x2, u), z));
}

DI void glds4(unsigned v0, unsigned v1, unsigned v2, unsigned v3, const void* sbase, unsigned lds_dst) {
  asm volatile(
      "s_mov_b32 m0, %5\n\ts_nop 0\n\t"
      "global_load_lds_dwordx4 %0, %4\n\t"
      "global_load_lds_dwordx4 %1, %4 offset:1024\n\t"
      "global_load_lds_dwordx4 %2, %4 offset:2048\n\t"
      "global_load_lds_dwordx4 %3, %4 offset:3072"
      :
      : "v"(v0), "v"(v1), "v"(v2), "v"(v3), "s"(sbase), "s"(lds_dst)
      : "memory", "m0");
}
DI void glds2(unsigned v0, unsigned v1, const void* sbase, unsigned lds_dst) {
  asm volatile(
      "s_mov_b32 m0, %3\n\ts_nop 0\n\t"
      "global_load_lds_dwordx4 %0, %2\n\t"
      "global_load_lds_dwordx4 %1, %2 offset:1024"
      :
      : "v"(v0), "v"(v1), "s"(sbase), "s"(lds_dst)
      : "memory", "m0");
}

#define SGB(mask, n) __builtin_amdgcn_sched_group_barrier(mask, n, 0)
#define SBAR() __builtin_amdgcn_sched_barrier(0)
#define WAITV(n) asm volatile("s_waitcnt vmcnt(" #n ")" ::: "memory");
#define WAITW(n)                  \
  WAITV(n)                        \
  __builtin_amdgcn_s_barrier();   \
  __builtin_amdgcn_sched_barrier(0);

#define STAMP(k)
#define LBAR()                                      \
  if (c.leader) __builtin_amdgcn_s_barrier();       \
  __builtin_amdgcn_sched_barrier(0);

struct Ctx {
  bool leader;
  unsigned long long* prof;
  unsigned wl;
  unsigned eread;
  unsigned ebuf;
  unsigned owr;
  unsigned ord;
  unsigned evoff[4];
  int gvoff;
  int lane, cc, g;
  const char* ebase;
  int obase;
  __amdgpu_buffer_rsrc_t rsrc;
};

struct St {
  u32x4 E[2][4];
  u32x4 W[3][4];
  u32x4 Qp[7][2], Kp[7][2];
  u32x2 Vp[7][4];
  f32x4 O[7][4];
};

DI void loadw(const Ctx& c, u32x4 (&W)[4], int n) {
  const int X = n >> 2, j = n & 3;
#pragma unroll
  for (int s = 0; s < 4; ++s) W[s] = *(lds_u32x4_ptr)(c.wl + X * 16384 + s * 4096 + j * 1024);
}
DI void relayout(const unsigned (&wb)[4], unsigned wl, int off) {
  u32x4 W[4];
#pragma unroll
  for (int j = 0; j < 4; ++j) {
    s16x4 lo = __builtin_amdgcn_ds_read_tr16_b64_v4i16((lds_s16x4_ptr)(wb[j] + off));
    s16x4 hi = __builtin_amdgcn_ds_read_tr16_b64_v4i16((lds_s16x4_ptr)(wb[j] + off + 2048));
    W[j] = __builtin_bit_cast(u32x4, __builtin_shufflevector(lo, hi, 0, 1, 2, 3, 4, 5, 6, 7));
  }
  asm volatile("s_waitcnt lgkmcnt(0)" ::: "memory");
#pragma unroll
  for (int j = 0; j < 4; ++j) *(lds_u32x4_ptr)(wl + off + j * 1024) = W[j];
  asm volatile("s_waitcnt lgkmcnt(0)" ::: "memory");
}

template <int G>
DI void dma_e(const Ctx& c) {
  const char* p = c.ebase + (size_t)G * 8192;
  if (G < 3) {
    glds4(c.evoff[0], c.evoff[1], c.evoff[2], c.evoff[3], p, c.ebuf);
    glds4(c.evoff[0], c.evoff[1], c.evoff[2], c.evoff[3], p + 4096, c.ebuf + 4096);
  } else {
    glds2(c.evoff[0], c.evoff[1], p, c.ebuf);
  }
}

template <int NT>
DI void read_e(const Ctx& c, u32x4 (&E)[2][4]) {
#pragma unroll
  for (int s = 0; s < 4; ++s) {
    const unsigned a = c.eread ^ (s << 6);
    E[0][s] = *(lds_u32x4_ptr)(a);
    if (NT > 1) E[1][s] = *(lds_u32x4_ptr)(a + 4096);
  }
}

DI void stage_out(const Ctx& c, const f32x4 (&O)[4], unsigned slot) {
#pragma unroll
  for (int j = 0; j < 4; ++j) {
    u32x2 v;
    v[0] = relu2(O[j][0], O[j][1]);
    v[1] = relu2(O[j][2], O[j][3]);
    *(lds_u32x2_ptr)((c.owr ^ (j << 5)) + slot) = v;
  }
}

DI float xmax(float m) {
  auto a = __builtin_amdgcn_permlane32_swap(__float_as_uint(m), __float_as_uint(m), false, false);
  asm("v_max_f32 %0, %1, %2" : "=v"(m) : "v"(a[0]), "v"(a[1]));
  auto b = __builtin_amdgcn_permlane16_swap(__float_as_uint(m), __float_as_uint(m), false, false);
  asm("v_max_f32 %0, %1, %2" : "=v"(m) : "v"(b[0]), "v"(b[1]));
  return m;
}
DI float xsum(float m) {
  auto a = __builtin_amdgcn_permlane32_swap(__float_as_uint(m), __float_as_uint(m), false, false);
  m = __uint_as_float(a[0]) + __uint_as_float(a[1]);
  auto b = __builtin_amdgcn_permlane16_swap(__float_as_uint(m), __float_as_uint(m), false, false);
  return __uint_as_float(b[0]) + __uint_as_float(b[1]);
}
DI u32x4 sel4(bool p, const u32x4& a, const u32x4& b) {
  u32x4 r;
#pragma unroll
  for (int i = 0; i < 4; ++i) r[i] = p ? a[i] : b[i];
  return r;
}

#define PAT(NT_, ND_, NV)                          \
  _Pragma("unroll") for (int q_ = 0; q_ < 4; ++q_) { \
    SGB(0x8, 1);                                   \
    SGB(0x100, ND_);                               \
    SGB(0x2, NV);                                  \
    if ((NT_) > 1) { SGB(0x8, 1); SGB(0x2, NV); }  \
    if ((NT_) > 2) { SGB(0x8, 1); SGB(0x2, NV); }  \
  }

struct Loc {
  f32x4 acc[2][2];
  f32x4 S[2][2];
  float sv[2][8], mx[2], sm[2];
  u32x4 Pp[2];
  u32x4 ob[4];
  u32x4 QA[2], KA[2];
  u32x2 VA[4];
};

template <int G, int n>
DI void job(const Ctx& c, St& st, Loc& L) {
  constexpr int NT = G < 3 ? 2 : 1;
  constexpr int T0 = 2 * G;
  constexpr int NM = 4 * NT;
  constexpr int NDS = 8 / NM;
  constexpr int VS = 2 / NT;
  constexpr int WB = (16 * G) % 3;
  constexpr int TA = G == 0 ? 0 : G == 1 ? 1 : G == 2 ? 3 : 4;
  constexpr int TB = G == 0 ? 1 : G == 1 ? 2 : G == 2 ? 4 : 5;
  constexpr int X = n >> 2, j = n & 3;
  const float cexp = 0.125f * 1.4426950408889634f;
  const float NINF = -__builtin_inff();
  const int g = c.g, cc = c.cc;
  if (n + 2 < 16) loadw(c, st.W[(WB + n + 2) % 3], n + 2);
  else if (G < 3) loadw(c, st.W[(WB + n + 2) % 3], n + 2 - 16);
  const u32x4(&W)[4] = st.W[(WB + n) % 3];
#pragma unroll
  for (int tt = 0; tt < NT; ++tt) L.acc[n & 1][tt] = f32x4{};
#pragma unroll
  for (int s = 0; s < 4; ++s)
#pragma unroll
    for (int tt = 0; tt < NT; ++tt)
      L.acc[n & 1][tt] = X == 2 ? MFMA(st.E[tt][s], W[s], L.acc[n & 1][tt]) : MFMA(W[s], st.E[tt][s], L.acc[n & 1][tt]);
  if (X == 3) {
#pragma unroll
    for (int tt = 0; tt < NT; ++tt) st.O[T0 + tt][j] = L.acc[n & 1][tt];
  }
  if (n > 0 && n <= 12) {
    constexpr int pX = (n - 1) >> 2, pj = (n - 1) & 3;
#pragma unroll
    for (int tt = 0; tt < NT; ++tt) {
      const f32x4 a = L.acc[(n - 1) & 1][tt];
      const unsigned lo = pack2(a[0], a[1]), hi = pack2(a[2], a[3]);
      if (pX == 0) { st.Qp[T0 + tt][pj >> 1][2 * (pj & 1)] = lo; st.Qp[T0 + tt][pj >> 1][2 * (pj & 1) + 1] = hi; }
      if (pX == 1) { st.Kp[T0 + tt][pj >> 1][2 * (pj & 1)] = lo; st.Kp[T0 + tt][pj >> 1][2 * (pj & 1) + 1] = hi; }
      if (pX == 2) { st.Vp[T0 + tt][pj][0] = lo; st.Vp[T0 + tt][pj][1] = hi; }
    }
  }
  constexpr int nv = (G > 0 && n == 0) || (G == 2 && n == 1) ? 3
                     : ((G & 1) && (n == 5 || n == 8)) ? 2
                     : (n >= 9) ? NVS : 1;
  if (G == 1 || G == 3) {
    constexpr int TO = G == 1 ? 0 : 3;
    if (n == 0) stage_out(c, st.O[TO], 0);
    if (n == 2) { L.ob[0] = *(lds_u32x4_ptr)(c.ord); L.ob[1] = *(lds_u32x4_ptr)(c.ord + 1024); }
    if (n == 4) {
      __builtin_amdgcn_raw_buffer_store_b128(L.ob[0], c.rsrc, c.gvoff, c.obase + TO * 4096, ST_AUX);
      __builtin_amdgcn_raw_buffer_store_b128(L.ob[1], c.rsrc, c.gvoff + 2048, c.obase + TO * 4096, ST_AUX);
    }
  }
  if (G == 2) {
    if (n == 0) stage_out(c, st.O[1], 0);
    if (n == 1) stage_out(c, st.O[2], 2048);
    if (n == 3) {
#pragma unroll
      for (int k = 0; k < 4; ++k) L.ob[k] = *(lds_u32x4_ptr)(c.ord + k * 1024);
    }
    if (n == 5) {
#pragma unroll
      for (int k = 0; k < 4; ++k)
        __builtin_amdgcn_raw_buffer_store_b128(L.ob[k], c.rsrc, c.gvoff + (k & 1) * 2048, c.obase + (1 + (k >> 1)) * 4096, ST_AUX);
    }
  }
  if (G == 1 || G == 3) {
    constexpr int TL = G == 1 ? 3 : 6;
    constexpr int CUT = G == 1 ? 10 : 14;
    if (n == 5) {
      L.QA[0] = sel4(cc >= CUT, st.Qp[TA][0], st.Qp[TL][0]);
      L.QA[1] = sel4(cc >= CUT, st.Qp[TA][1], st.Qp[TL][1]);
    }
    if (n == 8) {
      L.KA[0] = sel4(cc >= CUT, st.Kp[TA][0], st.Kp[TL][0]);
      L.KA[1] = sel4(cc >= CUT, st.Kp[TA][1], st.Kp[TL][1]);
    }
    if (n == 12) {
#pragma unroll
      for (int k = 0; k < 4; ++k) {
        L.VA[k][0] = (G == 1 && g == 3) ? st.Vp[TA][k][0] : st.Vp[TL][k][0];
        L.VA[k][1] = (G == 1 ? g >= 2 : g == 3) ? st.Vp[TA][k][1] : st.Vp[TL][k][1];
      }
    }
  } else {
    if (n == 5) { L.QA[0] = st.Qp[TA][0]; L.QA[1] = st.Qp[TA][1]; }
    if (n == 8) { L.KA[0] = st.Kp[TA][0]; L.KA[1] = st.Kp[TA][1]; }
    if (n == 12) {
#pragma unroll
      for (int k = 0; k < 4; ++k) L.VA[k] = st.Vp[TA][k];
    }
  }
  if (n == 9 || n == 10) {
    constexpr int qs = (n - 9) & 1;
#pragma unroll
    for (int ks = 0; ks < 2; ++ks)
#pragma unroll
      for (int i = 0; i < 2; ++i) {
        float lo = L.S[ks][qs][2 * i], hi = L.S[ks][qs][2 * i + 1];
        bool masked = false, ok = true;
        if (G == 0 && ks == 1) { masked = true; ok = i == 0 ? g < 3 : g < 2; }
        if (G == 1 && ks == 0) { masked = true; ok = i == 0 ? (g == 0 || g == 3) : g != 1; }
        if (G == 2 && ks == 0) { masked = true; ok = g >= 1; }
        if (G == 2 && ks == 1 && i == 1) { masked = true; ok = g < 3; }
        if (G == 3 && ks == 0) { masked = true; ok = i == 0 ? g < 2 : g != 2; }
        if (masked) { lo = ok ? lo : NINF; hi = ok ? hi : NINF; }
        L.sv[qs][4 * ks + 2 * i] = lo;
        L.sv[qs][4 * ks + 2 * i + 1] = hi;
      }
    float m = L.sv[qs][0];
#pragma unroll
    for (int i = 1; i < 8; ++i) m = fmaxf(m, L.sv[qs][i]);
    L.mx[qs] = xmax(m) * cexp;
  }
  if (n == 11 || n == 12) {
    constexpr int qs = (n - 11) & 1;
    float s = 0.f;
#pragma unroll
    for (int i = 0; i < 8; ++i) {
      L.sv[qs][i] = __builtin_amdgcn_exp2f(__builtin_fmaf(L.sv[qs][i], cexp, -L.mx[qs]));
      s += L.sv[qs][i];
    }
    L.sm[qs] = xsum(s);
  }
  if (n == 13 || n == 14) {
    constexpr int qs = (n - 13) & 1;
    float inv = __builtin_amdgcn_rcpf(L.sm[qs]);
    if (G == 0 && qs == 1) inv = cc < 10 ? inv : 0.f;
    if (G == 2 && qs == 0) inv = cc >= 4 ? inv : 0.f;
    if (G == 2 && qs == 1) inv = cc < 14 ? inv : 0.f;
#pragma unroll
    for (int i = 0; i < 4; ++i) L.Pp[qs][i] = pack2(L.sv[qs][2 * i] * inv, L.sv[qs][2 * i + 1] * inv);
  }
  if (n == 2 || n == 3) { PAT(NT, 2, nv * VS); }
  else { PAT(NT, 1, nv * VS); }
  if (n < 9 || n == 15) SBAR();
  if (G == 0 && (n == 1 || n == 5)) { LBAR(); }
  if (n == 8) {
    L.S[0][0] = MFMA(L.KA[0], L.QA[0], f32x4{});
    L.S[1][0] = MFMA(st.Kp[TB][0], L.QA[0], f32x4{});
    L.S[0][1] = MFMA(L.KA[0], st.Qp[TB][0], f32x4{});
    L.S[1][1] = MFMA(st.Kp[TB][0], st.Qp[TB][0], f32x4{});
    L.S[0][0] = MFMA(L.KA[1], L.QA[1], L.S[0][0]);
    L.S[1][0] = MFMA(st.Kp[TB][1], L.QA[1], L.S[1][0]);
    L.S[0][1] = MFMA(L.KA[1], st.Qp[TB][1], L.S[0][1]);
    L.S[1][1] = MFMA(st.Kp[TB][1], st.Qp[TB][1], L.S[1][1]);
    SBAR();
    if (G == 0) { LBAR(); }
  }
  if constexpr (n + 1 < 16) job<G, n + 1>(c, st, L);
}

template <int G>
DI void group_body(const Ctx& c, St& st) {
  constexpr int TA = G == 0 ? 0 : G == 1 ? 1 : G == 2 ? 3 : 4;
  constexpr int TB = G == 0 ? 1 : G == 1 ? 2 : G == 2 ? 4 : 5;
  const int cc = c.cc;
  Loc L;
  job<G, 0>(c, st, L);
  if (G < 3) {
    if (G == 0) { WAITV(0); } else if (G == 1) { WAITV(2); } else { WAITV(4); }
    SBAR();
    read_e<(G < 2 ? 2 : 1)>(c, st.E);
  }
  {
    u32x4 Vf[4];
#pragma unroll
    for (int j = 0; j < 4; ++j) {
      Vf[j][0] = L.VA[j][0]; Vf[j][1] = L.VA[j][1];
      Vf[j][2] = st.Vp[TB][j][0]; Vf[j][3] = st.Vp[TB][j][1];
    }
    const u32x4 z = {};
    if (G == 1 || G == 3) {
      constexpr int TL = G == 1 ? 3 : 6;
      constexpr int CUT = G == 1 ? 10 : 14;
      const u32x4 Ph = sel4(cc >= CUT, L.Pp[0], z);
      const u32x4 Pl = G == 1 ? sel4(cc < 4, L.Pp[0], z) : L.Pp[0];
#pragma unroll
      for (int j = 0; j < 4; ++j) st.O[TB][j] = MFMA(Vf[j], L.Pp[1], st.O[TB][j]);
#pragma unroll
      for (int j = 0; j < 4; ++j) st.O[TA][j] = MFMA(Vf[j], Ph, st.O[TA][j]);
#pragma unroll
      for (int j = 0; j < 4; ++j) st.O[TL][j] = MFMA(Vf[j], Pl, st.O[TL][j]);
    } else {
#pragma unroll
      for (int j = 0; j < 4; ++j) st.O[TA][j] = MFMA(Vf[j], L.Pp[0], st.O[TA][j]);
#pragma unroll
      for (int j = 0; j < 4; ++j) st.O[TB][j] = MFMA(Vf[j], L.Pp[1], st.O[TB][j]);
    }
  }
  SBAR();
  if (G < 2) {
    asm volatile("s_waitcnt lgkmcnt(0)" ::: "memory");
    dma_e<G + 2>(c);
    SBAR();
  }
}

__global__ __launch_bounds__(WAVES * 64, 2) void autoint_kernel(
    const __bf16* __restrict__ e, const __bf16* __restrict__ wq, const __bf16* __restrict__ wk,
    const __bf16* __restrict__ wv, const __bf16* __restrict__ wres, __bf16* __restrict__ out, unsigned long long* prof) {
  extern __shared__ __attribute__((aligned(16))) char smem[];
  const int tid = threadIdx.x;
  const int lane = tid & 63;
  const int wave = __builtin_amdgcn_readfirstlane(tid >> 6);
  const int cc = lane & 15, g = lane >> 4;
  const unsigned lds0 = __builtin_amdgcn_readfirstlane((unsigned)(size_t)(lds_void_ptr)smem);
  const int bid = blockIdx.x;
  const int hd = (bid >> 3) & 1;
  const int grp = (bid & 7) | ((bid >> 4) << 3);
  const int b0 = grp * BPG + wave * EPW;

  Ctx c;
  c.lane = lane;
  c.cc = cc;
  c.g = g;
  c.prof = prof + (size_t)(blockIdx.x * WAVES + wave) * 32;
  STAMP(0);
  c.ebuf = lds0 + 65536 + wave * 12288;
  c.ebase = (const char*)e + (size_t)b0 * EBYTES;
  c.obase = b0 * EBYTES;
  {
    const int c0 = cc ^ g;
#pragma unroll
    for (int j = 0; j < 4; ++j) c.evoff[j] = (unsigned)((g * 256 + (c0 << 4)) ^ (j << 6));
  }
  const bool leader = wave < 4;
  c.leader = leader;
  if (leader) {
    dma_e<0>(c);
  } else {
    unsigned o64[4], o128[4];
    const int r8 = lane >> 3;
    const int krow = 32 * (wave - 4) + 8 * (r8 >> 2) + (r8 & 3);
    const int ch = (lane & 7) ^ (r8 & 6);
#pragma unroll
    for (int i = 0; i < 4; ++i) {
      const int k = krow + 16 * (i & 1) + 4 * (i >> 1);
      o64[i] = (unsigned)(k * 128 + ch * 16 + 4096 - i * 1024);
      o128[i] = (unsigned)(k * 256 + ch * 16 + 4096 - i * 1024);
    }
    const char* sq = (const char*)wq + hd * 16384;
    const char* sk = (const char*)wk + hd * 16384;
    const char* sv = (const char*)wv + hd * 16384;
    const char* sr = (const char*)wres + hd * 128;
    const unsigned dst = lds0 + (wave - 4) * 4096;
    glds4(o64[0], o64[1], o64[2], o64[3], sq - 4096, dst);
    glds4(o64[0], o64[1], o64[2], o64[3], sk - 4096, dst + 16384);
    glds4(o64[0], o64[1], o64[2], o64[3], sv - 4096, dst + 32768);
    glds4(o128[0], o128[1], o128[2], o128[3], sr - 4096, dst + 49152);
    dma_e<0>(c);
  }
  SBAR();

  unsigned wb[4];
  {
    const int q4 = (lane & 15) >> 2, p4 = lane & 3;
    const int r8 = (g & 1) * 4 + q4;
#pragma unroll
    for (int j = 0; j < 4; ++j)
      wb[j] = lds0 + (wave & 3) * 4096 + (g >> 1) * 1024 + r8 * 128 + ((((2 * j) ^ (r8 & 6)) + (p4 >> 1)) << 4) + (p4 & 1) * 8;
    c.wl = lds0 + lane * 16;
    asm volatile("" : "+v"(c.wl));
    c.eread = c.ebuf + cc * 256 + ((g ^ cc) << 4);
    const unsigned obuf = c.ebuf + 8192;
    c.owr = obuf + cc * 128 + (((g >> 1) ^ (cc & 7)) << 4) + (g & 1) * 8;
    c.ord = obuf + lane * 16;
    const int orow = lane >> 3, oc = (lane & 7) ^ (orow & 7);
    c.gvoff = orow * 256 + hd * 128 + oc * 16;
    c.rsrc = __builtin_amdgcn_make_buffer_rsrc((void*)out, 0, NB_TOTAL * EBYTES, 0x00020000);
  }

  St st;
  STAMP(1);
  if (leader) {
    __builtin_amdgcn_s_barrier();
  } else {
    const unsigned wlw = c.wl + (wave & 3) * 4096;
    WAITV(20); relayout(wb, wlw, 0);     __builtin_amdgcn_s_barrier(); SBAR();
    WAITV(16); relayout(wb, wlw, 16384); __builtin_amdgcn_s_barrier(); SBAR();
    WAITV(12); relayout(wb, wlw, 32768); __builtin_amdgcn_s_barrier(); SBAR();
    WAITV(8);  relayout(wb, wlw, 49152); __builtin_amdgcn_s_barrier(); SBAR();
    __builtin_amdgcn_s_setprio(1);
  }
  WAITV(0);
  SBAR();
  STAMP(2);
  read_e<2>(c, st.E);
  loadw(c, st.W[0], 0);
  loadw(c, st.W[1], 1);
  asm volatile("s_waitcnt lgkmcnt(0)" ::: "memory");
  SBAR();
  dma_e<1>(c);
  SBAR();
  STAMP(3);

  group_body<0>(c, st);
  STAMP(4);
  group_body<1>(c, st);
  STAMP(5);
  group_body<2>(c, st);
  STAMP(6);
  __builtin_amdgcn_s_setprio(0);
  group_body<3>(c, st);
  STAMP(7);
  {
    stage_out(c, st.O[4], 0);
    stage_out(c, st.O[5], 2048);
    stage_out(c, st.O[6], (unsigned)-8192);
    u32x4 ob[5];
#pragma unroll
    for (int k = 0; k < 4; ++k) ob[k] = *(lds_u32x4_ptr)(c.ord + k * 1024);
    ob[4] = *(lds_u32x4_ptr)(c.ord - 8192);
#pragma unroll
    for (int k = 0; k < 4; ++k)
      __builtin_amdgcn_raw_buffer_store_b128(ob[k], c.rsrc, c.gvoff + (k & 1) * 2048, c.obase + (4 + (k >> 1)) * 4096, ST_AUX);
    __builtin_amdgcn_raw_buffer_store_b128(ob[4], c.rsrc, c.gvoff, c.obase + 6 * 4096, ST_AUX);
  }
  STAMP(8);
}

extern "C" void kernel_launch(void* const* d_in, const int* in_sizes, int n_in, void* d_out, int out_size,
                              void* d_ws, size_t ws_size, hipStream_t stream) {
  const __bf16* e = (const __bf16*)d_in[0];
  const __bf16* wq = (const __bf16*)d_in[1];
  const __bf16* wk = (const __bf16*)d_in[2];
  const __bf16* wv = (const __bf16*)d_in[3];
  const __bf16* wres = (const __bf16*)d_in[4];
  __bf16* out = (__bf16*)d_out;
  (void)hipFuncSetAttribute((const void*)autoint_kernel, hipFuncAttributeMaxDynamicSharedMemorySize, LDS_BYTES);
  autoint_kernel<<<2 * NB_TOTAL / BPG, WAVES * 64, LDS_BYTES, stream>>>(e, wq, wk, wv, wres, out, (unsigned long long*)d_ws);
}
```

```cpp
#define CODEPF 3
#include <hip/hip_runtime.h>
#include <hip/hip_bf16.h>

typedef __attribute__((ext_vector_type(8))) __bf16 bf16x8;
typedef __attribute__((ext_vector_type(2))) __bf16 bf16x2;
typedef __attribute__((ext_vector_type(4))) short s16x4;
typedef __attribute__((ext_vector_type(2))) short s16x2;
typedef __attribute__((ext_vector_type(4))) float f32x4;
typedef __attribute__((ext_vector_type(4))) unsigned u32x4;
typedef __attribute__((ext_vector_type(2))) unsigned u32x2;
typedef __attribute__((address_space(3))) s16x4* lds_s16x4_ptr;
typedef __attribute__((address_space(3))) u32x4* lds_u32x4_ptr;
typedef __attribute__((address_space(3))) u32x2* lds_u32x2_ptr;
typedef __attribute__((address_space(3))) void* lds_void_ptr;

#define DI __device__ __forceinline__
#define BF(x) __builtin_bit_cast(bf16x8, (x))
#define MFMA(a, b, c) __builtin_amdgcn_mfma_f32_16x16x32_bf16(BF(a), BF(b), (c), 0, 0, 0)

#ifndef NVS
#define NVS 3
#endif
#define PRIO 3
#define ST_AUX 17

constexpr int NB_TOTAL = 4096;
constexpr int NF = 26;
constexpr int ND = 128;
constexpr int WAVES = 8;
constexpr int EPW = 4;
constexpr int BPG = WAVES * EPW;
constexpr int EBYTES = NF * ND * 2;
constexpr int LDS_BYTES = 65536 + WAVES * 12288;

DI unsigned pack2(float lo, float hi) {
  bf16x2 v;
  v[0] = (__bf16)lo;
  v[1] = (__bf16)hi;
  return __builtin_bit_cast(unsigned, v);
}
DI unsigned relu2(float lo, float hi) {
  unsigned u = pack2(lo, hi);
  asm volatile("" : "+v"(u));
  s16x2 z = {};
  return __builtin_bit_cast(unsigned, __builtin_elementwise_max(__builtin_bit_cast(s16x2, u), z));
}

DI void glds4(unsigned v0, unsigned v1, unsigned v2, unsigned v3, const void* sbase, unsigned lds_dst) {
  asm volatile(
      "s_mov_b32 m0, %5\n\ts_nop 0\n\t"
      "global_load_lds_dwordx4 %0, %4\n\t"
      "global_load_lds_dwordx4 %1, %4 offset:1024\n\t"
      "global_load_lds_dwordx4 %2, %4 offset:2048\n\t"
      "global_load_lds_dwordx4 %3, %4 offset:3072"
      :
      : "v"(v0), "v"(v1), "v"(v2), "v"(v3), "s"(sbase), "s"(lds_dst)
      : "memory", "m0");
}
DI void glds2(unsigned v0, unsigned v1, const void* sbase, unsigned lds_dst) {
  asm volatile(
      "s_mov_b32 m0, %3\n\ts_nop 0\n\t"
      "global_load_lds_dwordx4 %0, %2\n\t"
      "global_load_lds_dwordx4 %1, %2 offset:1024"
      :
      : "v"(v0), "v"(v1), "s"(sbase), "s"(lds_dst)
      : "memory", "m0");
}

#define SGB(mask, n) __builtin_amdgcn_sched_group_barrier(mask, n, 0)
#define SBAR() __builtin_amdgcn_sched_barrier(0)
#define WAITV(n) asm volatile("s_waitcnt vmcnt(" #n ")" ::: "memory");
#define WAITW(n)                  \
  WAITV(n)                        \
  __builtin_amdgcn_s_barrier();   \
  __builtin_amdgcn_sched_barrier(0);

#define STAMP(k)
#define LBAR()                                      \
  if (c.leader) __builtin_amdgcn_s_barrier();       \
  __builtin_amdgcn_sched_barrier(0);

struct Ctx {
  bool leader;
  unsigned long long* prof;
  unsigned wl;
  unsigned eread;
  unsigned ebuf;
  unsigned owr;
  unsigned ord;
  unsigned evoff[4];
  int gvoff;
  int lane, cc, g;
  const char* ebase;
  int obase;
  __amdgpu_buffer_rsrc_t rsrc;
};

struct St {
  u32x4 E[2][4];
  u32x4 W[3][4];
  u32x4 Qp[7][2], Kp[7][2];
  u32x2 Vp[7][4];
  f32x4 O[7][4];
};

DI void loadw(const Ctx& c, u32x4 (&W)[4], int n) {
  const int X = n >> 2, j = n & 3;
#pragma unroll
  for (int s = 0; s < 4; ++s) W[s] = *(lds_u32x4_ptr)(c.wl + X * 16384 + s * 4096 + j * 1024);
}
DI void relayout(const unsigned (&wb)[4], unsigned wl, int off) {
  u32x4 W[4];
#pragma unroll
  for (int j = 0; j < 4; ++j) {
    s16x4 lo = __builtin_amdgcn_ds_read_tr16_b64_v4i16((lds_s16x4_ptr)(wb[j] + off));
    s16x4 hi = __builtin_amdgcn_ds_read_tr16_b64_v4i16((lds_s16x4_ptr)(wb[j] + off + 2048));
    W[j] = __builtin_bit_cast(u32x4, __builtin_shufflevector(lo, hi, 0, 1, 2, 3, 4, 5, 6, 7));
  }
  asm volatile("s_waitcnt lgkmcnt(0)" ::: "memory");
#pragma unroll
  for (int j = 0; j < 4; ++j) *(lds_u32x4_ptr)(wl + off + j * 1024) = W[j];
  asm volatile("s_waitcnt lgkmcnt(0)" ::: "memory");
}

template <int G>
DI void dma_e(const Ctx& c) {
  const char* p = c.ebase + (size_t)G * 8192;
  if (G < 3) {
    glds4(c.evoff[0], c.evoff[1], c.evoff[2], c.evoff[3], p, c.ebuf);
    glds4(c.evoff[0], c.evoff[1], c.evoff[2], c.evoff[3], p + 4096, c.ebuf + 4096);
  } else {
    glds2(c.evoff[0], c.evoff[1], p, c.ebuf);
  }
}

template <int NT>
DI void read_e(const Ctx& c, u32x4 (&E)[2][4]) {
#pragma unroll
  for (int s = 0; s < 4; ++s) {
    const unsigned a = c.eread ^ (s << 6);
    E[0][s] = *(lds_u32x4_ptr)(a);
    if (NT > 1) E[1][s] = *(lds_u32x4_ptr)(a + 4096);
  }
}

DI void stage_out(const Ctx& c, const f32x4 (&O)[4], unsigned slot) {
#pragma unroll
  for (int j = 0; j < 4; ++j) {
    u32x2 v;
    v[0] = relu2(O[j][0], O[j][1]);
    v[1] = relu2(O[j][2], O[j][3]);
    *(lds_u32x2_ptr)((c.owr ^ (j << 5)) + slot) = v;
  }
}

DI float xmax(float m) {
  auto a = __builtin_amdgcn_permlane32_swap(__float_as_uint(m), __float_as_uint(m), false, false);
  asm("v_max_f32 %0, %1, %2" : "=v"(m) : "v"(a[0]), "v"(a[1]));
  auto b = __builtin_amdgcn_permlane16_swap(__float_as_uint(m), __float_as_uint(m), false, false);
  asm("v_max_f32 %0, %1, %2" : "=v"(m) : "v"(b[0]), "v"(b[1]));
  return m;
}
DI float xsum(float m) {
  auto a = __builtin_amdgcn_permlane32_swap(__float_as_uint(m), __float_as_uint(m), false, false);
  m = __uint_as_float(a[0]) + __uint_as_float(a[1]);
  auto b = __builtin_amdgcn_permlane16_swap(__float_as_uint(m), __float_as_uint(m), false, false);
  return __uint_as_float(b[0]) + __uint_as_float(b[1]);
}
DI u32x4 sel4(bool p, const u32x4& a, const u32x4& b) {
  u32x4 r;
#pragma unroll
  for (int i = 0; i < 4; ++i) r[i] = p ? a[i] : b[i];
  return r;
}

#define PAT(NT_, ND_, NV)                          \
  _Pragma("unroll") for (int q_ = 0; q_ < 4; ++q_) { \
    SGB(0x8, 1);                                   \
    SGB(0x100, ND_);                               \
    SGB(0x2, NV);                                  \
    if ((NT_) > 1) { SGB(0x8, 1); SGB(0x2, NV); }  \
    if ((NT_) > 2) { SGB(0x8, 1); SGB(0x2, NV); }  \
  }

struct Loc {
  f32x4 acc[2][2];
  f32x4 S[2][2];
  float sv[2][8], mx[2], sm[2];
  u32x4 Pp[2];
  u32x4 ob[4];
  u32x4 QA[2], KA[2];
  u32x2 VA[4];
};

template <int G, int n>
DI void job(const Ctx& c, St& st, Loc& L) {
  constexpr int NT = G < 3 ? 2 : 1;
  constexpr int T0 = 2 * G;
  constexpr int NM = 4 * NT;
  constexpr int NDS = 8 / NM;
  constexpr int VS = 2 / NT;
  constexpr int WB = (16 * G) % 3;
  constexpr int TA = G == 0 ? 0 : G == 1 ? 1 : G == 2 ? 3 : 4;
  constexpr int TB = G == 0 ? 1 : G == 1 ? 2 : G == 2 ? 4 : 5;
  constexpr int X = n >> 2, j = n & 3;
  const float cexp = 0.125f * 1.4426950408889634f;
  const float NINF = -__builtin_inff();
  const int g = c.g, cc = c.cc;
  if (n + 2 < 16) loadw(c, st.W[(WB + n + 2) % 3], n + 2);
  else if (G < 3) loadw(c, st.W[(WB + n + 2) % 3], n + 2 - 16);
  const u32x4(&W)[4] = st.W[(WB + n) % 3];
#pragma unroll
  for (int tt = 0; tt < NT; ++tt) L.acc[n & 1][tt] = f32x4{};
#pragma unroll
  for (int s = 0; s < 4; ++s)
#pragma unroll
    for (int tt = 0; tt < NT; ++tt)
      L.acc[n & 1][tt] = X == 2 ? MFMA(st.E[tt][s], W[s], L.acc[n & 1][tt]) : MFMA(W[s], st.E[tt][s], L.acc[n & 1][tt]);
  if (X == 3) {
#pragma unroll
    for (int tt = 0; tt < NT; ++tt) st.O[T0 + tt][j] = L.acc[n & 1][tt];
  }
  if (n > 0 && n <= 12) {
    constexpr int pX = (n - 1) >> 2, pj = (n - 1) & 3;
#pragma unroll
    for (int tt = 0; tt < NT; ++tt) {
      const f32x4 a = L.acc[(n - 1) & 1][tt];
      const unsigned lo = pack2(a[0], a[1]), hi = pack2(a[2], a[3]);
      if (pX == 0) { st.Qp[T0 + tt][pj >> 1][2 * (pj & 1)] = lo; st.Qp[T0 + tt][pj >> 1][2 * (pj & 1) + 1] = hi; }
      if (pX == 1) { st.Kp[T0 + tt][pj >> 1][2 * (pj & 1)] = lo; st.Kp[T0 + tt][pj >> 1][2 * (pj & 1) + 1] = hi; }
      if (pX == 2) { st.Vp[T0 + tt][pj][0] = lo; st.Vp[T0 + tt][pj][1] = hi; }
    }
  }
  constexpr int nv = (G > 0 && n == 0) || (G == 2 && n == 1) ? 3
                     : ((G & 1) && (n == 5 || n == 8)) ? 2
                     : (n >= 9) ? NVS : 1;
  if (G == 1 || G == 3) {
    constexpr int TO = G == 1 ? 0 : 3;
    if (n == 0) stage_out(c, st.O[TO], 0);
    if (n == 2) { L.ob[0] = *(lds_u32x4_ptr)(c.ord); L.ob[1] = *(lds_u32x4_ptr)(c.ord + 1024); }
    if (n == 4) {
      __builtin_amdgcn_raw_buffer_store_b128(L.ob[0], c.rsrc, c.gvoff, c.obase + TO * 4096, ST_AUX);
      __builtin_amdgcn_raw_buffer_store_b128(L.ob[1], c.rsrc, c.gvoff + 2048, c.obase + TO * 4096, ST_AUX);
    }
  }
  if (G == 2) {
    if (n == 0) stage_out(c, st.O[1], 0);
    if (n == 1) stage_out(c, st.O[2], 2048);
    if (n == 3) {
#pragma unroll
      for (int k = 0; k < 4; ++k) L.ob[k] = *(lds_u32x4_ptr)(c.ord + k * 1024);
    }
    if (n == 5) {
#pragma unroll
      for (int k = 0; k < 4; ++k)
        __builtin_amdgcn_raw_buffer_store_b128(L.ob[k], c.rsrc, c.gvoff + (k & 1) * 2048, c.obase + (1 + (k >> 1)) * 4096, ST_AUX);
    }
  }
  if (G == 1 || G == 3) {
    constexpr int TL = G == 1 ? 3 : 6;
    constexpr int CUT = G == 1 ? 10 : 14;
    if (n == 5) {
      L.QA[0] = sel4(cc >= CUT, st.Qp[TA][0], st.Qp[TL][0]);
      L.QA[1] = sel4(cc >= CUT, st.Qp[TA][1], st.Qp[TL][1]);
    }
    if (n == 8) {
      L.KA[0] = sel4(cc >= CUT, st.Kp[TA][0], st.Kp[TL][0]);
      L.KA[1] = sel4(cc >= CUT, st.Kp[TA][1], st.Kp[TL][1]);
    }
    if (n == 12) {
#pragma unroll
      for (int k = 0; k < 4; ++k) {
        L.VA[k][0] = (G == 1 && g == 3) ? st.Vp[TA][k][0] : st.Vp[TL][k][0];
        L.VA[k][1] = (G == 1 ? g >= 2 : g == 3) ? st.Vp[TA][k][1] : st.Vp[TL][k][1];
      }
    }
  } else {
    if (n == 5) { L.QA[0] = st.Qp[TA][0]; L.QA[1] = st.Qp[TA][1]; }
    if (n == 8) { L.KA[0] = st.Kp[TA][0]; L.KA[1] = st.Kp[TA][1]; }
    if (n == 12) {
#pragma unroll
      for (int k = 0; k < 4; ++k) L.VA[k] = st.Vp[TA][k];
    }
  }
  if (n == 9 || n == 10) {
    constexpr int qs = (n - 9) & 1;
#pragma unroll
    for (int ks = 0; ks < 2; ++ks)
#pragma unroll
      for (int i = 0; i < 2; ++i) {
        float lo = L.S[ks][qs][2 * i], hi = L.S[ks][qs][2 * i + 1];
        bool masked = false, ok = true;
        if (G == 0 && ks == 1) { masked = true; ok = i == 0 ? g < 3 : g < 2; }
        if (G == 1 && ks == 0) { masked = true; ok = i == 0 ? (g == 0 || g == 3) : g != 1; }
        if (G == 2 && ks == 0) { masked = true; ok = g >= 1; }
        if (G == 2 && ks == 1 && i == 1) { masked = true; ok = g < 3; }
        if (G == 3 && ks == 0) { masked = true; ok = i == 0 ? g < 2 : g != 2; }
        if (masked) { lo = ok ? lo : NINF; hi = ok ? hi : NINF; }
        L.sv[qs][4 * ks + 2 * i] = lo;
        L.sv[qs][4 * ks + 2 * i + 1] = hi;
      }
    float m = L.sv[qs][0];
#pragma unroll
    for (int i = 1; i < 8; ++i) m = fmaxf(m, L.sv[qs][i]);
    L.mx[qs] = xmax(m) * cexp;
  }
  if (n == 11 || n == 12) {
    constexpr int qs = (n - 11) & 1;
    float s = 0.f;
#pragma unroll
    for (int i = 0; i < 8; ++i) {
      L.sv[qs][i] = __builtin_amdgcn_exp2f(__builtin_fmaf(L.sv[qs][i], cexp, -L.mx[qs]));
      s += L.sv[qs][i];
    }
    L.sm[qs] = xsum(s);
  }
  if (n == 13 || n == 14) {
    constexpr int qs = (n - 13) & 1;
    float inv = __builtin_amdgcn_rcpf(L.sm[qs]);
    if (G == 0 && qs == 1) inv = cc < 10 ? inv : 0.f;
    if (G == 2 && qs == 0) inv = cc >= 4 ? inv : 0.f;
    if (G == 2 && qs == 1) inv = cc < 14 ? inv : 0.f;
#pragma unroll
    for (int i = 0; i < 4; ++i) L.Pp[qs][i] = pack2(L.sv[qs][2 * i] * inv, L.sv[qs][2 * i + 1] * inv);
  }
  if (n == 2 || n == 3) { PAT(NT, 2, nv * VS); }
  else { PAT(NT, 1, nv * VS); }
  if (n < 9 || n == 15) SBAR();
  if (G == 0 && (n == 1 || n == 5)) { LBAR(); }
  if (n == 8) {
    L.S[0][0] = MFMA(L.KA[0], L.QA[0], f32x4{});
    L.S[1][0] = MFMA(st.Kp[TB][0], L.QA[0], f32x4{});
    L.S[0][1] = MFMA(L.KA[0], st.Qp[TB][0], f32x4{});
    L.S[1][1] = MFMA(st.Kp[TB][0], st.Qp[TB][0], f32x4{});
    L.S[0][0] = MFMA(L.KA[1], L.QA[1], L.S[0][0]);
    L.S[1][0] = MFMA(st.Kp[TB][1], L.QA[1], L.S[1][0]);
    L.S[0][1] = MFMA(L.KA[1], st.Qp[TB][1], L.S[0][1]);
    L.S[1][1] = MFMA(st.Kp[TB][1], st.Qp[TB][1], L.S[1][1]);
    SBAR();
    if (G == 0) { LBAR(); }
  }
  if constexpr (n + 1 < 16) job<G, n + 1>(c, st, L);
}

template <int G>
DI void group_body(const Ctx& c, St& st) {
  constexpr int TA = G == 0 ? 0 : G == 1 ? 1 : G == 2 ? 3 : 4;
  constexpr int TB = G == 0 ? 1 : G == 1 ? 2 : G == 2 ? 4 : 5;
  const int cc = c.cc;
  Loc L;
  job<G, 0>(c, st, L);
  if (G < 3) {
    if (G == 0) { WAITV(0); } else if (G == 1) { WAITV(2); } else { WAITV(4); }
    SBAR();
    read_e<(G < 2 ? 2 : 1)>(c, st.E);
  }
  {
    u32x4 Vf[4];
#pragma unroll
    for (int j = 0; j < 4; ++j) {
      Vf[j][0] = L.VA[j][0]; Vf[j][1] = L.VA[j][1];
      Vf[j][2] = st.Vp[TB][j][0]; Vf[j][3] = st.Vp[TB][j][1];
    }
    const u32x4 z = {};
    if (G == 1 || G == 3) {
      constexpr int TL = G == 1 ? 3 : 6;
      constexpr int CUT = G == 1 ? 10 : 14;
      const u32x4 Ph = sel4(cc >= CUT, L.Pp[0], z);
      const u32x4 Pl = G == 1 ? sel4(cc < 4, L.Pp[0], z) : L.Pp[0];
#pragma unroll
      for (int j = 0; j < 4; ++j) st.O[TB][j] = MFMA(Vf[j], L.Pp[1], st.O[TB][j]);
#pragma unroll
      for (int j = 0; j < 4; ++j) st.O[TA][j] = MFMA(Vf[j], Ph, st.O[TA][j]);
#pragma unroll
      for (int j = 0; j < 4; ++j) st.O[TL][j] = MFMA(Vf[j], Pl, st.O[TL][j]);
    } else {
#pragma unroll
      for (int j = 0; j < 4; ++j) st.O[TA][j] = MFMA(Vf[j], L.Pp[0], st.O[TA][j]);
#pragma unroll
      for (int j = 0; j < 4; ++j) st.O[TB][j] = MFMA(Vf[j], L.Pp[1], st.O[TB][j]);
    }
  }
  SBAR();
  if (G < 2) {
    asm volatile("s_waitcnt lgkmcnt(0)" ::: "memory");
    dma_e<G + 2>(c);
    SBAR();
  }
}

__global__ __launch_bounds__(WAVES * 64, 2) void autoint_kernel(
    const __bf16* __restrict__ e, const __bf16* __restrict__ wq, const __bf16* __restrict__ wk,
    const __bf16* __restrict__ wv, const __bf16* __restrict__ wres, __bf16* __restrict__ out, unsigned long long* prof) {
  extern __shared__ __attribute__((aligned(16))) char smem[];
  const int tid = threadIdx.x;
  const int lane = tid & 63;
  const int wave = __builtin_amdgcn_readfirstlane(tid >> 6);
  const int cc = lane & 15, g = lane >> 4;
  const unsigned lds0 = __builtin_amdgcn_readfirstlane((unsigned)(size_t)(lds_void_ptr)smem);
  const int bid = blockIdx.x;
  const int hd = (bid >> 3) & 1;
  const int grp = (bid & 7) | ((bid >> 4) << 3);
  const int b0 = grp * BPG + wave * EPW;

  Ctx c;
  c.lane = lane;
  c.cc = cc;
  c.g = g;
  c.prof = prof + (size_t)(blockIdx.x * WAVES + wave) * 32;
  STAMP(0);
  c.ebuf = lds0 + 65536 + wave * 12288;
  c.ebase = (const char*)e + (size_t)b0 * EBYTES;
  c.obase = b0 * EBYTES;
  {
    const int c0 = cc ^ g;
#pragma unroll
    for (int j = 0; j < 4; ++j) c.evoff[j] = (unsigned)((g * 256 + (c0 << 4)) ^ (j << 6));
  }
  const bool leader = wave < 4;
  c.leader = leader;
  if (wave == CODEPF) {
    unsigned long long pc;
    asm volatile("s_getpc_b64 %0" : "=s"(pc));
    const unsigned t0 = lane * 128, t1 = t0 + 8192;
    asm volatile(
        "s_mov_b32 m0, %3\n\ts_nop 0\n\t"
        "global_load_lds_dword %0, %2\n\t"
        "global_load_lds_dword %1, %2"
        :
        : "v"(t0), "v"(t1), "s"(pc), "s"(c.ebuf + 8192)
        : "memory", "m0");
  }
  if (leader) {
    dma_e<0>(c);
  } else {
    unsigned o64[4], o128[4];
    const int r8 = lane >> 3;
    const int krow = 32 * (wave - 4) + 8 * (r8 >> 2) + (r8 & 3);
    const int ch = (lane & 7) ^ (r8 & 6);
#pragma unroll
    for (int i = 0; i < 4; ++i) {
      const int k = krow + 16 * (i & 1) + 4 * (i >> 1);
      o64[i] = (unsigned)(k * 128 + ch * 16 + 4096 - i * 1024);
      o128[i] = (unsigned)(k * 256 + ch * 16 + 4096 - i * 1024);
    }
    const char* sq = (const char*)wq + hd * 16384;
    const char* sk = (const char*)wk + hd * 16384;
    const char* sv = (const char*)wv + hd * 16384;
    const char* sr = (const char*)wres + hd * 128;
    const unsigned dst = lds0 + (wave - 4) * 4096;
    glds4(o64[0], o64[1], o64[2], o64[3], sq - 4096, dst);
    glds4(o64[0], o64[1], o64[2], o64[3], sk - 4096, dst + 16384);
    glds4(o64[0], o64[1], o64[2], o64[3], sv - 4096, dst + 32768);
    glds4(o128[0], o128[1], o128[2], o128[3], sr - 4096, dst + 49152);
    dma_e<0>(c);
  }
  SBAR();

  unsigned wb[4];
  {
    const int q4 = (lane & 15) >> 2, p4 = lane & 3;
    const int r8 = (g & 1) * 4 + q4;
#pragma unroll
    for (int j = 0; j < 4; ++j)
      wb[j] = lds0 + (wave & 3) * 4096 + (g >> 1) * 1024 + r8 * 128 + ((((2 * j) ^ (r8 & 6)) + (p4 >> 1)) << 4) + (p4 & 1) * 8;
    c.wl = lds0 + lane * 16;
    asm volatile("" : "+v"(c.wl));
    c.eread = c.ebuf + cc * 256 + ((g ^ cc) << 4);
    const unsigned obuf = c.ebuf + 8192;
    c.owr = obuf + cc * 128 + (((g >> 1) ^ (cc & 7)) << 4) + (g & 1) * 8;
    c.ord = obuf + lane * 16;
    const int orow = lane >> 3, oc = (lane & 7) ^ (orow & 7);
    c.gvoff = orow * 256 + hd * 128 + oc * 16;
    c.rsrc = __builtin_amdgcn_make_buffer_rsrc((void*)out, 0, NB_TOTAL * EBYTES, 0x00020000);
  }

  St st;
  STAMP(1);
  if (leader) {
    __builtin_amdgcn_s_barrier();
  } else {
    const unsigned wlw = c.wl + (wave & 3) * 4096;
    WAITV(20); relayout(wb, wlw, 0);     __builtin_amdgcn_s_barrier(); SBAR();
    WAITV(16); relayout(wb, wlw, 16384); __builtin_amdgcn_s_barrier(); SBAR();
    WAITV(12); relayout(wb, wlw, 32768); __builtin_amdgcn_s_barrier(); SBAR();
    WAITV(8);  relayout(wb, wlw, 49152); __builtin_amdgcn_s_barrier(); SBAR();
    __builtin_amdgcn_s_setprio(1);
  }
  WAITV(0);
  SBAR();
  STAMP(2);
  read_e<2>(c, st.E);
  loadw(c, st.W[0], 0);
  loadw(c, st.W[1], 1);
  asm volatile("s_waitcnt lgkmcnt(0)" ::: "memory");
  SBAR();
  dma_e<1>(c);
  SBAR();
  STAMP(3);

  group_body<0>(c, st);
  STAMP(4);
  group_body<1>(c, st);
  STAMP(5);
  group_body<2>(c, st);
  STAMP(6);
  __builtin_amdgcn_s_setprio(0);
  group_body<3>(c, st);
  STAMP(7);
  {
    stage_out(c, st.O[4], 0);
    stage_out(c, st.O[5], 2048);
    stage_out(c, st.O[6], (unsigned)-8192);
    u32x4 ob[5];
#pragma unroll
    for (int k = 0; k < 4; ++k) ob[k] = *(lds_u32x4_ptr)(c.ord + k * 1024);
    ob[4] = *(lds_u32x4_ptr)(c.ord - 8192);
#pragma unroll
    for (int k = 0; k < 4; ++k)
      __builtin_amdgcn_raw_buffer_store_b128(ob[k], c.rsrc, c.gvoff + (k & 1) * 2048, c.obase + (4 + (k >> 1)) * 4096, ST_AUX);
    __builtin_amdgcn_raw_buffer_store_b128(ob[4], c.rsrc, c.gvoff, c.obase + 6 * 4096, ST_AUX);
  }
  STAMP(8);
}

extern "C" void kernel_launch(void* const* d_in, const int* in_sizes, int n_in, void* d_out, int out_size,
                              void* d_ws, size_t ws_size, hipStream_t stream) {
  const __bf16* e = (const __bf16*)d_in[0];
  const __bf16* wq = (const __bf16*)d_in[1];
  const __bf16* wk = (const __bf16*)d_in[2];
  const __bf16* wv = (const __bf16*)d_in[3];
  const __bf16* wres = (const __bf16*)d_in[4];
  __bf16* out = (__bf16*)d_out;
  (void)hipFuncSetAttribute((const void*)autoint_kernel, hipFuncAttributeMaxDynamicSharedMemorySize, LDS_BYTES);
  autoint_kernel<<<2 * NB_TOTAL / BPG, WAVES * 64, LDS_BYTES, stream>>>(e, wq, wk, wv, wres, out, (unsigned long long*)d_ws);
}
```

```cpp
#define PROF 1
#define DIAGK 0
#define DIAGM 0
#define DIAGS 0
#define CODEPF 3
#include <hip/hip_runtime.h>
#include <hip/hip_bf16.h>

typedef __attribute__((ext_vector_type(8))) __bf16 bf16x8;
typedef __attribute__((ext_vector_type(2))) __bf16 bf16x2;
typedef __attribute__((ext_vector_type(4))) short s16x4;
typedef __attribute__((ext_vector_type(2))) short s16x2;
typedef __attribute__((ext_vector_type(4))) float f32x4;
typedef __attribute__((ext_vector_type(4))) unsigned u32x4;
typedef __attribute__((ext_vector_type(2))) unsigned u32x2;
typedef __attribute__((address_space(3))) s16x4* lds_s16x4_ptr;
typedef __attribute__((address_space(3))) u32x4* lds_u32x4_ptr;
typedef __attribute__((address_space(3))) u32x2* lds_u32x2_ptr;
typedef __attribute__((address_space(3))) void* lds_void_ptr;

#define DI __device__ __forceinline__
#define BF(x) __builtin_bit_cast(bf16x8, (x))
#define MFMA(a, b, c) __builtin_amdgcn_mfma_f32_16x16x32_bf16(BF(a), BF(b), (c), 0, 0, 0)

#ifndef NVS
#define NVS 3
#endif
#define PRIO 3
#define ST_AUX 17

constexpr int NB_TOTAL = 4096;
constexpr int NF = 26;
constexpr int ND = 128;
constexpr int WAVES = 8;
constexpr int EPW = 4;
constexpr int BPG = WAVES * EPW;
constexpr int EBYTES = NF * ND * 2;
constexpr int LDS_BYTES = 65536 + WAVES * 12288;

DI unsigned pack2(float lo, float hi) {
  bf16x2 v;
  v[0] = (__bf16)lo;
  v[1] = (__bf16)hi;
  return __builtin_bit_cast(unsigned, v);
}
DI unsigned relu2(float lo, float hi) {
  unsigned u = pack2(lo, hi);
  asm volatile("" : "+v"(u));
  s16x2 z = {};
  return __builtin_bit_cast(unsigned, __builtin_elementwise_max(__builtin_bit_cast(s16x2, u), z));
}

DI void glds4(unsigned v0, unsigned v1, unsigned v2, unsigned v3, const void* sbase, unsigned lds_dst) {
  asm volatile(
      "s_mov_b32 m0, %5\n\ts_nop 0\n\t"
      "global_load_lds_dwordx4 %0, %4\n\t"
      "global_load_lds_dwordx4 %1, %4 offset:1024\n\t"
      "global_load_lds_dwordx4 %2, %4 offset:2048\n\t"
      "global_load_lds_dwordx4 %3, %4 offset:3072"
      :
      : "v"(v0), "v"(v1), "v"(v2), "v"(v3), "s"(sbase), "s"(lds_dst)
      : "memory", "m0");
}
DI void glds2(unsigned v0, unsigned v1, const void* sbase, unsigned lds_dst) {
  asm volatile(
      "s_mov_b32 m0, %3\n\ts_nop 0\n\t"
      "global_load_lds_dwordx4 %0, %2\n\t"
      "global_load_lds_dwordx4 %1, %2 offset:1024"
      :
      : "v"(v0), "v"(v1), "s"(sbase), "s"(lds_dst)
      : "memory", "m0");
}

#define SGB(mask, n) __builtin_amdgcn_sched_group_barrier(mask, n, 0)
#define SBAR() __builtin_amdgcn_sched_barrier(0)
#define WAITV(n) asm volatile("s_waitcnt vmcnt(" #n ")" ::: "memory");
#define WAITW(n)                  \
  WAITV(n)                        \
  __builtin_amdgcn_s_barrier();   \
  __builtin_amdgcn_sched_barrier(0);

#define STAMP(k) { unsigned long long t0_, t1_; asm volatile("s_memtime %0\n\ts_memrealtime %1\n\ts_waitcnt lgkmcnt(0)" : "=s"(t0_), "=s"(t1_) :: "memory"); \
   __builtin_amdgcn_sched_barrier(0); if (c.lane == 0) { c.prof[2*(k)] = t0_; c.prof[2*(k)+1] = t1_; } __builtin_amdgcn_sched_barrier(0); }
#define LBAR()                                      \
  if (c.leader) __builtin_amdgcn_s_barrier();       \
  __builtin_amdgcn_sched_barrier(0);

struct Ctx {
  bool leader;
  unsigned long long* prof;
  unsigned wl;
  unsigned eread;
  unsigned ebuf;
  unsigned owr;
  unsigned ord;
  unsigned evoff[4];
  int gvoff;
  int lane, cc, g;
  const char* ebase;
  int obase;
  __amdgpu_buffer_rsrc_t rsrc;
};

struct St {
  u32x4 E[2][4];
  u32x4 W[3][4];
  u32x4 Qp[7][2], Kp[7][2];
  u32x2 Vp[7][4];
  f32x4 O[7][4];
};

DI void loadw(const Ctx& c, u32x4 (&W)[4], int n) {
  const int X = n >> 2, j = n & 3;
#pragma unroll
  for (int s = 0; s < 4; ++s) W[s] = *(lds_u32x4_ptr)(c.wl + X * 16384 + s * 4096 + j * 1024);
}
DI void relayout(const unsigned (&wb)[4], unsigned wl, int off) {
  u32x4 W[4];
#pragma unroll
  for (int j = 0; j < 4; ++j) {
    s16x4 lo = __builtin_amdgcn_ds_read_tr16_b64_v4i16((lds_s16x4_ptr)(wb[j] + off));
    s16x4 hi = __builtin_amdgcn_ds_read_tr16_b64_v4i16((lds_s16x4_ptr)(wb[j] + off + 2048));
    W[j] = __builtin_bit_cast(u32x4, __builtin_shufflevector(lo, hi, 0, 1, 2, 3, 4, 5, 6, 7));
  }
  asm volatile("s_waitcnt lgkmcnt(0)" ::: "memory");
#pragma unroll
  for (int j = 0; j < 4; ++j) *(lds_u32x4_ptr)(wl + off + j * 1024) = W[j];
  asm volatile("s_waitcnt lgkmcnt(0)" ::: "memory");
}

template <int G>
DI void dma_e(const Ctx& c) {
  const char* p = c.ebase + (size_t)G * 8192;
  if (G < 3) {
    glds4(c.evoff[0], c.evoff[1], c.evoff[2], c.evoff[3], p, c.ebuf);
    glds4(c.evoff[0], c.evoff[1], c.evoff[2], c.evoff[3], p + 4096, c.ebuf + 4096);
  } else {
    glds2(c.evoff[0], c.evoff[1], p, c.ebuf);
  }
}

template <int NT>
DI void read_e(const Ctx& c, u32x4 (&E)[2][4]) {
#pragma unroll
  for (int s = 0; s < 4; ++s) {
    const unsigned a = c.eread ^ (s << 6);
    E[0][s] = *(lds_u32x4_ptr)(a);
    if (NT > 1) E[1][s] = *(lds_u32x4_ptr)(a + 4096);
  }
}

DI void stage_out(const Ctx& c, const f32x4 (&O)[4], unsigned slot) {
#pragma unroll
  for (int j = 0; j < 4; ++j) {
    u32x2 v;
    v[0] = relu2(O[j][0], O[j][1]);
    v[1] = relu2(O[j][2], O[j][3]);
    *(lds_u32x2_ptr)((c.owr ^ (j << 5)) + slot) = v;
  }
}

DI float xmax(float m) {
  auto a = __builtin_amdgcn_permlane32_swap(__float_as_uint(m), __float_as_uint(m), false, false);
  asm("v_max_f32 %0, %1, %2" : "=v"(m) : "v"(a[0]), "v"(a[1]));
  auto b = __builtin_amdgcn_permlane16_swap(__float_as_uint(m), __float_as_uint(m), false, false);
  asm("v_max_f32 %0, %1, %2" : "=v"(m) : "v"(b[0]), "v"(b[1]));
  return m;
}
DI float xsum(float m) {
  auto a = __builtin_amdgcn_permlane32_swap(__float_as_uint(m), __float_as_uint(m), false, false);
  m = __uint_as_float(a[0]) + __uint_as_float(a[1]);
  auto b = __builtin_amdgcn_permlane16_swap(__float_as_uint(m), __float_as_uint(m), false, false);
  return __uint_as_float(b[0]) + __uint_as_float(b[1]);
}
DI u32x4 sel4(bool p, const u32x4& a, const u32x4& b) {
  u32x4 r;
#pragma unroll
  for (int i = 0; i < 4; ++i) r[i] = p ? a[i] : b[i];
  return r;
}

#define PAT(NT_, ND_, NV)                          \
  _Pragma("unroll") for (int q_ = 0; q_ < 4; ++q_) { \
    SGB(0x8, 1);                                   \
    SGB(0x100, ND_);                               \
    SGB(0x2, NV);                                  \
    if ((NT_) > 1) { SGB(0x8, 1); SGB(0x2, NV); }  \
    if ((NT_) > 2) { SGB(0x8, 1); SGB(0x2, NV); }  \
  }

struct Loc {
  f32x4 acc[2][2];
  f32x4 S[2][2];
  float sv[2][8], mx[2], sm[2];
  u32x4 Pp[2];
  u32x4 ob[4];
  u32x4 QA[2], KA[2];
  u32x2 VA[4];
};

template <int G, int n>
DI void job(const Ctx& c, St& st, Loc& L) {
  constexpr int NT = G < 3 ? 2 : 1;
  constexpr int T0 = 2 * G;
  constexpr int NM = 4 * NT;
  constexpr int NDS = 8 / NM;
  constexpr int VS = 2 / NT;
  constexpr int WB = (16 * G) % 3;
  constexpr int TA = G == 0 ? 0 : G == 1 ? 1 : G == 2 ? 3 : 4;
  constexpr int TB = G == 0 ? 1 : G == 1 ? 2 : G == 2 ? 4 : 5;
  constexpr int X = n >> 2, j = n & 3;
  const float cexp = 0.125f * 1.4426950408889634f;
  const float NINF = -__builtin_inff();
  const int g = c.g, cc = c.cc;
  if (n + 2 < 16) loadw(c, st.W[(WB + n + 2) % 3], n + 2);
  else if (G < 3) loadw(c, st.W[(WB + n + 2) % 3], n + 2 - 16);
  const u32x4(&W)[4] = st.W[(WB + n) % 3];
#pragma unroll
  for (int tt = 0; tt < NT; ++tt) L.acc[n & 1][tt] = f32x4{};
#pragma unroll
  for (int s = 0; s < 4; ++s)
#pragma unroll
    for (int tt = 0; tt < NT; ++tt)
      L.acc[n & 1][tt] = X == 2 ? MFMA(st.E[tt][s], W[s], L.acc[n & 1][tt]) : MFMA(W[s], st.E[tt][s], L.acc[n & 1][tt]);
  if (X == 3) {
#pragma unroll
    for (int tt = 0; tt < NT; ++tt) st.O[T0 + tt][j] = L.acc[n & 1][tt];
  }
  if (n > 0 && n <= 12) {
    constexpr int pX = (n - 1) >> 2, pj = (n - 1) & 3;
#pragma unroll
    for (int tt = 0; tt < NT; ++tt) {
      const f32x4 a = L.acc[(n - 1) & 1][tt];
      const unsigned lo = pack2(a[0], a[1]), hi = pack2(a[2], a[3]);
      if (pX == 0) { st.Qp[T0 + tt][pj >> 1][2 * (pj & 1)] = lo; st.Qp[T0 + tt][pj >> 1][2 * (pj & 1) + 1] = hi; }
      if (pX == 1) { st.Kp[T0 + tt][pj >> 1][2 * (pj & 1)] = lo; st.Kp[T0 + tt][pj >> 1][2 * (pj & 1) + 1] = hi; }
      if (pX == 2) { st.Vp[T0 + tt][pj][0] = lo; st.Vp[T0 + tt][pj][1] = hi; }
    }
  }
  constexpr int nv = (G > 0 && n == 0) || (G == 2 && n == 1) ? 3
                     : ((G & 1) && (n == 5 || n == 8)) ? 2
                     : (n >= 9) ? NVS : 1;
  if (G == 1 || G == 3) {
    constexpr int TO = G == 1 ? 0 : 3;
    if (n == 0) stage_out(c, st.O[TO], 0);
    if (n == 2) { L.ob[0] = *(lds_u32x4_ptr)(c.ord); L.ob[1] = *(lds_u32x4_ptr)(c.ord + 1024); }
    if (n == 4) {
      __builtin_amdgcn_raw_buffer_store_b128(L.ob[0], c.rsrc, c.gvoff, c.obase + TO * 4096, ST_AUX);
      __builtin_amdgcn_raw_buffer_store_b128(L.ob[1], c.rsrc, c.gvoff + 2048, c.obase + TO * 4096, ST_AUX);
    }
  }
  if (G == 2) {
    if (n == 0) stage_out(c, st.O[1], 0);
    if (n == 1) stage_out(c, st.O[2], 2048);
    if (n == 3) {
#pragma unroll
      for (int k = 0; k < 4; ++k) L.ob[k] = *(lds_u32x4_ptr)(c.ord + k * 1024);
    }
    if (n == 5) {
#pragma unroll
      for (int k = 0; k < 4; ++k)
        __builtin_amdgcn_raw_buffer_store_b128(L.ob[k], c.rsrc, c.gvoff + (k & 1) * 2048, c.obase + (1 + (k >> 1)) * 4096, ST_AUX);
    }
  }
  if (G == 1 || G == 3) {
    constexpr int TL = G == 1 ? 3 : 6;
    constexpr int CUT = G == 1 ? 10 : 14;
    if (n == 5) {
      L.QA[0] = sel4(cc >= CUT, st.Qp[TA][0], st.Qp[TL][0]);
      L.QA[1] = sel4(cc >= CUT, st.Qp[TA][1], st.Qp[TL][1]);
    }
    if (n == 8) {
      L.KA[0] = sel4(cc >= CUT, st.Kp[TA][0], st.Kp[TL][0]);
      L.KA[1] = sel4(cc >= CUT, st.Kp[TA][1], st.Kp[TL][1]);
    }
    if (n == 12) {
#pragma unroll
      for (int k = 0; k < 4; ++k) {
        L.VA[k][0] = (G == 1 && g == 3) ? st.Vp[TA][k][0] : st.Vp[TL][k][0];
        L.VA[k][1] = (G == 1 ? g >= 2 : g == 3) ? st.Vp[TA][k][1] : st.Vp[TL][k][1];
      }
    }
  } else {
    if (n == 5) { L.QA[0] = st.Qp[TA][0]; L.QA[1] = st.Qp[TA][1]; }
    if (n == 8) { L.KA[0] = st.Kp[TA][0]; L.KA[1] = st.Kp[TA][1]; }
    if (n == 12) {
#pragma unroll
      for (int k = 0; k < 4; ++k) L.VA[k] = st.Vp[TA][k];
    }
  }
  if (n == 9 || n == 10) {
    constexpr int qs = (n - 9) & 1;
#pragma unroll
    for (int ks = 0; ks < 2; ++ks)
#pragma unroll
      for (int i = 0; i < 2; ++i) {
        float lo = L.S[ks][qs][2 * i], hi = L.S[ks][qs][2 * i + 1];
        bool masked = false, ok = true;
        if (G == 0 && ks == 1) { masked = true; ok = i == 0 ? g < 3 : g < 2; }
        if (G == 1 && ks == 0) { masked = true; ok = i == 0 ? (g == 0 || g == 3) : g != 1; }
        if (G == 2 && ks == 0) { masked = true; ok = g >= 1; }
        if (G == 2 && ks == 1 && i == 1) { masked = true; ok = g < 3; }
        if (G == 3 && ks == 0) { masked = true; ok = i == 0 ? g < 2 : g != 2; }
        if (masked) { lo = ok ? lo : NINF; hi = ok ? hi : NINF; }
        L.sv[qs][4 * ks + 2 * i] = lo;
        L.sv[qs][4 * ks + 2 * i + 1] = hi;
      }
    float m = L.sv[qs][0];
#pragma unroll
    for (int i = 1; i < 8; ++i) m = fmaxf(m, L.sv[qs][i]);
    L.mx[qs] = xmax(m) * cexp;
  }
  if (n == 11 || n == 12) {
    constexpr int qs = (n - 11) & 1;
    float s = 0.f;
#pragma unroll
    for (int i = 0; i < 8; ++i) {
      L.sv[qs][i] = __builtin_amdgcn_exp2f(__builtin_fmaf(L.sv[qs][i], cexp, -L.mx[qs]));
      s += L.sv[qs][i];
    }
    L.sm[qs] = xsum(s);
  }
  if (n == 13 || n == 14) {
    constexpr int qs = (n - 13) & 1;
    float inv = __builtin_amdgcn_rcpf(L.sm[qs]);
    if (G == 0 && qs == 1) inv = cc < 10 ? inv : 0.f;
    if (G == 2 && qs == 0) inv = cc >= 4 ? inv : 0.f;
    if (G == 2 && qs == 1) inv = cc < 14 ? inv : 0.f;
#pragma unroll
    for (int i = 0; i < 4; ++i) L.Pp[qs][i] = pack2(L.sv[qs][2 * i] * inv, L.sv[qs][2 * i + 1] * inv);
  }
  if (n == 2 || n == 3) { PAT(NT, 2, nv * VS); }
  else { PAT(NT, 1, nv * VS); }
  if (n < 9 || n == 15) SBAR();
  if (G == 0 && (n == 1 || n == 5)) { LBAR(); }
  if (n == 8) {
    L.S[0][0] = MFMA(L.KA[0], L.QA[0], f32x4{});
    L.S[1][0] = MFMA(st.Kp[TB][0], L.QA[0], f32x4{});
    L.S[0][1] = MFMA(L.KA[0], st.Qp[TB][0], f32x4{});
    L.S[1][1] = MFMA(st.Kp[TB][0], st.Qp[TB][0], f32x4{});
    L.S[0][0] = MFMA(L.KA[1], L.QA[1], L.S[0][0]);
    L.S[1][0] = MFMA(st.Kp[TB][1], L.QA[1], L.S[1][0]);
    L.S[0][1] = MFMA(L.KA[1], st.Qp[TB][1], L.S[0][1]);
    L.S[1][1] = MFMA(st.Kp[TB][1], st.Qp[TB][1], L.S[1][1]);
    SBAR();
    if (G == 0) { LBAR(); }
  }
  if constexpr (n + 1 < 16) job<G, n + 1>(c, st, L);
}

template <int G>
DI void group_body(const Ctx& c, St& st) {
  constexpr int TA = G == 0 ? 0 : G == 1 ? 1 : G == 2 ? 3 : 4;
  constexpr int TB = G == 0 ? 1 : G == 1 ? 2 : G == 2 ? 4 : 5;
  const int cc = c.cc;
  Loc L;
  job<G, 0>(c, st, L);
  if (G < 3) {
    if (G == 0) { WAITV(0); } else if (G == 1) { WAITV(2); } else { WAITV(4); }
    SBAR();
    read_e<(G < 2 ? 2 : 1)>(c, st.E);
  }
  {
    u32x4 Vf[4];
#pragma unroll
    for (int j = 0; j < 4; ++j) {
      Vf[j][0] = L.VA[j][0]; Vf[j][1] = L.VA[j][1];
      Vf[j][2] = st.Vp[TB][j][0]; Vf[j][3] = st.Vp[TB][j][1];
    }
    const u32x4 z = {};
    if (G == 1 || G == 3) {
      constexpr int TL = G == 1 ? 3 : 6;
      constexpr int CUT = G == 1 ? 10 : 14;
      const u32x4 Ph = sel4(cc >= CUT, L.Pp[0], z);
      const u32x4 Pl = G == 1 ? sel4(cc < 4, L.Pp[0], z) : L.Pp[0];
#pragma unroll
      for (int j = 0; j < 4; ++j) st.O[TB][j] = MFMA(Vf[j], L.Pp[1], st.O[TB][j]);
#pragma unroll
      for (int j = 0; j < 4; ++j) st.O[TA][j] = MFMA(Vf[j], Ph, st.O[TA][j]);
#pragma unroll
      for (int j = 0; j < 4; ++j) st.O[TL][j] = MFMA(Vf[j], Pl, st.O[TL][j]);
    } else {
#pragma unroll
      for (int j = 0; j < 4; ++j) st.O[TA][j] = MFMA(Vf[j], L.Pp[0], st.O[TA][j]);
#pragma unroll
      for (int j = 0; j < 4; ++j) st.O[TB][j] = MFMA(Vf[j], L.Pp[1], st.O[TB][j]);
    }
  }
  SBAR();
  if (G < 2) {
    asm volatile("s_waitcnt lgkmcnt(0)" ::: "memory");
    dma_e<G + 2>(c);
    SBAR();
  }
}

__global__ __launch_bounds__(WAVES * 64, 2) void autoint_kernel(
    const __bf16* __restrict__ e, const __bf16* __restrict__ wq, const __bf16* __restrict__ wk,
    const __bf16* __restrict__ wv, const __bf16* __restrict__ wres, __bf16* __restrict__ out, unsigned long long* prof) {
  extern __shared__ __attribute__((aligned(16))) char smem[];
  const int tid = threadIdx.x;
  const int lane = tid & 63;
  const int wave = __builtin_amdgcn_readfirstlane(tid >> 6);
  const int cc = lane & 15, g = lane >> 4;
  const unsigned lds0 = __builtin_amdgcn_readfirstlane((unsigned)(size_t)(lds_void_ptr)smem);
  const int bid = blockIdx.x;
  const int hd = (bid >> 3) & 1;
  const int grp = (bid & 7) | ((bid >> 4) << 3);
  const int b0 = grp * BPG + wave * EPW;

  Ctx c;
  c.lane = lane;
  c.cc = cc;
  c.g = g;
  c.prof = prof + (size_t)(blockIdx.x * WAVES + wave) * 32;
  STAMP(0);
  c.ebuf = lds0 + 65536 + wave * 12288;
  c.ebase = (const char*)e + (size_t)b0 * EBYTES;
  c.obase = b0 * EBYTES;
  {
    const int c0 = cc ^ g;
#pragma unroll
    for (int j = 0; j < 4; ++j) c.evoff[j] = (unsigned)((g * 256 + (c0 << 4)) ^ (j << 6));
  }
  const bool leader = wave < 4;
  c.leader = leader;
  if (wave == CODEPF) {
    unsigned long long pc;
    asm volatile("s_getpc_b64 %0" : "=s"(pc));
    const unsigned t0 = lane * 128, t1 = t0 + 8192;
    asm volatile(
        "s_mov_b32 m0, %3\n\ts_nop 0\n\t"
        "global_load_lds_dword %0, %2\n\t"
        "global_load_lds_dword %1, %2"
        :
        : "v"(t0), "v"(t1), "s"(pc), "s"(c.ebuf + 8192)
        : "memory", "m0");
  }
  if (leader) {
    dma_e<0>(c);
  } else {
    unsigned o64[4], o128[4];
    const int r8 = lane >> 3;
    const int krow = 32 * (wave - 4) + 8 * (r8 >> 2) + (r8 & 3);
    const int ch = (lane & 7) ^ (r8 & 6);
#pragma unroll
    for (int i = 0; i < 4; ++i) {
      const int k = krow + 16 * (i & 1) + 4 * (i >> 1);
      o64[i] = (unsigned)(k * 128 + ch * 16 + 4096 - i * 1024);
      o128[i] = (unsigned)(k * 256 + ch * 16 + 4096 - i * 1024);
    }
    const char* sq = (const char*)wq + hd * 16384;
    const char* sk = (const char*)wk + hd * 16384;
    const char* sv = (const char*)wv + hd * 16384;
    const char* sr = (const char*)wres + hd * 128;
    const unsigned dst = lds0 + (wave - 4) * 4096;
    glds4(o64[0], o64[1], o64[2], o64[3], sq - 4096, dst);
    glds4(o64[0], o64[1], o64[2], o64[3], sk - 4096, dst + 16384);
    glds4(o64[0], o64[1], o64[2], o64[3], sv - 4096, dst + 32768);
    glds4(o128[0], o128[1], o128[2], o128[3], sr - 4096, dst + 49152);
    dma_e<0>(c);
  }
  SBAR();

  unsigned wb[4];
  {
    const int q4 = (lane & 15) >> 2, p4 = lane & 3;
    const int r8 = (g & 1) * 4 + q4;
#pragma unroll
    for (int j = 0; j < 4; ++j)
      wb[j] = lds0 + (wave & 3) * 4096 + (g >> 1) * 1024 + r8 * 128 + ((((2 * j) ^ (r8 & 6)) + (p4 >> 1)) << 4) + (p4 & 1) * 8;
    c.wl = lds0 + lane * 16;
    asm volatile("" : "+v"(c.wl));
    c.eread = c.ebuf + cc * 256 + ((g ^ cc) << 4);
    const unsigned obuf = c.ebuf + 8192;
    c.owr = obuf + cc * 128 + (((g >> 1) ^ (cc & 7)) << 4) + (g & 1) * 8;
    c.ord = obuf + lane * 16;
    const int orow = lane >> 3, oc = (lane & 7) ^ (orow & 7);
    c.gvoff = orow * 256 + hd * 128 + oc * 16;
    c.rsrc = __builtin_amdgcn_make_buffer_rsrc((void*)out, 0, NB_TOTAL * EBYTES, 0x00020000);
  }

  St st;
  STAMP(1);
  if (leader) {
    __builtin_amdgcn_s_barrier();
  } else {
    const unsigned wlw = c.wl + (wave & 3) * 4096;
    WAITV(20); relayout(wb, wlw, 0);     __builtin_amdgcn_s_barrier(); SBAR();
    WAITV(16); relayout(wb, wlw, 16384); __builtin_amdgcn_s_barrier(); SBAR();
    WAITV(12); relayout(wb, wlw, 32768); __builtin_amdgcn_s_barrier(); SBAR();
    WAITV(8);  relayout(wb, wlw, 49152); __builtin_amdgcn_s_barrier(); SBAR();
    __builtin_amdgcn_s_setprio(1);
  }
  WAITV(0);
  SBAR();
  STAMP(2);
  read_e<2>(c, st.E);
  loadw(c, st.W[0], 0);
  loadw(c, st.W[1], 1);
  asm volatile("s_waitcnt lgkmcnt(0)" ::: "memory");
  SBAR();
  dma_e<1>(c);
  SBAR();
  STAMP(3);

  group_body<0>(c, st);
  STAMP(4);
  group_body<1>(c, st);
  STAMP(5);
  group_body<2>(c, st);
  STAMP(6);
  __builtin_amdgcn_s_setprio(0);
  group_body<3>(c, st);
  STAMP(7);
  {
    stage_out(c, st.O[4], 0);
    stage_out(c, st.O[5], 2048);
    stage_out(c, st.O[6], (unsigned)-8192);
    u32x4 ob[5];
#pragma unroll
    for (int k = 0; k < 4; ++k) ob[k] = *(lds_u32x4_ptr)(c.ord + k * 1024);
    ob[4] = *(lds_u32x4_ptr)(c.ord - 8192);
#pragma unroll
    for (int k = 0; k < 4; ++k)
      __builtin_amdgcn_raw_buffer_store_b128(ob[k], c.rsrc, c.gvoff + (k & 1) * 2048, c.obase + (4 + (k >> 1)) * 4096, ST_AUX);
    __builtin_amdgcn_raw_buffer_store_b128(ob[4], c.rsrc, c.gvoff, c.obase + 6 * 4096, ST_AUX);
  }
  asm volatile("s_waitcnt vmcnt(0)" ::: "memory");
  STAMP(8);
}

__global__ void diag_kernel(const unsigned long long* prof) {
  __shared__ unsigned long long smin[256];
  __shared__ double sacc[256];
  const int t = threadIdx.x;
  unsigned long long mn = ~0ull;
  for (int i = t; i < 2048; i += 256) { unsigned long long a = prof[(size_t)i * 32 + 1]; mn = a < mn ? a : mn; }
  smin[t] = mn;
  __syncthreads();
  for (int s = 128; s > 0; s >>= 1) { if (t < s) smin[t] = smin[t] < smin[t + s] ? smin[t] : smin[t + s]; __syncthreads(); }
  mn = smin[0];
  double acc = DIAGM == 2 ? 1e30 : 0;
  for (int i = t; i < 2048; i += 256) {
    double v = (double)(prof[(size_t)i * 32 + 2 * DIAGK + 1] - mn);
    if (DIAGM == 3) v = (double)(prof[(size_t)i * 32 + 2 * DIAGK] - prof[(size_t)i * 32]) * 0.05;
    if (DIAGM == 0 || DIAGM == 3) acc += v; else if (DIAGM == 1) acc = v > acc ? v : acc; else acc = v < acc ? v : acc;
  }
  sacc[t] = acc;
  __syncthreads();
  for (int s = 128; s > 0; s >>= 1) {
    if (t < s) { double a = sacc[t], b = sacc[t + s]; sacc[t] = (DIAGM == 0 || DIAGM == 3) ? a + b : DIAGM == 1 ? (a > b ? a : b) : (a < b ? a : b); }
    __syncthreads();
  }
  double v = sacc[0];
  if (DIAGM == 0 || DIAGM == 3) v /= 2048;
  if (v < 0) v = 0; if (v > 20000) v = 20000;
  unsigned long long dur = (unsigned long long)(v * DIAGS);
  if (t == 0) {
    unsigned long long s0, s1;
    asm volatile("s_memrealtime %0\n\ts_waitcnt lgkmcnt(0)" : "=s"(s0) :: "memory");
    for (int it = 0; it < 1000000; ++it) {
      asm volatile("s_memrealtime %0\n\ts_waitcnt lgkmcnt(0)" : "=s"(s1) :: "memory");
      if (s1 - s0 >= dur) break;
    }
  }
}

extern "C" void kernel_launch(void* const* d_in, const int* in_sizes, int n_in, void* d_out, int out_size,
                              void* d_ws, size_t ws_size, hipStream_t stream) {
  const __bf16* e = (const __bf16*)d_in[0];
  const __bf16* wq = (const __bf16*)d_in[1];
  const __bf16* wk = (const __bf16*)d_in[2];
  const __bf16* wv = (const __bf16*)d_in[3];
  const __bf16* wres = (const __bf16*)d_in[4];
  __bf16* out = (__bf16*)d_out;
  (void)hipFuncSetAttribute((const void*)autoint_kernel, hipFuncAttributeMaxDynamicSharedMemorySize, LDS_BYTES);
  autoint_kernel<<<2 * NB_TOTAL / BPG, WAVES * 64, LDS_BYTES, stream>>>(e, wq, wk, wv, wres, out, (unsigned long long*)d_ws);
  diag_kernel<<<1, 256, 0, stream>>>((const unsigned long long*)d_ws);
}
```

```cpp
#define XMODE 1
#define XAUX 17
#define XDST 0
#define XPAT 0
#define DIAGK 1
#define DIAGM 1
#define DIAGS 0
#define CODEPF 3
#include <hip/hip_runtime.h>
#include <hip/hip_bf16.h>

typedef __attribute__((ext_vector_type(8))) __bf16 bf16x8;
typedef __attribute__((ext_vector_type(2))) __bf16 bf16x2;
typedef __attribute__((ext_vector_type(4))) short s16x4;
typedef __attribute__((ext_vector_type(2))) short s16x2;
typedef __attribute__((ext_vector_type(4))) float f32x4;
typedef __attribute__((ext_vector_type(4))) unsigned u32x4;
typedef __attribute__((ext_vector_type(2))) unsigned u32x2;
typedef __attribute__((address_space(3))) s16x4* lds_s16x4_ptr;
typedef __attribute__((address_space(3))) u32x4* lds_u32x4_ptr;
typedef __attribute__((address_space(3))) u32x2* lds_u32x2_ptr;
typedef __attribute__((address_space(3))) void* lds_void_ptr;

#define DI __device__ __forceinline__
#define BF(x) __builtin_bit_cast(bf16x8, (x))
#define MFMA(a, b, c) __builtin_amdgcn_mfma_f32_16x16x32_bf16(BF(a), BF(b), (c), 0, 0, 0)

#ifndef NVS
#define NVS 3
#endif
#define PRIO 3
#define ST_AUX 17

constexpr int NB_TOTAL = 4096;
constexpr int NF = 26;
constexpr int ND = 128;
constexpr int WAVES = 8;
constexpr int EPW = 4;
constexpr int BPG = WAVES * EPW;
constexpr int EBYTES = NF * ND * 2;
constexpr int LDS_BYTES = 65536 + WAVES * 12288;

DI unsigned pack2(float lo, float hi) {
  bf16x2 v;
  v[0] = (__bf16)lo;
  v[1] = (__bf16)hi;
  return __builtin_bit_cast(unsigned, v);
}
DI unsigned relu2(float lo, float hi) {
  unsigned u = pack2(lo, hi);
  asm volatile("" : "+v"(u));
  s16x2 z = {};
  return __builtin_bit_cast(unsigned, __builtin_elementwise_max(__builtin_bit_cast(s16x2, u), z));
}

DI void glds4(unsigned v0, unsigned v1, unsigned v2, unsigned v3, const void* sbase, unsigned lds_dst) {
  asm volatile(
      "s_mov_b32 m0, %5\n\ts_nop 0\n\t"
      "global_load_lds_dwordx4 %0, %4\n\t"
      "global_load_lds_dwordx4 %1, %4 offset:1024\n\t"
      "global_load_lds_dwordx4 %2, %4 offset:2048\n\t"
      "global_load_lds_dwordx4 %3, %4 offset:3072"
      :
      : "v"(v0), "v"(v1), "v"(v2), "v"(v3), "s"(sbase), "s"(lds_dst)
      : "memory", "m0");
}
DI void glds2(unsigned v0, unsigned v1, const void* sbase, unsigned lds_dst) {
  asm volatile(
      "s_mov_b32 m0, %3\n\ts_nop 0\n\t"
      "global_load_lds_dwordx4 %0, %2\n\t"
      "global_load_lds_dwordx4 %1, %2 offset:1024"
      :
      : "v"(v0), "v"(v1), "s"(sbase), "s"(lds_dst)
      : "memory", "m0");
}

#define SGB(mask, n) __builtin_amdgcn_sched_group_barrier(mask, n, 0)
#define SBAR() __builtin_amdgcn_sched_barrier(0)
#define WAITV(n) asm volatile("s_waitcnt vmcnt(" #n ")" ::: "memory");
#define WAITW(n)                  \
  WAITV(n)                        \
  __builtin_amdgcn_s_barrier();   \
  __builtin_amdgcn_sched_barrier(0);

#define STAMP(k)
#define LBAR()                                      \
  if (c.leader) __builtin_amdgcn_s_barrier();       \
  __builtin_amdgcn_sched_barrier(0);

struct Ctx {
  bool leader;
  unsigned long long* prof;
  unsigned wl;
  unsigned eread;
  unsigned ebuf;
  unsigned owr;
  unsigned ord;
  unsigned evoff[4];
  int gvoff;
  int lane, cc, g;
  const char* ebase;
  int obase;
  __amdgpu_buffer_rsrc_t rsrc;
};

struct St {
  u32x4 E[2][4];
  u32x4 W[3][4];
  u32x4 Qp[7][2], Kp[7][2];
  u32x2 Vp[7][4];
  f32x4 O[7][4];
};

DI void loadw(const Ctx& c, u32x4 (&W)[4], int n) {
  const int X = n >> 2, j = n & 3;
#pragma unroll
  for (int s = 0; s < 4; ++s) W[s] = *(lds_u32x4_ptr)(c.wl + X * 16384 + s * 4096 + j * 1024);
}
DI void relayout(const unsigned (&wb)[4], unsigned wl, int off) {
  u32x4 W[4];
#pragma unroll
  for (int j = 0; j < 4; ++j) {
    s16x4 lo = __builtin_amdgcn_ds_read_tr16_b64_v4i16((lds_s16x4_ptr)(wb[j] + off));
    s16x4 hi = __builtin_amdgcn_ds_read_tr16_b64_v4i16((lds_s16x4_ptr)(wb[j] + off + 2048));
    W[j] = __builtin_bit_cast(u32x4, __builtin_shufflevector(lo, hi, 0, 1, 2, 3, 4, 5, 6, 7));
  }
  asm volatile("s_waitcnt lgkmcnt(0)" ::: "memory");
#pragma unroll
  for (int j = 0; j < 4; ++j) *(lds_u32x4_ptr)(wl + off + j * 1024) = W[j];
  asm volatile("s_waitcnt lgkmcnt(0)" ::: "memory");
}

template <int G>
DI void dma_e(const Ctx& c) {
  const char* p = c.ebase + (size_t)G * 8192;
  if (G < 3) {
    glds4(c.evoff[0], c.evoff[1], c.evoff[2], c.evoff[3], p, c.ebuf);
    glds4(c.evoff[0], c.evoff[1], c.evoff[2], c.evoff[3], p + 4096, c.ebuf + 4096);
  } else {
    glds2(c.evoff[0], c.evoff[1], p, c.ebuf);
  }
}

template <int NT>
DI void read_e(const Ctx& c, u32x4 (&E)[2][4]) {
#pragma unroll
  for (int s = 0; s < 4; ++s) {
    const unsigned a = c.eread ^ (s << 6);
    E[0][s] = *(lds_u32x4_ptr)(a);
    if (NT > 1) E[1][s] = *(lds_u32x4_ptr)(a + 4096);
  }
}

DI void stage_out(const Ctx& c, const f32x4 (&O)[4], unsigned slot) {
#pragma unroll
  for (int j = 0; j < 4; ++j) {
    u32x2 v;
    v[0] = relu2(O[j][0], O[j][1]);
    v[1] = relu2(O[j][2], O[j][3]);
    *(lds_u32x2_ptr)((c.owr ^ (j << 5)) + slot) = v;
  }
}

DI float xmax(float m) {
  auto a = __builtin_amdgcn_permlane32_swap(__float_as_uint(m), __float_as_uint(m), false, false);
  asm("v_max_f32 %0, %1, %2" : "=v"(m) : "v"(a[0]), "v"(a[1]));
  auto b = __builtin_amdgcn_permlane16_swap(__float_as_uint(m), __float_as_uint(m), false, false);
  asm("v_max_f32 %0, %1, %2" : "=v"(m) : "v"(b[0]), "v"(b[1]));
  return m;
}
DI float xsum(float m) {
  auto a = __builtin_amdgcn_permlane32_swap(__float_as_uint(m), __float_as_uint(m), false, false);
  m = __uint_as_float(a[0]) + __uint_as_float(a[1]);
  auto b = __builtin_amdgcn_permlane16_swap(__float_as_uint(m), __float_as_uint(m), false, false);
  return __uint_as_float(b[0]) + __uint_as_float(b[1]);
}
DI u32x4 sel4(bool p, const u32x4& a, const u32x4& b) {
  u32x4 r;
#pragma unroll
  for (int i = 0; i < 4; ++i) r[i] = p ? a[i] : b[i];
  return r;
}

#define PAT(NT_, ND_, NV)                          \
  _Pragma("unroll") for (int q_ = 0; q_ < 4; ++q_) { \
    SGB(0x8, 1);                                   \
    SGB(0x100, ND_);                               \
    SGB(0x2, NV);                                  \
    if ((NT_) > 1) { SGB(0x8, 1); SGB(0x2, NV); }  \
    if ((NT_) > 2) { SGB(0x8, 1); SGB(0x2, NV); }  \
  }

struct Loc {
  f32x4 acc[2][2];
  f32x4 S[2][2];
  float sv[2][8], mx[2], sm[2];
  u32x4 Pp[2];
  u32x4 ob[4];
  u32x4 QA[2], KA[2];
  u32x2 VA[4];
};

template <int G, int n>
DI void job(const Ctx& c, St& st, Loc& L) {
  constexpr int NT = G < 3 ? 2 : 1;
  constexpr int T0 = 2 * G;
  constexpr int NM = 4 * NT;
  constexpr int NDS = 8 / NM;
  constexpr int VS = 2 / NT;
  constexpr int WB = (16 * G) % 3;
  constexpr int TA = G == 0 ? 0 : G == 1 ? 1 : G == 2 ? 3 : 4;
  constexpr int TB = G == 0 ? 1 : G == 1 ? 2 : G == 2 ? 4 : 5;
  constexpr int X = n >> 2, j = n & 3;
  const float cexp = 0.125f * 1.4426950408889634f;
  const float NINF = -__builtin_inff();
  const int g = c.g, cc = c.cc;
  if (n + 2 < 16) loadw(c, st.W[(WB + n + 2) % 3], n + 2);
  else if (G < 3) loadw(c, st.W[(WB + n + 2) % 3], n + 2 - 16);
  const u32x4(&W)[4] = st.W[(WB + n) % 3];
#pragma unroll
  for (int tt = 0; tt < NT; ++tt) L.acc[n & 1][tt] = f32x4{};
#pragma unroll
  for (int s = 0; s < 4; ++s)
#pragma unroll
    for (int tt = 0; tt < NT; ++tt)
      L.acc[n & 1][tt] = X == 2 ? MFMA(st.E[tt][s], W[s], L.acc[n & 1][tt]) : MFMA(W[s], st.E[tt][s], L.acc[n & 1][tt]);
  if (X == 3) {
#pragma unroll
    for (int tt = 0; tt < NT; ++tt) st.O[T0 + tt][j] = L.acc[n & 1][tt];
  }
  if (n > 0 && n <= 12) {
    constexpr int pX = (n - 1) >> 2, pj = (n - 1) & 3;
#pragma unroll
    for (int tt = 0; tt < NT; ++tt) {
      const f32x4 a = L.acc[(n - 1) & 1][tt];
      const unsigned lo = pack2(a[0], a[1]), hi = pack2(a[2], a[3]);
      if (pX == 0) { st.Qp[T0 + tt][pj >> 1][2 * (pj & 1)] = lo; st.Qp[T0 + tt][pj >> 1][2 * (pj & 1) + 1] = hi; }
      if (pX == 1) { st.Kp[T0 + tt][pj >> 1][2 * (pj & 1)] = lo; st.Kp[T0 + tt][pj >> 1][2 * (pj & 1) + 1] = hi; }
      if (pX == 2) { st.Vp[T0 + tt][pj][0] = lo; st.Vp[T0 + tt][pj][1] = hi; }
    }
  }
  constexpr int nv = (G > 0 && n == 0) || (G == 2 && n == 1) ? 3
                     : ((G & 1) && (n == 5 || n == 8)) ? 2
                     : (n >= 9) ? NVS : 1;
  if (G == 1 || G == 3) {
    constexpr int TO = G == 1 ? 0 : 3;
    if (n == 0) stage_out(c, st.O[TO], 0);
    if (n == 2) { L.ob[0] = *(lds_u32x4_ptr)(c.ord); L.ob[1] = *(lds_u32x4_ptr)(c.ord + 1024); }
    if (n == 4) {
      __builtin_amdgcn_raw_buffer_store_b128(L.ob[0], c.rsrc, c.gvoff, c.obase + TO * 4096, ST_AUX);
      __builtin_amdgcn_raw_buffer_store_b128(L.ob[1], c.rsrc, c.gvoff + 2048, c.obase + TO * 4096, ST_AUX);
    }
  }
  if (G == 2) {
    if (n == 0) stage_out(c, st.O[1], 0);
    if (n == 1) stage_out(c, st.O[2], 2048);
    if (n == 3) {
#pragma unroll
      for (int k = 0; k < 4; ++k) L.ob[k] = *(lds_u32x4_ptr)(c.ord + k * 1024);
    }
    if (n == 5) {
#pragma unroll
      for (int k = 0; k < 4; ++k)
        __builtin_amdgcn_raw_buffer_store_b128(L.ob[k], c.rsrc, c.gvoff + (k & 1) * 2048, c.obase + (1 + (k >> 1)) * 4096, ST_AUX);
    }
  }
  if (G == 1 || G == 3) {
    constexpr int TL = G == 1 ? 3 : 6;
    constexpr int CUT = G == 1 ? 10 : 14;
    if (n == 5) {
      L.QA[0] = sel4(cc >= CUT, st.Qp[TA][0], st.Qp[TL][0]);
      L.QA[1] = sel4(cc >= CUT, st.Qp[TA][1], st.Qp[TL][1]);
    }
    if (n == 8) {
      L.KA[0] = sel4(cc >= CUT, st.Kp[TA][0], st.Kp[TL][0]);
      L.KA[1] = sel4(cc >= CUT, st.Kp[TA][1], st.Kp[TL][1]);
    }
    if (n == 12) {
#pragma unroll
      for (int k = 0; k < 4; ++k) {
        L.VA[k][0] = (G == 1 && g == 3) ? st.Vp[TA][k][0] : st.Vp[TL][k][0];
        L.VA[k][1] = (G == 1 ? g >= 2 : g == 3) ? st.Vp[TA][k][1] : st.Vp[TL][k][1];
      }
    }
  } else {
    if (n == 5) { L.QA[0] = st.Qp[TA][0]; L.QA[1] = st.Qp[TA][1]; }
    if (n == 8) { L.KA[0] = st.Kp[TA][0]; L.KA[1] = st.Kp[TA][1]; }
    if (n == 12) {
#pragma unroll
      for (int k = 0; k < 4; ++k) L.VA[k] = st.Vp[TA][k];
    }
  }
  if (n == 9 || n == 10) {
    constexpr int qs = (n - 9) & 1;
#pragma unroll
    for (int ks = 0; ks < 2; ++ks)
#pragma unroll
      for (int i = 0; i < 2; ++i) {
        float lo = L.S[ks][qs][2 * i], hi = L.S[ks][qs][2 * i + 1];
        bool masked = false, ok = true;
        if (G == 0 && ks == 1) { masked = true; ok = i == 0 ? g < 3 : g < 2; }
        if (G == 1 && ks == 0) { masked = true; ok = i == 0 ? (g == 0 || g == 3) : g != 1; }
        if (G == 2 && ks == 0) { masked = true; ok = g >= 1; }
        if (G == 2 && ks == 1 && i == 1) { masked = true; ok = g < 3; }
        if (G == 3 && ks == 0) { masked = true; ok = i == 0 ? g < 2 : g != 2; }
        if (masked) { lo = ok ? lo : NINF; hi = ok ? hi : NINF; }
        L.sv[qs][4 * ks + 2 * i] = lo;
        L.sv[qs][4 * ks + 2 * i + 1] = hi;
      }
    float m = L.sv[qs][0];
#pragma unroll
    for (int i = 1; i < 8; ++i) m = fmaxf(m, L.sv[qs][i]);
    L.mx[qs] = xmax(m) * cexp;
  }
  if (n == 11 || n == 12) {
    constexpr int qs = (n - 11) & 1;
    float s = 0.f;
#pragma unroll
    for (int i = 0; i < 8; ++i) {
      L.sv[qs][i] = __builtin_amdgcn_exp2f(__builtin_fmaf(L.sv[qs][i], cexp, -L.mx[qs]));
      s += L.sv[qs][i];
    }
    L.sm[qs] = xsum(s);
  }
  if (n == 13 || n == 14) {
    constexpr int qs = (n - 13) & 1;
    float inv = __builtin_amdgcn_rcpf(L.sm[qs]);
    if (G == 0 && qs == 1) inv = cc < 10 ? inv : 0.f;
    if (G == 2 && qs == 0) inv = cc >= 4 ? inv : 0.f;
    if (G == 2 && qs == 1) inv = cc < 14 ? inv : 0.f;
#pragma unroll
    for (int i = 0; i < 4; ++i) L.Pp[qs][i] = pack2(L.sv[qs][2 * i] * inv, L.sv[qs][2 * i + 1] * inv);
  }
  if (n == 2 || n == 3) { PAT(NT, 2, nv * VS); }
  else { PAT(NT, 1, nv * VS); }
  if (n < 9 || n == 15) SBAR();
  if (G == 0 && (n == 1 || n == 5)) { LBAR(); }
  if (n == 8) {
    L.S[0][0] = MFMA(L.KA[0], L.QA[0], f32x4{});
    L.S[1][0] = MFMA(st.Kp[TB][0], L.QA[0], f32x4{});
    L.S[0][1] = MFMA(L.KA[0], st.Qp[TB][0], f32x4{});
    L.S[1][1] = MFMA(st.Kp[TB][0], st.Qp[TB][0], f32x4{});
    L.S[0][0] = MFMA(L.KA[1], L.QA[1], L.S[0][0]);
    L.S[1][0] = MFMA(st.Kp[TB][1], L.QA[1], L.S[1][0]);
    L.S[0][1] = MFMA(L.KA[1], st.Qp[TB][1], L.S[0][1]);
    L.S[1][1] = MFMA(st.Kp[TB][1], st.Qp[TB][1], L.S[1][1]);
    SBAR();
    if (G == 0) { LBAR(); }
  }
  if constexpr (n + 1 < 16) job<G, n + 1>(c, st, L);
}

template <int G>
DI void group_body(const Ctx& c, St& st) {
  constexpr int TA = G == 0 ? 0 : G == 1 ? 1 : G == 2 ? 3 : 4;
  constexpr int TB = G == 0 ? 1 : G == 1 ? 2 : G == 2 ? 4 : 5;
  const int cc = c.cc;
  Loc L;
  job<G, 0>(c, st, L);
  if (G < 3) {
    if (G == 0) { WAITV(0); } else if (G == 1) { WAITV(2); } else { WAITV(4); }
    SBAR();
    read_e<(G < 2 ? 2 : 1)>(c, st.E);
  }
  {
    u32x4 Vf[4];
#pragma unroll
    for (int j = 0; j < 4; ++j) {
      Vf[j][0] = L.VA[j][0]; Vf[j][1] = L.VA[j][1];
      Vf[j][2] = st.Vp[TB][j][0]; Vf[j][3] = st.Vp[TB][j][1];
    }
    const u32x4 z = {};
    if (G == 1 || G == 3) {
      constexpr int TL = G == 1 ? 3 : 6;
      constexpr int CUT = G == 1 ? 10 : 14;
      const u32x4 Ph = sel4(cc >= CUT, L.Pp[0], z);
      const u32x4 Pl = G == 1 ? sel4(cc < 4, L.Pp[0], z) : L.Pp[0];
#pragma unroll
      for (int j = 0; j < 4; ++j) st.O[TB][j] = MFMA(Vf[j], L.Pp[1], st.O[TB][j]);
#pragma unroll
      for (int j = 0; j < 4; ++j) st.O[TA][j] = MFMA(Vf[j], Ph, st.O[TA][j]);
#pragma unroll
      for (int j = 0; j < 4; ++j) st.O[TL][j] = MFMA(Vf[j], Pl, st.O[TL][j]);
    } else {
#pragma unroll
      for (int j = 0; j < 4; ++j) st.O[TA][j] = MFMA(Vf[j], L.Pp[0], st.O[TA][j]);
#pragma unroll
      for (int j = 0; j < 4; ++j) st.O[TB][j] = MFMA(Vf[j], L.Pp[1], st.O[TB][j]);
    }
  }
  SBAR();
  if (G < 2) {
    asm volatile("s_waitcnt lgkmcnt(0)" ::: "memory");
    dma_e<G + 2>(c);
    SBAR();
  }
}

__global__ __launch_bounds__(WAVES * 64, 2) void autoint_kernel(
    const __bf16* __restrict__ e, const __bf16* __restrict__ wq, const __bf16* __restrict__ wk,
    const __bf16* __restrict__ wv, const __bf16* __restrict__ wres, __bf16* __restrict__ out, unsigned long long* prof) {
  extern __shared__ __attribute__((aligned(16))) char smem[];
  const int tid = threadIdx.x;
  const int lane = tid & 63;
  const int wave = __builtin_amdgcn_readfirstlane(tid >> 6);
  const int cc = lane & 15, g = lane >> 4;
  const unsigned lds0 = __builtin_amdgcn_readfirstlane((unsigned)(size_t)(lds_void_ptr)smem);
  const int bid = blockIdx.x;
  const int hd = (bid >> 3) & 1;
  const int grp = (bid & 7) | ((bid >> 4) << 3);
  const int b0 = grp * BPG + wave * EPW;

  Ctx c;
  c.lane = lane;
  c.cc = cc;
  c.g = g;
  c.prof = prof + (size_t)(blockIdx.x * WAVES + wave) * 32;
  STAMP(0);
  c.ebuf = lds0 + 65536 + wave * 12288;
  c.ebase = (const char*)e + (size_t)b0 * EBYTES;
  c.obase = b0 * EBYTES;
  {
    const int c0 = cc ^ g;
#pragma unroll
    for (int j = 0; j < 4; ++j) c.evoff[j] = (unsigned)((g * 256 + (c0 << 4)) ^ (j << 6));
  }
  const bool leader = wave < 4;
  c.leader = leader;
  if (wave == CODEPF) {
    unsigned long long pc;
    asm volatile("s_getpc_b64 %0" : "=s"(pc));
    const unsigned t0 = lane * 128, t1 = t0 + 8192;
    asm volatile(
        "s_mov_b32 m0, %3\n\ts_nop 0\n\t"
        "global_load_lds_dword %0, %2\n\t"
        "global_load_lds_dword %1, %2"
        :
        : "v"(t0), "v"(t1), "s"(pc), "s"(c.ebuf + 8192)
        : "memory", "m0");
  }
  if (leader) {
    dma_e<0>(c);
  } else {
    unsigned o64[4], o128[4];
    const int r8 = lane >> 3;
    const int krow = 32 * (wave - 4) + 8 * (r8 >> 2) + (r8 & 3);
    const int ch = (lane & 7) ^ (r8 & 6);
#pragma unroll
    for (int i = 0; i < 4; ++i) {
      const int k = krow + 16 * (i & 1) + 4 * (i >> 1);
      o64[i] = (unsigned)(k * 128 + ch * 16 + 4096 - i * 1024);
      o128[i] = (unsigned)(k * 256 + ch * 16 + 4096 - i * 1024);
    }
    const char* sq = (const char*)wq + hd * 16384;
    const char* sk = (const char*)wk + hd * 16384;
    const char* sv = (const char*)wv + hd * 16384;
    const char* sr = (const char*)wres + hd * 128;
    const unsigned dst = lds0 + (wave - 4) * 4096;
    glds4(o64[0], o64[1], o64[2], o64[3], sq - 4096, dst);
    glds4(o64[0], o64[1], o64[2], o64[3], sk - 4096, dst + 16384);
    glds4(o64[0], o64[1], o64[2], o64[3], sv - 4096, dst + 32768);
    glds4(o128[0], o128[1], o128[2], o128[3], sr - 4096, dst + 49152);
    dma_e<0>(c);
  }
  SBAR();

  unsigned wb[4];
  {
    const int q4 = (lane & 15) >> 2, p4 = lane & 3;
    const int r8 = (g & 1) * 4 + q4;
#pragma unroll
    for (int j = 0; j < 4; ++j)
      wb[j] = lds0 + (wave & 3) * 4096 + (g >> 1) * 1024 + r8 * 128 + ((((2 * j) ^ (r8 & 6)) + (p4 >> 1)) << 4) + (p4 & 1) * 8;
    c.wl = lds0 + lane * 16;
    asm volatile("" : "+v"(c.wl));
    c.eread = c.ebuf + cc * 256 + ((g ^ cc) << 4);
    const unsigned obuf = c.ebuf + 8192;
    c.owr = obuf + cc * 128 + (((g >> 1) ^ (cc & 7)) << 4) + (g & 1) * 8;
    c.ord = obuf + lane * 16;
    const int orow = lane >> 3, oc = (lane & 7) ^ (orow & 7);
    c.gvoff = orow * 256 + hd * 128 + oc * 16;
    c.rsrc = __builtin_amdgcn_make_buffer_rsrc((void*)out, 0, NB_TOTAL * EBYTES, 0x00020000);
  }

  St st;
  STAMP(1);
  if (leader) {
    __builtin_amdgcn_s_barrier();
  } else {
    const unsigned wlw = c.wl + (wave & 3) * 4096;
    WAITV(20); relayout(wb, wlw, 0);     __builtin_amdgcn_s_barrier(); SBAR();
    WAITV(16); relayout(wb, wlw, 16384); __builtin_amdgcn_s_barrier(); SBAR();
    WAITV(12); relayout(wb, wlw, 32768); __builtin_amdgcn_s_barrier(); SBAR();
    WAITV(8);  relayout(wb, wlw, 49152); __builtin_amdgcn_s_barrier(); SBAR();
    __builtin_amdgcn_s_setprio(1);
  }
  WAITV(0);
  SBAR();
  STAMP(2);
  read_e<2>(c, st.E);
  loadw(c, st.W[0], 0);
  loadw(c, st.W[1], 1);
  asm volatile("s_waitcnt lgkmcnt(0)" ::: "memory");
  SBAR();
  dma_e<1>(c);
  SBAR();
  STAMP(3);

  group_body<0>(c, st);
  STAMP(4);
  group_body<1>(c, st);
  STAMP(5);
  group_body<2>(c, st);
  STAMP(6);
  __builtin_amdgcn_s_setprio(0);
  group_body<3>(c, st);
  STAMP(7);
  {
    stage_out(c, st.O[4], 0);
    stage_out(c, st.O[5], 2048);
    stage_out(c, st.O[6], (unsigned)-8192);
    u32x4 ob[5];
#pragma unroll
    for (int k = 0; k < 4; ++k) ob[k] = *(lds_u32x4_ptr)(c.ord + k * 1024);
    ob[4] = *(lds_u32x4_ptr)(c.ord - 8192);
#pragma unroll
    for (int k = 0; k < 4; ++k)
      __builtin_amdgcn_raw_buffer_store_b128(ob[k], c.rsrc, c.gvoff + (k & 1) * 2048, c.obase + (4 + (k >> 1)) * 4096, ST_AUX);
    __builtin_amdgcn_raw_buffer_store_b128(ob[4], c.rsrc, c.gvoff, c.obase + 6 * 4096, ST_AUX);
  }
  STAMP(8);
}

__global__ __launch_bounds__(512) void exp_kernel(char* dst, const char* srcp, unsigned long long* prof) {
  const int lane = threadIdx.x & 63, wave = threadIdx.x >> 6;
  const int bid = blockIdx.x;
  const int hd = (bid >> 3) & 1;
  const int grp = (bid & 7) | ((bid >> 4) << 3);
  const int b0 = grp * BPG + wave * EPW;
  unsigned long long t0, t1;
  asm volatile("s_memrealtime %0\n\ts_waitcnt lgkmcnt(0)" : "=s"(t0) :: "memory");
  __amdgpu_buffer_rsrc_t rs = __builtin_amdgcn_make_buffer_rsrc((void*)dst, 0, NB_TOTAL * EBYTES, 0x00020000);
  const int orow = lane >> 3, oc = (lane & 7) ^ (orow & 7);
  const int gv = orow * 256 + hd * 128 + oc * 16;
  const int ob = b0 * EBYTES;
  const int step = 2048;
  u32x4 v = {(unsigned)lane, 1u, 2u, 3u};
#pragma unroll
  for (int k = 0; k < 13; ++k) __builtin_amdgcn_raw_buffer_store_b128(v, rs, gv + k * step, ob, XAUX);
  asm volatile("s_waitcnt vmcnt(0)" ::: "memory");
  asm volatile("s_memrealtime %0\n\ts_waitcnt lgkmcnt(0)" : "=s"(t1) :: "memory");
  if (lane == 0) { prof[(size_t)(bid * 8 + wave) * 32 + 1] = t0; prof[(size_t)(bid * 8 + wave) * 32 + 3] = t1; }
}
__global__ void diag_kernel(const unsigned long long* prof) {
  __shared__ unsigned long long smin[256];
  __shared__ double sacc[256];
  const int t = threadIdx.x;
  unsigned long long mn = ~0ull;
  for (int i = t; i < 2048; i += 256) { unsigned long long a = prof[(size_t)i * 32 + 1]; mn = a < mn ? a : mn; }
  smin[t] = mn;
  __syncthreads();
  for (int s = 128; s > 0; s >>= 1) { if (t < s) smin[t] = smin[t] < smin[t + s] ? smin[t] : smin[t + s]; __syncthreads(); }
  mn = smin[0];
  double acc = DIAGM == 2 ? 1e30 : 0;
  for (int i = t; i < 2048; i += 256) {
    double v = (double)(prof[(size_t)i * 32 + 2 * DIAGK + 1] - mn);
    if (DIAGM == 0) acc += v; else if (DIAGM == 1) acc = v > acc ? v : acc; else acc = v < acc ? v : acc;
  }
  sacc[t] = acc;
  __syncthreads();
  for (int s = 128; s > 0; s >>= 1) {
    if (t < s) { double a = sacc[t], b = sacc[t + s]; sacc[t] = (DIAGM == 0) ? a + b : DIAGM == 1 ? (a > b ? a : b) : (a < b ? a : b); }
    __syncthreads();
  }
  double v = sacc[0];
  if (DIAGM == 0) v /= 2048;
  if (v < 0) v = 0; if (v > 20000) v = 20000;
  unsigned long long dur = (unsigned long long)(v * DIAGS);
  if (t == 0) {
    unsigned long long s0, s1;
    asm volatile("s_memrealtime %0\n\ts_waitcnt lgkmcnt(0)" : "=s"(s0) :: "memory");
    for (int it = 0; it < 1000000; ++it) {
      asm volatile("s_memrealtime %0\n\ts_waitcnt lgkmcnt(0)" : "=s"(s1) :: "memory");
      if (s1 - s0 >= dur) break;
    }
  }
}

extern "C" void kernel_launch(void* const* d_in, const int* in_sizes, int n_in, void* d_out, int out_size,
                              void* d_ws, size_t ws_size, hipStream_t stream) {
  const __bf16* e = (const __bf16*)d_in[0];
  const __bf16* wq = (const __bf16*)d_in[1];
  const __bf16* wk = (const __bf16*)d_in[2];
  const __bf16* wv = (const __bf16*)d_in[3];
  const __bf16* wres = (const __bf16*)d_in[4];
  __bf16* out = (__bf16*)d_out;
  (void)hipFuncSetAttribute((const void*)autoint_kernel, hipFuncAttributeMaxDynamicSharedMemorySize, LDS_BYTES);
  exp_kernel<<<256, 512, 0, stream>>>(XDST == 0 ? (char*)d_out : (char*)d_ws + (64 << 20), (const char*)d_in[0], (unsigned long long*)d_ws);
  diag_kernel<<<1, 256, 0, stream>>>((const unsigned long long*)d_ws);
  autoint_kernel<<<2 * NB_TOTAL / BPG, WAVES * 64, LDS_BYTES, stream>>>(e, wq, wk, wv, wres, out, (unsigned long long*)d_ws);
}
```

```cpp
#define CODEPF 3
#define CPFN 7
#include <hip/hip_runtime.h>
#include <hip/hip_bf16.h>

typedef __attribute__((ext_vector_type(8))) __bf16 bf16x8;
typedef __attribute__((ext_vector_type(2))) __bf16 bf16x2;
typedef __attribute__((ext_vector_type(4))) short s16x4;
typedef __attribute__((ext_vector_type(2))) short s16x2;
typedef __attribute__((ext_vector_type(4))) float f32x4;
typedef __attribute__((ext_vector_type(4))) unsigned u32x4;
typedef __attribute__((ext_vector_type(2))) unsigned u32x2;
typedef __attribute__((address_space(3))) s16x4* lds_s16x4_ptr;
typedef __attribute__((address_space(3))) u32x4* lds_u32x4_ptr;
typedef __attribute__((address_space(3))) u32x2* lds_u32x2_ptr;
typedef __attribute__((address_space(3))) void* lds_void_ptr;

#define DI __device__ __forceinline__
#define BF(x) __builtin_bit_cast(bf16x8, (x))
#define MFMA(a, b, c) __builtin_amdgcn_mfma_f32_16x16x32_bf16(BF(a), BF(b), (c), 0, 0, 0)

#ifndef NVS
#define NVS 3
#endif
#define PRIO 3
#define ST_AUX 17

constexpr int NB_TOTAL = 4096;
constexpr int NF = 26;
constexpr int ND = 128;
constexpr int WAVES = 8;
constexpr int EPW = 4;
constexpr int BPG = WAVES * EPW;
constexpr int EBYTES = NF * ND * 2;
constexpr int LDS_BYTES = 65536 + WAVES * 12288;

DI unsigned pack2(float lo, float hi) {
  bf16x2 v;
  v[0] = (__bf16)lo;
  v[1] = (__bf16)hi;
  return __builtin_bit_cast(unsigned, v);
}
DI unsigned relu2(float lo, float hi) {
  unsigned u = pack2(lo, hi);
  asm volatile("" : "+v"(u));
  s16x2 z = {};
  return __builtin_bit_cast(unsigned, __builtin_elementwise_max(__builtin_bit_cast(s16x2, u), z));
}

DI void glds4(unsigned v0, unsigned v1, unsigned v2, unsigned v3, const void* sbase, unsigned lds_dst) {
  asm volatile(
      "s_mov_b32 m0, %5\n\ts_nop 0\n\t"
      "global_load_lds_dwordx4 %0, %4\n\t"
      "global_load_lds_dwordx4 %1, %4 offset:1024\n\t"
      "global_load_lds_dwordx4 %2, %4 offset:2048\n\t"
      "global_load_lds_dwordx4 %3, %4 offset:3072"
      :
      : "v"(v0), "v"(v1), "v"(v2), "v"(v3), "s"(sbase), "s"(lds_dst)
      : "memory", "m0");
}
DI void glds2(unsigned v0, unsigned v1, const void* sbase, unsigned lds_dst) {
  asm volatile(
      "s_mov_b32 m0, %3\n\ts_nop 0\n\t"
      "global_load_lds_dwordx4 %0, %2\n\t"
      "global_load_lds_dwordx4 %1, %2 offset:1024"
      :
      : "v"(v0), "v"(v1), "s"(sbase), "s"(lds_dst)
      : "memory", "m0");
}

#define SGB(mask, n) __builtin_amdgcn_sched_group_barrier(mask, n, 0)
#define SBAR() __builtin_amdgcn_sched_barrier(0)
#define WAITV(n) asm volatile("s_waitcnt vmcnt(" #n ")" ::: "memory");
#define WAITW(n)                  \
  WAITV(n)                        \
  __builtin_amdgcn_s_barrier();   \
  __builtin_amdgcn_sched_barrier(0);

#define STAMP(k)
#define LBAR()                                      \
  if (c.leader) __builtin_amdgcn_s_barrier();       \
  __builtin_amdgcn_sched_barrier(0);

struct Ctx {
  bool leader;
  unsigned long long* prof;
  unsigned wl;
  unsigned eread;
  unsigned ebuf;
  unsigned owr;
  unsigned ord;
  unsigned evoff[4];
  int gvoff;
  int lane, cc, g;
  const char* ebase;
  int obase;
  __amdgpu_buffer_rsrc_t rsrc;
};

struct St {
  u32x4 E[2][4];
  u32x4 W[3][4];
  u32x4 Qp[7][2], Kp[7][2];
  u32x2 Vp[7][4];
  f32x4 O[7][4];
};

DI void loadw(const Ctx& c, u32x4 (&W)[4], int n) {
  const int X = n >> 2, j = n & 3;
#pragma unroll
  for (int s = 0; s < 4; ++s) W[s] = *(lds_u32x4_ptr)(c.wl + X * 16384 + s * 4096 + j * 1024);
}
DI void relayout(const unsigned (&wb)[4], unsigned wl, int off) {
  u32x4 W[4];
#pragma unroll
  for (int j = 0; j < 4; ++j) {
    s16x4 lo = __builtin_amdgcn_ds_read_tr16_b64_v4i16((lds_s16x4_ptr)(wb[j] + off));
    s16x4 hi = __builtin_amdgcn_ds_read_tr16_b64_v4i16((lds_s16x4_ptr)(wb[j] + off + 2048));
    W[j] = __builtin_bit_cast(u32x4, __builtin_shufflevector(lo, hi, 0, 1, 2, 3, 4, 5, 6, 7));
  }
  asm volatile("s_waitcnt lgkmcnt(0)" ::: "memory");
#pragma unroll
  for (int j = 0; j < 4; ++j) *(lds_u32x4_ptr)(wl + off + j * 1024) = W[j];
  asm volatile("s_waitcnt lgkmcnt(0)" ::: "memory");
}

template <int G>
DI void dma_e(const Ctx& c) {
  const char* p = c.ebase + (size_t)G * 8192;
  if (G < 3) {
    glds4(c.evoff[0], c.evoff[1], c.evoff[2], c.evoff[3], p, c.ebuf);
    glds4(c.evoff[0], c.evoff[1], c.evoff[2], c.evoff[3], p + 4096, c.ebuf + 4096);
  } else {
    glds2(c.evoff[0], c.evoff[1], p, c.ebuf);
  }
}

template <int NT>
DI void read_e(const Ctx& c, u32x4 (&E)[2][4]) {
#pragma unroll
  for (int s = 0; s < 4; ++s) {
    const unsigned a = c.eread ^ (s << 6);
    E[0][s] = *(lds_u32x4_ptr)(a);
    if (NT > 1) E[1][s] = *(lds_u32x4_ptr)(a + 4096);
  }
}

DI void stage_out(const Ctx& c, const f32x4 (&O)[4], unsigned slot) {
#pragma unroll
  for (int j = 0; j < 4; ++j) {
    u32x2 v;
    v[0] = relu2(O[j][0], O[j][1]);
    v[1] = relu2(O[j][2], O[j][3]);
    *(lds_u32x2_ptr)((c.owr ^ (j << 5)) + slot) = v;
  }
}

DI float xmax(float m) {
  auto a = __builtin_amdgcn_permlane32_swap(__float_as_uint(m), __float_as_uint(m), false, false);
  asm("v_max_f32 %0, %1, %2" : "=v"(m) : "v"(a[0]), "v"(a[1]));
  auto b = __builtin_amdgcn_permlane16_swap(__float_as_uint(m), __float_as_uint(m), false, false);
  asm("v_max_f32 %0, %1, %2" : "=v"(m) : "v"(b[0]), "v"(b[1]));
  return m;
}
DI float xsum(float m) {
  auto a = __builtin_amdgcn_permlane32_swap(__float_as_uint(m), __float_as_uint(m), false, false);
  m = __uint_as_float(a[0]) + __uint_as_float(a[1]);
  auto b = __builtin_amdgcn_permlane16_swap(__float_as_uint(m), __float_as_uint(m), false, false);
  return __uint_as_float(b[0]) + __uint_as_float(b[1]);
}
DI u32x4 sel4(bool p, const u32x4& a, const u32x4& b) {
  u32x4 r;
#pragma unroll
  for (int i = 0; i < 4; ++i) r[i] = p ? a[i] : b[i];
  return r;
}

#define PAT(NT_, ND_, NV)                          \
  _Pragma("unroll") for (int q_ = 0; q_ < 4; ++q_) { \
    SGB(0x8, 1);                                   \
    SGB(0x100, ND_);                               \
    SGB(0x2, NV);                                  \
    if ((NT_) > 1) { SGB(0x8, 1); SGB(0x2, NV); }  \
    if ((NT_) > 2) { SGB(0x8, 1); SGB(0x2, NV); }  \
  }

struct Loc {
  f32x4 acc[2][2];
  f32x4 S[2][2];
  float sv[2][8], mx[2], sm[2];
  u32x4 Pp[2];
  u32x4 ob[4];
  u32x4 QA[2], KA[2];
  u32x2 VA[4];
};

template <int G, int n>
DI void job(const Ctx& c, St& st, Loc& L) {
  constexpr int NT = G < 3 ? 2 : 1;
  constexpr int T0 = 2 * G;
  constexpr int NM = 4 * NT;
  constexpr int NDS = 8 / NM;
  constexpr int VS = 2 / NT;
  constexpr int WB = (16 * G) % 3;
  constexpr int TA = G == 0 ? 0 : G == 1 ? 1 : G == 2 ? 3 : 4;
  constexpr int TB = G == 0 ? 1 : G == 1 ? 2 : G == 2 ? 4 : 5;
  constexpr int X = n >> 2, j = n & 3;
  const float cexp = 0.125f * 1.4426950408889634f;
  const float NINF = -__builtin_inff();
  const int g = c.g, cc = c.cc;
  if (n + 2 < 16) loadw(c, st.W[(WB + n + 2) % 3], n + 2);
  else if (G < 3) loadw(c, st.W[(WB + n + 2) % 3], n + 2 - 16);
  const u32x4(&W)[4] = st.W[(WB + n) % 3];
#pragma unroll
  for (int tt = 0; tt < NT; ++tt) L.acc[n & 1][tt] = f32x4{};
#pragma unroll
  for (int s = 0; s < 4; ++s)
#pragma unroll
    for (int tt = 0; tt < NT; ++tt)
      L.acc[n & 1][tt] = X == 2 ? MFMA(st.E[tt][s], W[s], L.acc[n & 1][tt]) : MFMA(W[s], st.E[tt][s], L.acc[n & 1][tt]);
  if (X == 3) {
#pragma unroll
    for (int tt = 0; tt < NT; ++tt) st.O[T0 + tt][j] = L.acc[n & 1][tt];
  }
  if (n > 0 && n <= 12) {
    constexpr int pX = (n - 1) >> 2, pj = (n - 1) & 3;
#pragma unroll
    for (int tt = 0; tt < NT; ++tt) {
      const f32x4 a = L.acc[(n - 1) & 1][tt];
      const unsigned lo = pack2(a[0], a[1]), hi = pack2(a[2], a[3]);
      if (pX == 0) { st.Qp[T0 + tt][pj >> 1][2 * (pj & 1)] = lo; st.Qp[T0 + tt][pj >> 1][2 * (pj & 1) + 1] = hi; }
      if (pX == 1) { st.Kp[T0 + tt][pj >> 1][2 * (pj & 1)] = lo; st.Kp[T0 + tt][pj >> 1][2 * (pj & 1) + 1] = hi; }
      if (pX == 2) { st.Vp[T0 + tt][pj][0] = lo; st.Vp[T0 + tt][pj][1] = hi; }
    }
  }
  constexpr int nv = (G > 0 && n == 0) || (G == 2 && n == 1) ? 3
                     : ((G & 1) && (n == 5 || n == 8)) ? 2
                     : (n >= 9) ? NVS : 1;
  if (G == 1 || G == 3) {
    constexpr int TO = G == 1 ? 0 : 3;
    if (n == 0) stage_out(c, st.O[TO], 0);
    if (n == 2) { L.ob[0] = *(lds_u32x4_ptr)(c.ord); L.ob[1] = *(lds_u32x4_ptr)(c.ord + 1024); }
    if (n == 4) {
      __builtin_amdgcn_raw_buffer_store_b128(L.ob[0], c.rsrc, c.gvoff, c.obase + TO * 4096, ST_AUX);
      __builtin_amdgcn_raw_buffer_store_b128(L.ob[1], c.rsrc, c.gvoff + 2048, c.obase + TO * 4096, ST_AUX);
    }
  }
  if (G == 2) {
    if (n == 0) stage_out(c, st.O[1], 0);
    if (n == 1) stage_out(c, st.O[2], 2048);
    if (n == 3) {
#pragma unroll
      for (int k = 0; k < 4; ++k) L.ob[k] = *(lds_u32x4_ptr)(c.ord + k * 1024);
    }
    if (n == 5) {
#pragma unroll
      for (int k = 0; k < 4; ++k)
        __builtin_amdgcn_raw_buffer_store_b128(L.ob[k], c.rsrc, c.gvoff + (k & 1) * 2048, c.obase + (1 + (k >> 1)) * 4096, ST_AUX);
    }
  }
  if (G == 1 || G == 3) {
    constexpr int TL = G == 1 ? 3 : 6;
    constexpr int CUT = G == 1 ? 10 : 14;
    if (n == 5) {
      L.QA[0] = sel4(cc >= CUT, st.Qp[TA][0], st.Qp[TL][0]);
      L.QA[1] = sel4(cc >= CUT, st.Qp[TA][1], st.Qp[TL][1]);
    }
    if (n == 8) {
      L.KA[0] = sel4(cc >= CUT, st.Kp[TA][0], st.Kp[TL][0]);
      L.KA[1] = sel4(cc >= CUT, st.Kp[TA][1], st.Kp[TL][1]);
    }
    if (n == 12) {
#pragma unroll
      for (int k = 0; k < 4; ++k) {
        L.VA[k][0] = (G == 1 && g == 3) ? st.Vp[TA][k][0] : st.Vp[TL][k][0];
        L.VA[k][1] = (G == 1 ? g >= 2 : g == 3) ? st.Vp[TA][k][1] : st.Vp[TL][k][1];
      }
    }
  } else {
    if (n == 5) { L.QA[0] = st.Qp[TA][0]; L.QA[1] = st.Qp[TA][1]; }
    if (n == 8) { L.KA[0] = st.Kp[TA][0]; L.KA[1] = st.Kp[TA][1]; }
    if (n == 12) {
#pragma unroll
      for (int k = 0; k < 4; ++k) L.VA[k] = st.Vp[TA][k];
    }
  }
  if (n == 9 || n == 10) {
    constexpr int qs = (n - 9) & 1;
#pragma unroll
    for (int ks = 0; ks < 2; ++ks)
#pragma unroll
      for (int i = 0; i < 2; ++i) {
        float lo = L.S[ks][qs][2 * i], hi = L.S[ks][qs][2 * i + 1];
        bool masked = false, ok = true;
        if (G == 0 && ks == 1) { masked = true; ok = i == 0 ? g < 3 : g < 2; }
        if (G == 1 && ks == 0) { masked = true; ok = i == 0 ? (g == 0 || g == 3) : g != 1; }
        if (G == 2 && ks == 0) { masked = true; ok = g >= 1; }
        if (G == 2 && ks == 1 && i == 1) { masked = true; ok = g < 3; }
        if (G == 3 && ks == 0) { masked = true; ok = i == 0 ? g < 2 : g != 2; }
        if (masked) { lo = ok ? lo : NINF; hi = ok ? hi : NINF; }
        L.sv[qs][4 * ks + 2 * i] = lo;
        L.sv[qs][4 * ks + 2 * i + 1] = hi;
      }
    float m = L.sv[qs][0];
#pragma unroll
    for (int i = 1; i < 8; ++i) m = fmaxf(m, L.sv[qs][i]);
    L.mx[qs] = xmax(m) * cexp;
  }
  if (n == 11 || n == 12) {
    constexpr int qs = (n - 11) & 1;
    float s = 0.f;
#pragma unroll
    for (int i = 0; i < 8; ++i) {
      L.sv[qs][i] = __builtin_amdgcn_exp2f(__builtin_fmaf(L.sv[qs][i], cexp, -L.mx[qs]));
      s += L.sv[qs][i];
    }
    L.sm[qs] = xsum(s);
  }
  if (n == 13 || n == 14) {
    constexpr int qs = (n - 13) & 1;
    float inv = __builtin_amdgcn_rcpf(L.sm[qs]);
    if (G == 0 && qs == 1) inv = cc < 10 ? inv : 0.f;
    if (G == 2 && qs == 0) inv = cc >= 4 ? inv : 0.f;
    if (G == 2 && qs == 1) inv = cc < 14 ? inv : 0.f;
#pragma unroll
    for (int i = 0; i < 4; ++i) L.Pp[qs][i] = pack2(L.sv[qs][2 * i] * inv, L.sv[qs][2 * i + 1] * inv);
  }
  if (n == 2 || n == 3) { PAT(NT, 2, nv * VS); }
  else { PAT(NT, 1, nv * VS); }
  if (n < 9 || n == 15) SBAR();
  if (G == 0 && (n == 1 || n == 5)) { LBAR(); }
  if (n == 8) {
    L.S[0][0] = MFMA(L.KA[0], L.QA[0], f32x4{});
    L.S[1][0] = MFMA(st.Kp[TB][0], L.QA[0], f32x4{});
    L.S[0][1] = MFMA(L.KA[0], st.Qp[TB][0], f32x4{});
    L.S[1][1] = MFMA(st.Kp[TB][0], st.Qp[TB][0], f32x4{});
    L.S[0][0] = MFMA(L.KA[1], L.QA[1], L.S[0][0]);
    L.S[1][0] = MFMA(st.Kp[TB][1], L.QA[1], L.S[1][0]);
    L.S[0][1] = MFMA(L.KA[1], st.Qp[TB][1], L.S[0][1]);
    L.S[1][1] = MFMA(st.Kp[TB][1], st.Qp[TB][1], L.S[1][1]);
    SBAR();
    if (G == 0) { LBAR(); }
  }
  if constexpr (n + 1 < 16) job<G, n + 1>(c, st, L);
}

template <int G>
DI void group_body(const Ctx& c, St& st) {
  constexpr int TA = G == 0 ? 0 : G == 1 ? 1 : G == 2 ? 3 : 4;
  constexpr int TB = G == 0 ? 1 : G == 1 ? 2 : G == 2 ? 4 : 5;
  const int cc = c.cc;
  Loc L;
  job<G, 0>(c, st, L);
  if (G < 3) {
    if (G == 0) { WAITV(0); } else if (G == 1) { WAITV(2); } else { WAITV(4); }
    SBAR();
    read_e<(G < 2 ? 2 : 1)>(c, st.E);
  }
  {
    u32x4 Vf[4];
#pragma unroll
    for (int j = 0; j < 4; ++j) {
      Vf[j][0] = L.VA[j][0]; Vf[j][1] = L.VA[j][1];
      Vf[j][2] = st.Vp[TB][j][0]; Vf[j][3] = st.Vp[TB][j][1];
    }
    const u32x4 z = {};
    if (G == 1 || G == 3) {
      constexpr int TL = G == 1 ? 3 : 6;
      constexpr int CUT = G == 1 ? 10 : 14;
      const u32x4 Ph = sel4(cc >= CUT, L.Pp[0], z);
      const u32x4 Pl = G == 1 ? sel4(cc < 4, L.Pp[0], z) : L.Pp[0];
#pragma unroll
      for (int j = 0; j < 4; ++j) st.O[TB][j] = MFMA(Vf[j], L.Pp[1], st.O[TB][j]);
#pragma unroll
      for (int j = 0; j < 4; ++j) st.O[TA][j] = MFMA(Vf[j], Ph, st.O[TA][j]);
#pragma unroll
      for (int j = 0; j < 4; ++j) st.O[TL][j] = MFMA(Vf[j], Pl, st.O[TL][j]);
    } else {
#pragma unroll
      for (int j = 0; j < 4; ++j) st.O[TA][j] = MFMA(Vf[j], L.Pp[0], st.O[TA][j]);
#pragma unroll
      for (int j = 0; j < 4; ++j) st.O[TB][j] = MFMA(Vf[j], L.Pp[1], st.O[TB][j]);
    }
  }
  SBAR();
  if (G < 2) {
    asm volatile("s_waitcnt lgkmcnt(0)" ::: "memory");
    dma_e<G + 2>(c);
    SBAR();
  }
}

__global__ __launch_bounds__(WAVES * 64, 2) void autoint_kernel(
    const __bf16* __restrict__ e, const __bf16* __restrict__ wq, const __bf16* __restrict__ wk,
    const __bf16* __restrict__ wv, const __bf16* __restrict__ wres, __bf16* __restrict__ out, unsigned long long* prof) {
  extern __shared__ __attribute__((aligned(16))) char smem[];
  const int tid = threadIdx.x;
  const int lane = tid & 63;
  const int wave = __builtin_amdgcn_readfirstlane(tid >> 6);
  const int cc = lane & 15, g = lane >> 4;
  const unsigned lds0 = __builtin_amdgcn_readfirstlane((unsigned)(size_t)(lds_void_ptr)smem);
  const int bid = blockIdx.x;
  const int hd = (bid >> 3) & 1;
  const int grp = (bid & 7) | ((bid >> 4) << 3);
  const int b0 = grp * BPG + wave * EPW;

  Ctx c;
  c.lane = lane;
  c.cc = cc;
  c.g = g;
  c.prof = prof + (size_t)(blockIdx.x * WAVES + wave) * 32;
  STAMP(0);
  c.ebuf = lds0 + 65536 + wave * 12288;
  c.ebase = (const char*)e + (size_t)b0 * EBYTES;
  c.obase = b0 * EBYTES;
  {
    const int c0 = cc ^ g;
#pragma unroll
    for (int j = 0; j < 4; ++j) c.evoff[j] = (unsigned)((g * 256 + (c0 << 4)) ^ (j << 6));
  }
  const bool leader = wave < 4;
  c.leader = leader;
  if (wave == CODEPF) {
    unsigned long long pc;
    asm volatile("s_getpc_b64 %0" : "=s"(pc));
    const unsigned t0 = lane * 128, t1 = t0 + 8192, t2 = 16384 + (lane < CPFN ? lane : CPFN) * 128;
    asm volatile(
        "s_mov_b32 m0, %4\n\ts_nop 0\n\t"
        "global_load_lds_dword %0, %3\n\t"
        "global_load_lds_dword %1, %3\n\t"
        "global_load_lds_dword %2, %3"
        :
        : "v"(t0), "v"(t1), "v"(t2), "s"(pc), "s"(c.ebuf + 8192)
        : "memory", "m0");
  }
  if (leader) {
    dma_e<0>(c);
  } else {
    unsigned o64[4], o128[4];
    const int r8 = lane >> 3;
    const int krow = 32 * (wave - 4) + 8 * (r8 >> 2) + (r8 & 3);
    const int ch = (lane & 7) ^ (r8 & 6);
#pragma unroll
    for (int i = 0; i < 4; ++i) {
      const int k = krow + 16 * (i & 1) + 4 * (i >> 1);
      o64[i] = (unsigned)(k * 128 + ch * 16 + 4096 - i * 1024);
      o128[i] = (unsigned)(k * 256 + ch * 16 + 4096 - i * 1024);
    }
    const char* sq = (const char*)wq + hd * 16384;
    const char* sk = (const char*)wk + hd * 16384;
    const char* sv = (const char*)wv + hd * 16384;
    const char* sr = (const char*)wres + hd * 128;
    const unsigned dst = lds0 + (wave - 4) * 4096;
    glds4(o64[0], o64[1], o64[2], o64[3], sq - 4096, dst);
    glds4(o64[0], o64[1], o64[2], o64[3], sk - 4096, dst + 16384);
    glds4(o64[0], o64[1], o64[2], o64[3], sv - 4096, dst + 32768);
    glds4(o128[0], o128[1], o128[2], o128[3], sr - 4096, dst + 49152);
    dma_e<0>(c);
  }
  SBAR();

  unsigned wb[4];
  {
    const int q4 = (lane & 15) >> 2, p4 = lane & 3;
    const int r8 = (g & 1) * 4 + q4;
#pragma unroll
    for (int j = 0; j < 4; ++j)
      wb[j] = lds0 + (wave & 3) * 4096 + (g >> 1) * 1024 + r8 * 128 + ((((2 * j) ^ (r8 & 6)) + (p4 >> 1)) << 4) + (p4 & 1) * 8;
    c.wl = lds0 + lane * 16;
    asm volatile("" : "+v"(c.wl));
    c.eread = c.ebuf + cc * 256 + ((g ^ cc) << 4);
    const unsigned obuf = c.ebuf + 8192;
    c.owr = obuf + cc * 128 + (((g >> 1) ^ (cc & 7)) << 4) + (g & 1) * 8;
    c.ord = obuf + lane * 16;
    const int orow = lane >> 3, oc = (lane & 7) ^ (orow & 7);
    c.gvoff = orow * 256 + hd * 128 + oc * 16;
    c.rsrc = __builtin_amdgcn_make_buffer_rsrc((void*)out, 0, NB_TOTAL * EBYTES, 0x00020000);
  }

  St st;
  STAMP(1);
  if (leader) {
    __builtin_amdgcn_s_barrier();
  } else {
    const unsigned wlw = c.wl + (wave & 3) * 4096;
    WAITV(20); relayout(wb, wlw, 0);     __builtin_amdgcn_s_barrier(); SBAR();
    WAITV(16); relayout(wb, wlw, 16384); __builtin_amdgcn_s_barrier(); SBAR();
    WAITV(12); relayout(wb, wlw, 32768); __builtin_amdgcn_s_barrier(); SBAR();
    WAITV(8);  relayout(wb, wlw, 49152); __builtin_amdgcn_s_barrier(); SBAR();
    __builtin_amdgcn_s_setprio(1);
  }
  WAITV(0);
  SBAR();
  STAMP(2);
  read_e<2>(c, st.E);
  loadw(c, st.W[0], 0);
  loadw(c, st.W[1], 1);
  asm volatile("s_waitcnt lgkmcnt(0)" ::: "memory");
  SBAR();
  dma_e<1>(c);
  SBAR();
  STAMP(3);

  group_body<0>(c, st);
  STAMP(4);
  group_body<1>(c, st);
  STAMP(5);
  group_body<2>(c, st);
  STAMP(6);
  __builtin_amdgcn_s_setprio(0);
  group_body<3>(c, st);
  STAMP(7);
  {
    stage_out(c, st.O[4], 0);
    stage_out(c, st.O[5], 2048);
    stage_out(c, st.O[6], (unsigned)-8192);
    u32x4 ob[5];
#pragma unroll
    for (int k = 0; k < 4; ++k) ob[k] = *(lds_u32x4_ptr)(c.ord + k * 1024);
    ob[4] = *(lds_u32x4_ptr)(c.ord - 8192);
#pragma unroll
    for (int k = 0; k < 4; ++k)
      __builtin_amdgcn_raw_buffer_store_b128(ob[k], c.rsrc, c.gvoff + (k & 1) * 2048, c.obase + (4 + (k >> 1)) * 4096, ST_AUX);
    __builtin_amdgcn_raw_buffer_store_b128(ob[4], c.rsrc, c.gvoff, c.obase + 6 * 4096, ST_AUX);
  }
  STAMP(8);
}

extern "C" void kernel_launch(void* const* d_in, const int* in_sizes, int n_in, void* d_out, int out_size,
                              void* d_ws, size_t ws_size, hipStream_t stream) {
  const __bf16* e = (const __bf16*)d_in[0];
  const __bf16* wq = (const __bf16*)d_in[1];
  const __bf16* wk = (const __bf16*)d_in[2];
  const __bf16* wv = (const __bf16*)d_in[3];
  const __bf16* wres = (const __bf16*)d_in[4];
  __bf16* out = (__bf16*)d_out;
  (void)hipFuncSetAttribute((const void*)autoint_kernel, hipFuncAttributeMaxDynamicSharedMemorySize, LDS_BYTES);
  autoint_kernel<<<2 * NB_TOTAL / BPG, WAVES * 64, LDS_BYTES, stream>>>(e, wq, wk, wv, wres, out, (unsigned long long*)d_ws);
}
```

```cpp
#define DIAGK 0
#define DIAGM 0
#define DIAGS 0
#define CPFN 12
#define CODEPF 3
#include <hip/hip_runtime.h>
#include <hip/hip_bf16.h>

typedef __attribute__((ext_vector_type(8))) __bf16 bf16x8;
typedef __attribute__((ext_vector_type(2))) __bf16 bf16x2;
typedef __attribute__((ext_vector_type(4))) short s16x4;
typedef __attribute__((ext_vector_type(2))) short s16x2;
typedef __attribute__((ext_vector_type(4))) float f32x4;
typedef __attribute__((ext_vector_type(4))) unsigned u32x4;
typedef __attribute__((ext_vector_type(2))) unsigned u32x2;
typedef __attribute__((address_space(3))) s16x4* lds_s16x4_ptr;
typedef __attribute__((address_space(3))) u32x4* lds_u32x4_ptr;
typedef __attribute__((address_space(3))) u32x2* lds_u32x2_ptr;
typedef __attribute__((address_space(3))) void* lds_void_ptr;

#define DI __device__ __forceinline__
#define BF(x) __builtin_bit_cast(bf16x8, (x))
#define MFMA(a, b, c) __builtin_amdgcn_mfma_f32_16x16x32_bf16(BF(a), BF(b), (c), 0, 0, 0)

#ifndef NVS
#define NVS 3
#endif
#define PRIO 3
#define ST_AUX 17

constexpr int NB_TOTAL = 4096;
constexpr int NF = 26;
constexpr int ND = 128;
constexpr int WAVES = 8;
constexpr int EPW = 4;
constexpr int BPG = WAVES * EPW;
constexpr int EBYTES = NF * ND * 2;
constexpr int LDS_BYTES = 65536 + WAVES * 12288;

DI unsigned pack2(float lo, float hi) {
  bf16x2 v;
  v[0] = (__bf16)lo;
  v[1] = (__bf16)hi;
  return __builtin_bit_cast(unsigned, v);
}
DI unsigned relu2(float lo, float hi) {
  unsigned u = pack2(lo, hi);
  asm volatile("" : "+v"(u));
  s16x2 z = {};
  return __builtin_bit_cast(unsigned, __builtin_elementwise_max(__builtin_bit_cast(s16x2, u), z));
}

DI void glds4(unsigned v0, unsigned v1, unsigned v2, unsigned v3, const void* sbase, unsigned lds_dst) {
  asm volatile(
      "s_mov_b32 m0, %5\n\ts_nop 0\n\t"
      "global_load_lds_dwordx4 %0, %4\n\t"
      "global_load_lds_dwordx4 %1, %4 offset:1024\n\t"
      "global_load_lds_dwordx4 %2, %4 offset:2048\n\t"
      "global_load_lds_dwordx4 %3, %4 offset:3072"
      :
      : "v"(v0), "v"(v1), "v"(v2), "v"(v3), "s"(sbase), "s"(lds_dst)
      : "memory", "m0");
}
DI void glds2(unsigned v0, unsigned v1, const void* sbase, unsigned lds_dst) {
  asm volatile(
      "s_mov_b32 m0, %3\n\ts_nop 0\n\t"
      "global_load_lds_dwordx4 %0, %2\n\t"
      "global_load_lds_dwordx4 %1, %2 offset:1024"
      :
      : "v"(v0), "v"(v1), "s"(sbase), "s"(lds_dst)
      : "memory", "m0");
}

#define SGB(mask, n) __builtin_amdgcn_sched_group_barrier(mask, n, 0)
#define SBAR() __builtin_amdgcn_sched_barrier(0)
#define WAITV(n) asm volatile("s_waitcnt vmcnt(" #n ")" ::: "memory");
#define WAITW(n)                  \
  WAITV(n)                        \
  __builtin_amdgcn_s_barrier();   \
  __builtin_amdgcn_sched_barrier(0);

#define STAMP(k) { asm volatile("s_memrealtime %0\n\ts_waitcnt lgkmcnt(0)" : "=s"(c.ts[k]) :: "memory"); __builtin_amdgcn_sched_barrier(0); }
#define LBAR()                                      \
  if (c.leader) __builtin_amdgcn_s_barrier();       \
  __builtin_amdgcn_sched_barrier(0);

struct Ctx {
  mutable unsigned long long ts[12];
  bool leader;
  unsigned long long* prof;
  unsigned wl;
  unsigned eread;
  unsigned ebuf;
  unsigned owr;
  unsigned ord;
  unsigned evoff[4];
  int gvoff;
  int lane, cc, g;
  const char* ebase;
  int obase;
  __amdgpu_buffer_rsrc_t rsrc;
};

struct St {
  u32x4 E[2][4];
  u32x4 W[3][4];
  u32x4 Qp[7][2], Kp[7][2];
  u32x2 Vp[7][4];
  f32x4 O[7][4];
};

DI void loadw(const Ctx& c, u32x4 (&W)[4], int n) {
  const int X = n >> 2, j = n & 3;
#pragma unroll
  for (int s = 0; s < 4; ++s) W[s] = *(lds_u32x4_ptr)(c.wl + X * 16384 + s * 4096 + j * 1024);
}
DI void relayout(const unsigned (&wb)[4], unsigned wl, int off) {
  u32x4 W[4];
#pragma unroll
  for (int j = 0; j < 4; ++j) {
    s16x4 lo = __builtin_amdgcn_ds_read_tr16_b64_v4i16((lds_s16x4_ptr)(wb[j] + off));
    s16x4 hi = __builtin_amdgcn_ds_read_tr16_b64_v4i16((lds_s16x4_ptr)(wb[j] + off + 2048));
    W[j] = __builtin_bit_cast(u32x4, __builtin_shufflevector(lo, hi, 0, 1, 2, 3, 4, 5, 6, 7));
  }
  asm volatile("s_waitcnt lgkmcnt(0)" ::: "memory");
#pragma unroll
  for (int j = 0; j < 4; ++j) *(lds_u32x4_ptr)(wl + off + j * 1024) = W[j];
  asm volatile("s_waitcnt lgkmcnt(0)" ::: "memory");
}

template <int G>
DI void dma_e(const Ctx& c) {
  const char* p = c.ebase + (size_t)G * 8192;
  if (G < 3) {
    glds4(c.evoff[0], c.evoff[1], c.evoff[2], c.evoff[3], p, c.ebuf);
    glds4(c.evoff[0], c.evoff[1], c.evoff[2], c.evoff[3], p + 4096, c.ebuf + 4096);
  } else {
    glds2(c.evoff[0], c.evoff[1], p, c.ebuf);
  }
}

template <int NT>
DI void read_e(const Ctx& c, u32x4 (&E)[2][4]) {
#pragma unroll
  for (int s = 0; s < 4; ++s) {
    const unsigned a = c.eread ^ (s << 6);
    E[0][s] = *(lds_u32x4_ptr)(a);
    if (NT > 1) E[1][s] = *(lds_u32x4_ptr)(a + 4096);
  }
}

DI void stage_out(const Ctx& c, const f32x4 (&O)[4], unsigned slot) {
#pragma unroll
  for (int j = 0; j < 4; ++j) {
    u32x2 v;
    v[0] = relu2(O[j][0], O[j][1]);
    v[1] = relu2(O[j][2], O[j][3]);
    *(lds_u32x2_ptr)((c.owr ^ (j << 5)) + slot) = v;
  }
}

DI float xmax(float m) {
  auto a = __builtin_amdgcn_permlane32_swap(__float_as_uint(m), __float_as_uint(m), false, false);
  asm("v_max_f32 %0, %1, %2" : "=v"(m) : "v"(a[0]), "v"(a[1]));
  auto b = __builtin_amdgcn_permlane16_swap(__float_as_uint(m), __float_as_uint(m), false, false);
  asm("v_max_f32 %0, %1, %2" : "=v"(m) : "v"(b[0]), "v"(b[1]));
  return m;
}
DI float xsum(float m) {
  auto a = __builtin_amdgcn_permlane32_swap(__float_as_uint(m), __float_as_uint(m), false, false);
  m = __uint_as_float(a[0]) + __uint_as_float(a[1]);
  auto b = __builtin_amdgcn_permlane16_swap(__float_as_uint(m), __float_as_uint(m), false, false);
  return __uint_as_float(b[0]) + __uint_as_float(b[1]);
}
DI u32x4 sel4(bool p, const u32x4& a, const u32x4& b) {
  u32x4 r;
#pragma unroll
  for (int i = 0; i < 4; ++i) r[i] = p ? a[i] : b[i];
  return r;
}

#define PAT(NT_, ND_, NV)                          \
  _Pragma("unroll") for (int q_ = 0; q_ < 4; ++q_) { \
    SGB(0x8, 1);                                   \
    SGB(0x100, ND_);                               \
    SGB(0x2, NV);                                  \
    if ((NT_) > 1) { SGB(0x8, 1); SGB(0x2, NV); }  \
    if ((NT_) > 2) { SGB(0x8, 1); SGB(0x2, NV); }  \
  }

struct Loc {
  f32x4 acc[2][2];
  f32x4 S[2][2];
  float sv[2][8], mx[2], sm[2];
  u32x4 Pp[2];
  u32x4 ob[4];
  u32x4 QA[2], KA[2];
  u32x2 VA[4];
};

template <int G, int n>
DI void job(const Ctx& c, St& st, Loc& L) {
  constexpr int NT = G < 3 ? 2 : 1;
  constexpr int T0 = 2 * G;
  constexpr int NM = 4 * NT;
  constexpr int NDS = 8 / NM;
  constexpr int VS = 2 / NT;
  constexpr int WB = (16 * G) % 3;
  constexpr int TA = G == 0 ? 0 : G == 1 ? 1 : G == 2 ? 3 : 4;
  constexpr int TB = G == 0 ? 1 : G == 1 ? 2 : G == 2 ? 4 : 5;
  constexpr int X = n >> 2, j = n & 3;
  const float cexp = 0.125f * 1.4426950408889634f;
  const float NINF = -__builtin_inff();
  const int g = c.g, cc = c.cc;
  if (n + 2 < 16) loadw(c, st.W[(WB + n + 2) % 3], n + 2);
  else if (G < 3) loadw(c, st.W[(WB + n + 2) % 3], n + 2 - 16);
  const u32x4(&W)[4] = st.W[(WB + n) % 3];
#pragma unroll
  for (int tt = 0; tt < NT; ++tt) L.acc[n & 1][tt] = f32x4{};
#pragma unroll
  for (int s = 0; s < 4; ++s)
#pragma unroll
    for (int tt = 0; tt < NT; ++tt)
      L.acc[n & 1][tt] = X == 2 ? MFMA(st.E[tt][s], W[s], L.acc[n & 1][tt]) : MFMA(W[s], st.E[tt][s], L.acc[n & 1][tt]);
  if (X == 3) {
#pragma unroll
    for (int tt = 0; tt < NT; ++tt) st.O[T0 + tt][j] = L.acc[n & 1][tt];
  }
  if (n > 0 && n <= 12) {
    constexpr int pX = (n - 1) >> 2, pj = (n - 1) & 3;
#pragma unroll
    for (int tt = 0; tt < NT; ++tt) {
      const f32x4 a = L.acc[(n - 1) & 1][tt];
      const unsigned lo = pack2(a[0], a[1]), hi = pack2(a[2], a[3]);
      if (pX == 0) { st.Qp[T0 + tt][pj >> 1][2 * (pj & 1)] = lo; st.Qp[T0 + tt][pj >> 1][2 * (pj & 1) + 1] = hi; }
      if (pX == 1) { st.Kp[T0 + tt][pj >> 1][2 * (pj & 1)] = lo; st.Kp[T0 + tt][pj >> 1][2 * (pj & 1) + 1] = hi; }
      if (pX == 2) { st.Vp[T0 + tt][pj][0] = lo; st.Vp[T0 + tt][pj][1] = hi; }
    }
  }
  constexpr int nv = (G > 0 && n == 0) || (G == 2 && n == 1) ? 3
                     : ((G & 1) && (n == 5 || n == 8)) ? 2
                     : (n >= 9) ? NVS : 1;
  if (G == 1 || G == 3) {
    constexpr int TO = G == 1 ? 0 : 3;
    if (n == 0) stage_out(c, st.O[TO], 0);
    if (n == 2) { L.ob[0] = *(lds_u32x4_ptr)(c.ord); L.ob[1] = *(lds_u32x4_ptr)(c.ord + 1024); }
    if (n == 4) {
      __builtin_amdgcn_raw_buffer_store_b128(L.ob[0], c.rsrc, c.gvoff, c.obase + TO * 4096, ST_AUX);
      __builtin_amdgcn_raw_buffer_store_b128(L.ob[1], c.rsrc, c.gvoff + 2048, c.obase + TO * 4096, ST_AUX);
    }
  }
  if (G == 2) {
    if (n == 0) stage_out(c, st.O[1], 0);
    if (n == 1) stage_out(c, st.O[2], 2048);
    if (n == 3) {
#pragma unroll
      for (int k = 0; k < 4; ++k) L.ob[k] = *(lds_u32x4_ptr)(c.ord + k * 1024);
    }
    if (n == 5) {
#pragma unroll
      for (int k = 0; k < 4; ++k)
        __builtin_amdgcn_raw_buffer_store_b128(L.ob[k], c.rsrc, c.gvoff + (k & 1) * 2048, c.obase + (1 + (k >> 1)) * 4096, ST_AUX);
    }
  }
  if (G == 1 || G == 3) {
    constexpr int TL = G == 1 ? 3 : 6;
    constexpr int CUT = G == 1 ? 10 : 14;
    if (n == 5) {
      L.QA[0] = sel4(cc >= CUT, st.Qp[TA][0], st.Qp[TL][0]);
      L.QA[1] = sel4(cc >= CUT, st.Qp[TA][1], st.Qp[TL][1]);
    }
    if (n == 8) {
      L.KA[0] = sel4(cc >= CUT, st.Kp[TA][0], st.Kp[TL][0]);
      L.KA[1] = sel4(cc >= CUT, st.Kp[TA][1], st.Kp[TL][1]);
    }
    if (n == 12) {
#pragma unroll
      for (int k = 0; k < 4; ++k) {
        L.VA[k][0] = (G == 1 && g == 3) ? st.Vp[TA][k][0] : st.Vp[TL][k][0];
        L.VA[k][1] = (G == 1 ? g >= 2 : g == 3) ? st.Vp[TA][k][1] : st.Vp[TL][k][1];
      }
    }
  } else {
    if (n == 5) { L.QA[0] = st.Qp[TA][0]; L.QA[1] = st.Qp[TA][1]; }
    if (n == 8) { L.KA[0] = st.Kp[TA][0]; L.KA[1] = st.Kp[TA][1]; }
    if (n == 12) {
#pragma unroll
      for (int k = 0; k < 4; ++k) L.VA[k] = st.Vp[TA][k];
    }
  }
  if (n == 9 || n == 10) {
    constexpr int qs = (n - 9) & 1;
#pragma unroll
    for (int ks = 0; ks < 2; ++ks)
#pragma unroll
      for (int i = 0; i < 2; ++i) {
        float lo = L.S[ks][qs][2 * i], hi = L.S[ks][qs][2 * i + 1];
        bool masked = false, ok = true;
        if (G == 0 && ks == 1) { masked = true; ok = i == 0 ? g < 3 : g < 2; }
        if (G == 1 && ks == 0) { masked = true; ok = i == 0 ? (g == 0 || g == 3) : g != 1; }
        if (G == 2 && ks == 0) { masked = true; ok = g >= 1; }
        if (G == 2 && ks == 1 && i == 1) { masked = true; ok = g < 3; }
        if (G == 3 && ks == 0) { masked = true; ok = i == 0 ? g < 2 : g != 2; }
        if (masked) { lo = ok ? lo : NINF; hi = ok ? hi : NINF; }
        L.sv[qs][4 * ks + 2 * i] = lo;
        L.sv[qs][4 * ks + 2 * i + 1] = hi;
      }
    float m = L.sv[qs][0];
#pragma unroll
    for (int i = 1; i < 8; ++i) m = fmaxf(m, L.sv[qs][i]);
    L.mx[qs] = xmax(m) * cexp;
  }
  if (n == 11 || n == 12) {
    constexpr int qs = (n - 11) & 1;
    float s = 0.f;
#pragma unroll
    for (int i = 0; i < 8; ++i) {
      L.sv[qs][i] = __builtin_amdgcn_exp2f(__builtin_fmaf(L.sv[qs][i], cexp, -L.mx[qs]));
      s += L.sv[qs][i];
    }
    L.sm[qs] = xsum(s);
  }
  if (n == 13 || n == 14) {
    constexpr int qs = (n - 13) & 1;
    float inv = __builtin_amdgcn_rcpf(L.sm[qs]);
    if (G == 0 && qs == 1) inv = cc < 10 ? inv : 0.f;
    if (G == 2 && qs == 0) inv = cc >= 4 ? inv : 0.f;
    if (G == 2 && qs == 1) inv = cc < 14 ? inv : 0.f;
#pragma unroll
    for (int i = 0; i < 4; ++i) L.Pp[qs][i] = pack2(L.sv[qs][2 * i] * inv, L.sv[qs][2 * i + 1] * inv);
  }
  if (n == 2 || n == 3) { PAT(NT, 2, nv * VS); }
  else { PAT(NT, 1, nv * VS); }
  if (n < 9 || n == 15) SBAR();
  if (G == 0 && (n == 1 || n == 5)) { LBAR(); }
  if (n == 8) {
    L.S[0][0] = MFMA(L.KA[0], L.QA[0], f32x4{});
    L.S[1][0] = MFMA(st.Kp[TB][0], L.QA[0], f32x4{});
    L.S[0][1] = MFMA(L.KA[0], st.Qp[TB][0], f32x4{});
    L.S[1][1] = MFMA(st.Kp[TB][0], st.Qp[TB][0], f32x4{});
    L.S[0][0] = MFMA(L.KA[1], L.QA[1], L.S[0][0]);
    L.S[1][0] = MFMA(st.Kp[TB][1], L.QA[1], L.S[1][0]);
    L.S[0][1] = MFMA(L.KA[1], st.Qp[TB][1], L.S[0][1]);
    L.S[1][1] = MFMA(st.Kp[TB][1], st.Qp[TB][1], L.S[1][1]);
    SBAR();
    if (G == 0) { LBAR(); }
  }
  if constexpr (n + 1 < 16) job<G, n + 1>(c, st, L);
}

template <int G>
DI void group_body(const Ctx& c, St& st) {
  constexpr int TA = G == 0 ? 0 : G == 1 ? 1 : G == 2 ? 3 : 4;
  constexpr int TB = G == 0 ? 1 : G == 1 ? 2 : G == 2 ? 4 : 5;
  const int cc = c.cc;
  Loc L;
  job<G, 0>(c, st, L);
  if (G < 3) {
    if (G == 0) { WAITV(0); } else if (G == 1) { WAITV(2); } else { WAITV(4); }
    SBAR();
    read_e<(G < 2 ? 2 : 1)>(c, st.E);
  }
  {
    u32x4 Vf[4];
#pragma unroll
    for (int j = 0; j < 4; ++j) {
      Vf[j][0] = L.VA[j][0]; Vf[j][1] = L.VA[j][1];
      Vf[j][2] = st.Vp[TB][j][0]; Vf[j][3] = st.Vp[TB][j][1];
    }
    const u32x4 z = {};
    if (G == 1 || G == 3) {
      constexpr int TL = G == 1 ? 3 : 6;
      constexpr int CUT = G == 1 ? 10 : 14;
      const u32x4 Ph = sel4(cc >= CUT, L.Pp[0], z);
      const u32x4 Pl = G == 1 ? sel4(cc < 4, L.Pp[0], z) : L.Pp[0];
#pragma unroll
      for (int j = 0; j < 4; ++j) st.O[TB][j] = MFMA(Vf[j], L.Pp[1], st.O[TB][j]);
#pragma unroll
      for (int j = 0; j < 4; ++j) st.O[TA][j] = MFMA(Vf[j], Ph, st.O[TA][j]);
#pragma unroll
      for (int j = 0; j < 4; ++j) st.O[TL][j] = MFMA(Vf[j], Pl, st.O[TL][j]);
    } else {
#pragma unroll
      for (int j = 0; j < 4; ++j) st.O[TA][j] = MFMA(Vf[j], L.Pp[0], st.O[TA][j]);
#pragma unroll
      for (int j = 0; j < 4; ++j) st.O[TB][j] = MFMA(Vf[j], L.Pp[1], st.O[TB][j]);
    }
  }
  SBAR();
  if (G < 2) {
    asm volatile("s_waitcnt lgkmcnt(0)" ::: "memory");
    dma_e<G + 2>(c);
    SBAR();
  }
}

__global__ __launch_bounds__(WAVES * 64, 2) void autoint_kernel(
    const __bf16* __restrict__ e, const __bf16* __restrict__ wq, const __bf16* __restrict__ wk,
    const __bf16* __restrict__ wv, const __bf16* __restrict__ wres, __bf16* __restrict__ out, unsigned long long* prof) {
  extern __shared__ __attribute__((aligned(16))) char smem[];
  const int tid = threadIdx.x;
  const int lane = tid & 63;
  const int wave = __builtin_amdgcn_readfirstlane(tid >> 6);
  const int cc = lane & 15, g = lane >> 4;
  const unsigned lds0 = __builtin_amdgcn_readfirstlane((unsigned)(size_t)(lds_void_ptr)smem);
  const int bid = blockIdx.x;
  const int hd = (bid >> 3) & 1;
  const int grp = (bid & 7) | ((bid >> 4) << 3);
  const int b0 = grp * BPG + wave * EPW;

  Ctx c;
  c.lane = lane;
  c.cc = cc;
  c.g = g;
  c.prof = prof + (size_t)(blockIdx.x * WAVES + wave) * 32;
  for (int k = 0; k < 12; ++k) c.ts[k] = 0;
  STAMP(0);
  c.ebuf = lds0 + 65536 + wave * 12288;
  c.ebase = (const char*)e + (size_t)b0 * EBYTES;
  c.obase = b0 * EBYTES;
  {
    const int c0 = cc ^ g;
#pragma unroll
    for (int j = 0; j < 4; ++j) c.evoff[j] = (unsigned)((g * 256 + (c0 << 4)) ^ (j << 6));
  }
  const bool leader = wave < 4;
  c.leader = leader;
  if (wave == CODEPF) {
    unsigned long long pc;
    asm volatile("s_getpc_b64 %0" : "=s"(pc));
    const unsigned t0 = lane * 128, t1 = t0 + 8192, t2 = 16384 + (lane < CPFN ? lane : CPFN) * 128;
    asm volatile(
        "s_mov_b32 m0, %4\n\ts_nop 0\n\t"
        "global_load_lds_dword %0, %3\n\t"
        "global_load_lds_dword %1, %3\n\t"
        "global_load_lds_dword %2, %3"
        :
        : "v"(t0), "v"(t1), "v"(t2), "s"(pc), "s"(c.ebuf + 8192)
        : "memory", "m0");
  }
  if (leader) {
    dma_e<0>(c);
  } else {
    unsigned o64[4], o128[4];
    const int r8 = lane >> 3;
    const int krow = 32 * (wave - 4) + 8 * (r8 >> 2) + (r8 & 3);
    const int ch = (lane & 7) ^ (r8 & 6);
#pragma unroll
    for (int i = 0; i < 4; ++i) {
      const int k = krow + 16 * (i & 1) + 4 * (i >> 1);
      o64[i] = (unsigned)(k * 128 + ch * 16 + 4096 - i * 1024);
      o128[i] = (unsigned)(k * 256 + ch * 16 + 4096 - i * 1024);
    }
    const char* sq = (const char*)wq + hd * 16384;
    const char* sk = (const char*)wk + hd * 16384;
    const char* sv = (const char*)wv + hd * 16384;
    const char* sr = (const char*)wres + hd * 128;
    const unsigned dst = lds0 + (wave - 4) * 4096;
    glds4(o64[0], o64[1], o64[2], o64[3], sq - 4096, dst);
    glds4(o64[0], o64[1], o64[2], o64[3], sk - 4096, dst + 16384);
    glds4(o64[0], o64[1], o64[2], o64[3], sv - 4096, dst + 32768);
    glds4(o128[0], o128[1], o128[2], o128[3], sr - 4096, dst + 49152);
    dma_e<0>(c);
  }
  SBAR();

  unsigned wb[4];
  {
    const int q4 = (lane & 15) >> 2, p4 = lane & 3;
    const int r8 = (g & 1) * 4 + q4;
#pragma unroll
    for (int j = 0; j < 4; ++j)
      wb[j] = lds0 + (wave & 3) * 4096 + (g >> 1) * 1024 + r8 * 128 + ((((2 * j) ^ (r8 & 6)) + (p4 >> 1)) << 4) + (p4 & 1) * 8;
    c.wl = lds0 + lane * 16;
    asm volatile("" : "+v"(c.wl));
    c.eread = c.ebuf + cc * 256 + ((g ^ cc) << 4);
    const unsigned obuf = c.ebuf + 8192;
    c.owr = obuf + cc * 128 + (((g >> 1) ^ (cc & 7)) << 4) + (g & 1) * 8;
    c.ord = obuf + lane * 16;
    const int orow = lane >> 3, oc = (lane & 7) ^ (orow & 7);
    c.gvoff = orow * 256 + hd * 128 + oc * 16;
    c.rsrc = __builtin_amdgcn_make_buffer_rsrc((void*)out, 0, NB_TOTAL * EBYTES, 0x00020000);
  }

  St st;
  STAMP(1);
  if (leader) {
    __builtin_amdgcn_s_barrier();
  } else {
    const unsigned wlw = c.wl + (wave & 3) * 4096;
    WAITV(20); relayout(wb, wlw, 0);     __builtin_amdgcn_s_barrier(); SBAR();
    WAITV(16); relayout(wb, wlw, 16384); __builtin_amdgcn_s_barrier(); SBAR();
    WAITV(12); relayout(wb, wlw, 32768); __builtin_amdgcn_s_barrier(); SBAR();
    WAITV(8);  relayout(wb, wlw, 49152); __builtin_amdgcn_s_barrier(); SBAR();
    __builtin_amdgcn_s_setprio(1);
  }
  WAITV(0);
  SBAR();
  STAMP(2);
  read_e<2>(c, st.E);
  loadw(c, st.W[0], 0);
  loadw(c, st.W[1], 1);
  asm volatile("s_waitcnt lgkmcnt(0)" ::: "memory");
  SBAR();
  dma_e<1>(c);
  SBAR();
  STAMP(3);

  group_body<0>(c, st);
  STAMP(4);
  group_body<1>(c, st);
  STAMP(5);
  group_body<2>(c, st);
  STAMP(6);
  __builtin_amdgcn_s_setprio(0);
  group_body<3>(c, st);
  STAMP(7);
  {
    stage_out(c, st.O[4], 0);
    stage_out(c, st.O[5], 2048);
    stage_out(c, st.O[6], (unsigned)-8192);
    u32x4 ob[5];
#pragma unroll
    for (int k = 0; k < 4; ++k) ob[k] = *(lds_u32x4_ptr)(c.ord + k * 1024);
    ob[4] = *(lds_u32x4_ptr)(c.ord - 8192);
    STAMP(9);
#pragma unroll
    for (int k = 0; k < 4; ++k)
      __builtin_amdgcn_raw_buffer_store_b128(ob[k], c.rsrc, c.gvoff + (k & 1) * 2048, c.obase + (4 + (k >> 1)) * 4096, ST_AUX);
    __builtin_amdgcn_raw_buffer_store_b128(ob[4], c.rsrc, c.gvoff, c.obase + 6 * 4096, ST_AUX);
  }
  STAMP(10);
  asm volatile("s_waitcnt vmcnt(0)" ::: "memory");
  STAMP(8);
  if (c.lane == 0) {
#pragma unroll
    for (int k = 0; k < 12; ++k) c.prof[2 * k + 1] = c.ts[k];
  }
}

__global__ void diag_kernel(const unsigned long long* prof) {
  __shared__ unsigned long long smin[256];
  __shared__ double sacc[256];
  const int t = threadIdx.x;
  unsigned long long mn = ~0ull;
  for (int i = t; i < 2048; i += 256) { unsigned long long a = prof[(size_t)i * 32 + 1]; mn = a < mn ? a : mn; }
  smin[t] = mn;
  __syncthreads();
  for (int s = 128; s > 0; s >>= 1) { if (t < s) smin[t] = smin[t] < smin[t + s] ? smin[t] : smin[t + s]; __syncthreads(); }
  mn = smin[0];
  double acc = DIAGM == 2 ? 1e30 : 0;
  for (int i = t; i < 2048; i += 256) {
    double v = (double)(prof[(size_t)i * 32 + 2 * DIAGK + 1] - mn);
    if (DIAGM == 0) acc += v; else if (DIAGM == 1) acc = v > acc ? v : acc; else acc = v < acc ? v : acc;
  }
  sacc[t] = acc;
  __syncthreads();
  for (int s = 128; s > 0; s >>= 1) {
    if (t < s) { double a = sacc[t], b = sacc[t + s]; sacc[t] = (DIAGM == 0) ? a + b : DIAGM == 1 ? (a > b ? a : b) : (a < b ? a : b); }
    __syncthreads();
  }
  double v = sacc[0];
  if (DIAGM == 0) v /= 2048;
  if (v < 0) v = 0; if (v > 20000) v = 20000;
  unsigned long long dur = (unsigned long long)(v * DIAGS);
  if (t == 0) {
    unsigned long long s0, s1;
    asm volatile("s_memrealtime %0\n\ts_waitcnt lgkmcnt(0)" : "=s"(s0) :: "memory");
    for (int it = 0; it < 1000000; ++it) {
      asm volatile("s_memrealtime %0\n\ts_waitcnt lgkmcnt(0)" : "=s"(s1) :: "memory");
      if (s1 - s0 >= dur) break;
    }
  }
}

extern "C" void kernel_launch(void* const* d_in, const int* in_sizes, int n_in, void* d_out, int out_size,
                              void* d_ws, size_t ws_size, hipStream_t stream) {
  const __bf16* e = (const __bf16*)d_in[0];
  const __bf16* wq = (const __bf16*)d_in[1];
  const __bf16* wk = (const __bf16*)d_in[2];
  const __bf16* wv = (const __bf16*)d_in[3];
  const __bf16* wres = (const __bf16*)d_in[4];
  __bf16* out = (__bf16*)d_out;
  (void)hipFuncSetAttribute((const void*)autoint_kernel, hipFuncAttributeMaxDynamicSharedMemorySize, LDS_BYTES);
  autoint_kernel<<<2 * NB_TOTAL / BPG, WAVES * 64, LDS_BYTES, stream>>>(e, wq, wk, wv, wres, out, (unsigned long long*)d_ws);
  diag_kernel<<<1, 256, 0, stream>>>((const unsigned long long*)d_ws);
}
```

```cpp
#define DIAGK 0
#define DIAGM 0
#define DIAGS 0
#define CODEPF 3
#define CPFN 7
#include <hip/hip_runtime.h>
#include <hip/hip_bf16.h>

typedef __attribute__((ext_vector_type(8))) __bf16 bf16x8;
typedef __attribute__((ext_vector_type(2))) __bf16 bf16x2;
typedef __attribute__((ext_vector_type(4))) short s16x4;
typedef __attribute__((ext_vector_type(2))) short s16x2;
typedef __attribute__((ext_vector_type(4))) float f32x4;
typedef __attribute__((ext_vector_type(4))) unsigned u32x4;
typedef __attribute__((ext_vector_type(2))) unsigned u32x2;
typedef __attribute__((address_space(3))) s16x4* lds_s16x4_ptr;
typedef __attribute__((address_space(3))) u32x4* lds_u32x4_ptr;
typedef __attribute__((address_space(3))) u32x2* lds_u32x2_ptr;
typedef __attribute__((address_space(3))) void* lds_void_ptr;

#define DI __device__ __forceinline__
#define BF(x) __builtin_bit_cast(bf16x8, (x))
#define MFMA(a, b, c) __builtin_amdgcn_mfma_f32_16x16x32_bf16(BF(a), BF(b), (c), 0, 0, 0)

#ifndef NVS
#define NVS 3
#endif
#define PRIO 3
#define ST_AUX 17

constexpr int NB_TOTAL = 4096;
constexpr int NF = 26;
constexpr int ND = 128;
constexpr int WAVES = 8;
constexpr int EPW = 4;
constexpr int BPG = WAVES * EPW;
constexpr int EBYTES = NF * ND * 2;
constexpr int LDS_BYTES = 65536 + WAVES * 12288;

DI unsigned pack2(float lo, float hi) {
  bf16x2 v;
  v[0] = (__bf16)lo;
  v[1] = (__bf16)hi;
  return __builtin_bit_cast(unsigned, v);
}
DI unsigned relu2(float lo, float hi) {
  unsigned u = pack2(lo, hi);
  asm volatile("" : "+v"(u));
  s16x2 z = {};
  return __builtin_bit_cast(unsigned, __builtin_elementwise_max(__builtin_bit_cast(s16x2, u), z));
}

DI void glds4(unsigned v0, unsigned v1, unsigned v2, unsigned v3, const void* sbase, unsigned lds_dst) {
  asm volatile(
      "s_mov_b32 m0, %5\n\ts_nop 0\n\t"
      "global_load_lds_dwordx4 %0, %4\n\t"
      "global_load_lds_dwordx4 %1, %4 offset:1024\n\t"
      "global_load_lds_dwordx4 %2, %4 offset:2048\n\t"
      "global_load_lds_dwordx4 %3, %4 offset:3072"
      :
      : "v"(v0), "v"(v1), "v"(v2), "v"(v3), "s"(sbase), "s"(lds_dst)
      : "memory", "m0");
}
DI void glds2(unsigned v0, unsigned v1, const void* sbase, unsigned lds_dst) {
  asm volatile(
      "s_mov_b32 m0, %3\n\ts_nop 0\n\t"
      "global_load_lds_dwordx4 %0, %2\n\t"
      "global_load_lds_dwordx4 %1, %2 offset:1024"
      :
      : "v"(v0), "v"(v1), "s"(sbase), "s"(lds_dst)
      : "memory", "m0");
}

#define SGB(mask, n) __builtin_amdgcn_sched_group_barrier(mask, n, 0)
#define SBAR() __builtin_amdgcn_sched_barrier(0)
#define WAITV(n) asm volatile("s_waitcnt vmcnt(" #n ")" ::: "memory");
#define WAITW(n)                  \
  WAITV(n)                        \
  __builtin_amdgcn_s_barrier();   \
  __builtin_amdgcn_sched_barrier(0);

#define STAMP(k) { asm volatile("s_memrealtime %0\n\ts_waitcnt lgkmcnt(0)" : "=s"(c.ts[k]) :: "memory"); __builtin_amdgcn_sched_barrier(0); }
#define LBAR()                                      \
  if (c.leader) __builtin_amdgcn_s_barrier();       \
  __builtin_amdgcn_sched_barrier(0);

struct Ctx {
  mutable unsigned long long ts[16];
  bool leader;
  unsigned long long* prof;
  unsigned wl;
  unsigned eread;
  unsigned ebuf;
  unsigned owr;
  unsigned ord;
  unsigned evoff[4];
  int gvoff;
  int lane, cc, g;
  const char* ebase;
  int obase;
  __amdgpu_buffer_rsrc_t rsrc;
};

struct St {
  u32x4 E[2][4];
  u32x4 W[3][4];
  u32x4 Qp[7][2], Kp[7][2];
  u32x2 Vp[7][4];
  f32x4 O[7][4];
};

DI void loadw(const Ctx& c, u32x4 (&W)[4], int n) {
  const int X = n >> 2, j = n & 3;
#pragma unroll
  for (int s = 0; s < 4; ++s) W[s] = *(lds_u32x4_ptr)(c.wl + X * 16384 + s * 4096 + j * 1024);
}
DI void relayout(const unsigned (&wb)[4], unsigned wl, int off) {
  u32x4 W[4];
#pragma unroll
  for (int j = 0; j < 4; ++j) {
    s16x4 lo = __builtin_amdgcn_ds_read_tr16_b64_v4i16((lds_s16x4_ptr)(wb[j] + off));
    s16x4 hi = __builtin_amdgcn_ds_read_tr16_b64_v4i16((lds_s16x4_ptr)(wb[j] + off + 2048));
    W[j] = __builtin_bit_cast(u32x4, __builtin_shufflevector(lo, hi, 0, 1, 2, 3, 4, 5, 6, 7));
  }
  asm volatile("s_waitcnt lgkmcnt(0)" ::: "memory");
#pragma unroll
  for (int j = 0; j < 4; ++j) *(lds_u32x4_ptr)(wl + off + j * 1024) = W[j];
  asm volatile("s_waitcnt lgkmcnt(0)" ::: "memory");
}

template <int G>
DI void dma_e(const Ctx& c) {
  const char* p = c.ebase + (size_t)G * 8192;
  if (G < 3) {
    glds4(c.evoff[0], c.evoff[1], c.evoff[2], c.evoff[3], p, c.ebuf);
    glds4(c.evoff[0], c.evoff[1], c.evoff[2], c.evoff[3], p + 4096, c.ebuf + 4096);
  } else {
    glds2(c.evoff[0], c.evoff[1], p, c.ebuf);
  }
}

template <int NT>
DI void read_e(const Ctx& c, u32x4 (&E)[2][4]) {
#pragma unroll
  for (int s = 0; s < 4; ++s) {
    const unsigned a = c.eread ^ (s << 6);
    E[0][s] = *(lds_u32x4_ptr)(a);
    if (NT > 1) E[1][s] = *(lds_u32x4_ptr)(a + 4096);
  }
}

DI void stage_out(const Ctx& c, const f32x4 (&O)[4], unsigned slot) {
#pragma unroll
  for (int j = 0; j < 4; ++j) {
    u32x2 v;
    v[0] = relu2(O[j][0], O[j][1]);
    v[1] = relu2(O[j][2], O[j][3]);
    *(lds_u32x2_ptr)((c.owr ^ (j << 5)) + slot) = v;
  }
}

DI float xmax(float m) {
  auto a = __builtin_amdgcn_permlane32_swap(__float_as_uint(m), __float_as_uint(m), false, false);
  asm("v_max_f32 %0, %1, %2" : "=v"(m) : "v"(a[0]), "v"(a[1]));
  auto b = __builtin_amdgcn_permlane16_swap(__float_as_uint(m), __float_as_uint(m), false, false);
  asm("v_max_f32 %0, %1, %2" : "=v"(m) : "v"(b[0]), "v"(b[1]));
  return m;
}
DI float xsum(float m) {
  auto a = __builtin_amdgcn_permlane32_swap(__float_as_uint(m), __float_as_uint(m), false, false);
  m = __uint_as_float(a[0]) + __uint_as_float(a[1]);
  auto b = __builtin_amdgcn_permlane16_swap(__float_as_uint(m), __float_as_uint(m), false, false);
  return __uint_as_float(b[0]) + __uint_as_float(b[1]);
}
DI u32x4 sel4(bool p, const u32x4& a, const u32x4& b) {
  u32x4 r;
#pragma unroll
  for (int i = 0; i < 4; ++i) r[i] = p ? a[i] : b[i];
  return r;
}

#define PAT(NT_, ND_, NV)                          \
  _Pragma("unroll") for (int q_ = 0; q_ < 4; ++q_) { \
    SGB(0x8, 1);                                   \
    SGB(0x100, ND_);                               \
    SGB(0x2, NV);                                  \
    if ((NT_) > 1) { SGB(0x8, 1); SGB(0x2, NV); }  \
    if ((NT_) > 2) { SGB(0x8, 1); SGB(0x2, NV); }  \
  }

struct Loc {
  f32x4 acc[2][2];
  f32x4 S[2][2];
  float sv[2][8], mx[2], sm[2];
  u32x4 Pp[2];
  u32x4 ob[4];
  u32x4 QA[2], KA[2];
  u32x2 VA[4];
};

template <int G, int n>
DI void job(const Ctx& c, St& st, Loc& L) {
  constexpr int NT = G < 3 ? 2 : 1;
  constexpr int T0 = 2 * G;
  constexpr int NM = 4 * NT;
  constexpr int NDS = 8 / NM;
  constexpr int VS = 2 / NT;
  constexpr int WB = (16 * G) % 3;
  constexpr int TA = G == 0 ? 0 : G == 1 ? 1 : G == 2 ? 3 : 4;
  constexpr int TB = G == 0 ? 1 : G == 1 ? 2 : G == 2 ? 4 : 5;
  constexpr int X = n >> 2, j = n & 3;
  const float cexp = 0.125f * 1.4426950408889634f;
  const float NINF = -__builtin_inff();
  const int g = c.g, cc = c.cc;
  if (n + 2 < 16) loadw(c, st.W[(WB + n + 2) % 3], n + 2);
  else if (G < 3) loadw(c, st.W[(WB + n + 2) % 3], n + 2 - 16);
  const u32x4(&W)[4] = st.W[(WB + n) % 3];
#pragma unroll
  for (int tt = 0; tt < NT; ++tt) L.acc[n & 1][tt] = f32x4{};
#pragma unroll
  for (int s = 0; s < 4; ++s)
#pragma unroll
    for (int tt = 0; tt < NT; ++tt)
      L.acc[n & 1][tt] = X == 2 ? MFMA(st.E[tt][s], W[s], L.acc[n & 1][tt]) : MFMA(W[s], st.E[tt][s], L.acc[n & 1][tt]);
  if (X == 3) {
#pragma unroll
    for (int tt = 0; tt < NT; ++tt) st.O[T0 + tt][j] = L.acc[n & 1][tt];
  }
  if (n > 0 && n <= 12) {
    constexpr int pX = (n - 1) >> 2, pj = (n - 1) & 3;
#pragma unroll
    for (int tt = 0; tt < NT; ++tt) {
      const f32x4 a = L.acc[(n - 1) & 1][tt];
      const unsigned lo = pack2(a[0], a[1]), hi = pack2(a[2], a[3]);
      if (pX == 0) { st.Qp[T0 + tt][pj >> 1][2 * (pj & 1)] = lo; st.Qp[T0 + tt][pj >> 1][2 * (pj & 1) + 1] = hi; }
      if (pX == 1) { st.Kp[T0 + tt][pj >> 1][2 * (pj & 1)] = lo; st.Kp[T0 + tt][pj >> 1][2 * (pj & 1) + 1] = hi; }
      if (pX == 2) { st.Vp[T0 + tt][pj][0] = lo; st.Vp[T0 + tt][pj][1] = hi; }
    }
  }
  constexpr int nv = (G > 0 && n == 0) || (G == 2 && n == 1) ? 3
                     : ((G & 1) && (n == 5 || n == 8)) ? 2
                     : (n >= 9) ? NVS : 1;
  if (G == 1 || G == 3) {
    constexpr int TO = G == 1 ? 0 : 3;
    if (n == 0) stage_out(c, st.O[TO], 0);
    if (n == 2) { L.ob[0] = *(lds_u32x4_ptr)(c.ord); L.ob[1] = *(lds_u32x4_ptr)(c.ord + 1024); }
    if (n == 4) {
      __builtin_amdgcn_raw_buffer_store_b128(L.ob[0], c.rsrc, c.gvoff, c.obase + TO * 4096, ST_AUX);
      __builtin_amdgcn_raw_buffer_store_b128(L.ob[1], c.rsrc, c.gvoff + 2048, c.obase + TO * 4096, ST_AUX);
    }
  }
  if (G == 2) {
    if (n == 0) stage_out(c, st.O[1], 0);
    if (n == 1) stage_out(c, st.O[2], 2048);
    if (n == 3) {
#pragma unroll
      for (int k = 0; k < 4; ++k) L.ob[k] = *(lds_u32x4_ptr)(c.ord + k * 1024);
    }
    if (n == 5) {
#pragma unroll
      for (int k = 0; k < 4; ++k)
        __builtin_amdgcn_raw_buffer_store_b128(L.ob[k], c.rsrc, c.gvoff + (k & 1) * 2048, c.obase + (1 + (k >> 1)) * 4096, ST_AUX);
    }
  }
  if (G == 1 || G == 3) {
    constexpr int TL = G == 1 ? 3 : 6;
    constexpr int CUT = G == 1 ? 10 : 14;
    if (n == 5) {
      L.QA[0] = sel4(cc >= CUT, st.Qp[TA][0], st.Qp[TL][0]);
      L.QA[1] = sel4(cc >= CUT, st.Qp[TA][1], st.Qp[TL][1]);
    }
    if (n == 8) {
      L.KA[0] = sel4(cc >= CUT, st.Kp[TA][0], st.Kp[TL][0]);
      L.KA[1] = sel4(cc >= CUT, st.Kp[TA][1], st.Kp[TL][1]);
    }
    if (n == 12) {
#pragma unroll
      for (int k = 0; k < 4; ++k) {
        L.VA[k][0] = (G == 1 && g == 3) ? st.Vp[TA][k][0] : st.Vp[TL][k][0];
        L.VA[k][1] = (G == 1 ? g >= 2 : g == 3) ? st.Vp[TA][k][1] : st.Vp[TL][k][1];
      }
    }
  } else {
    if (n == 5) { L.QA[0] = st.Qp[TA][0]; L.QA[1] = st.Qp[TA][1]; }
    if (n == 8) { L.KA[0] = st.Kp[TA][0]; L.KA[1] = st.Kp[TA][1]; }
    if (n == 12) {
#pragma unroll
      for (int k = 0; k < 4; ++k) L.VA[k] = st.Vp[TA][k];
    }
  }
  if (n == 9 || n == 10) {
    constexpr int qs = (n - 9) & 1;
#pragma unroll
    for (int ks = 0; ks < 2; ++ks)
#pragma unroll
      for (int i = 0; i < 2; ++i) {
        float lo = L.S[ks][qs][2 * i], hi = L.S[ks][qs][2 * i + 1];
        bool masked = false, ok = true;
        if (G == 0 && ks == 1) { masked = true; ok = i == 0 ? g < 3 : g < 2; }
        if (G == 1 && ks == 0) { masked = true; ok = i == 0 ? (g == 0 || g == 3) : g != 1; }
        if (G == 2 && ks == 0) { masked = true; ok = g >= 1; }
        if (G == 2 && ks == 1 && i == 1) { masked = true; ok = g < 3; }
        if (G == 3 && ks == 0) { masked = true; ok = i == 0 ? g < 2 : g != 2; }
        if (masked) { lo = ok ? lo : NINF; hi = ok ? hi : NINF; }
        L.sv[qs][4 * ks + 2 * i] = lo;
        L.sv[qs][4 * ks + 2 * i + 1] = hi;
      }
    float m = L.sv[qs][0];
#pragma unroll
    for (int i = 1; i < 8; ++i) m = fmaxf(m, L.sv[qs][i]);
    L.mx[qs] = xmax(m) * cexp;
  }
  if (n == 11 || n == 12) {
    constexpr int qs = (n - 11) & 1;
    float s = 0.f;
#pragma unroll
    for (int i = 0; i < 8; ++i) {
      L.sv[qs][i] = __builtin_amdgcn_exp2f(__builtin_fmaf(L.sv[qs][i], cexp, -L.mx[qs]));
      s += L.sv[qs][i];
    }
    L.sm[qs] = xsum(s);
  }
  if (n == 13 || n == 14) {
    constexpr int qs = (n - 13) & 1;
    float inv = __builtin_amdgcn_rcpf(L.sm[qs]);
    if (G == 0 && qs == 1) inv = cc < 10 ? inv : 0.f;
    if (G == 2 && qs == 0) inv = cc >= 4 ? inv : 0.f;
    if (G == 2 && qs == 1) inv = cc < 14 ? inv : 0.f;
#pragma unroll
    for (int i = 0; i < 4; ++i) L.Pp[qs][i] = pack2(L.sv[qs][2 * i] * inv, L.sv[qs][2 * i + 1] * inv);
  }
  if (n == 2 || n == 3) { PAT(NT, 2, nv * VS); }
  else { PAT(NT, 1, nv * VS); }
  if (n < 9 || n == 15) SBAR();
  if (G == 0 && (n == 1 || n == 5)) { LBAR(); }
  if (n == 8) {
    L.S[0][0] = MFMA(L.KA[0], L.QA[0], f32x4{});
    L.S[1][0] = MFMA(st.Kp[TB][0], L.QA[0], f32x4{});
    L.S[0][1] = MFMA(L.KA[0], st.Qp[TB][0], f32x4{});
    L.S[1][1] = MFMA(st.Kp[TB][0], st.Qp[TB][0], f32x4{});
    L.S[0][0] = MFMA(L.KA[1], L.QA[1], L.S[0][0]);
    L.S[1][0] = MFMA(st.Kp[TB][1], L.QA[1], L.S[1][0]);
    L.S[0][1] = MFMA(L.KA[1], st.Qp[TB][1], L.S[0][1]);
    L.S[1][1] = MFMA(st.Kp[TB][1], st.Qp[TB][1], L.S[1][1]);
    SBAR();
    if (G == 0) { LBAR(); }
  }
  if constexpr (n + 1 < 16) job<G, n + 1>(c, st, L);
}

template <int G>
DI void group_body(const Ctx& c, St& st) {
  constexpr int TA = G == 0 ? 0 : G == 1 ? 1 : G == 2 ? 3 : 4;
  constexpr int TB = G == 0 ? 1 : G == 1 ? 2 : G == 2 ? 4 : 5;
  const int cc = c.cc;
  Loc L;
  job<G, 0>(c, st, L);
  if (G < 3) {
    if (G == 0) { WAITV(0); } else if (G == 1) { WAITV(2); } else { WAITV(4); }
    SBAR();
    read_e<(G < 2 ? 2 : 1)>(c, st.E);
  }
  {
    u32x4 Vf[4];
#pragma unroll
    for (int j = 0; j < 4; ++j) {
      Vf[j][0] = L.VA[j][0]; Vf[j][1] = L.VA[j][1];
      Vf[j][2] = st.Vp[TB][j][0]; Vf[j][3] = st.Vp[TB][j][1];
    }
    const u32x4 z = {};
    if (G == 1 || G == 3) {
      constexpr int TL = G == 1 ? 3 : 6;
      constexpr int CUT = G == 1 ? 10 : 14;
      const u32x4 Ph = sel4(cc >= CUT, L.Pp[0], z);
      const u32x4 Pl = G == 1 ? sel4(cc < 4, L.Pp[0], z) : L.Pp[0];
#pragma unroll
      for (int j = 0; j < 4; ++j) st.O[TB][j] = MFMA(Vf[j], L.Pp[1], st.O[TB][j]);
#pragma unroll
      for (int j = 0; j < 4; ++j) st.O[TA][j] = MFMA(Vf[j], Ph, st.O[TA][j]);
#pragma unroll
      for (int j = 0; j < 4; ++j) st.O[TL][j] = MFMA(Vf[j], Pl, st.O[TL][j]);
    } else {
#pragma unroll
      for (int j = 0; j < 4; ++j) st.O[TA][j] = MFMA(Vf[j], L.Pp[0], st.O[TA][j]);
#pragma unroll
      for (int j = 0; j < 4; ++j) st.O[TB][j] = MFMA(Vf[j], L.Pp[1], st.O[TB][j]);
    }
  }
  SBAR();
  if (G < 2) {
    asm volatile("s_waitcnt lgkmcnt(0)" ::: "memory");
    dma_e<G + 2>(c);
    SBAR();
  }
}

__global__ __launch_bounds__(WAVES * 64, 2) void autoint_kernel(
    const __bf16* __restrict__ e, const __bf16* __restrict__ wq, const __bf16* __restrict__ wk,
    const __bf16* __restrict__ wv, const __bf16* __restrict__ wres, __bf16* __restrict__ out, unsigned long long* prof) {
  extern __shared__ __attribute__((aligned(16))) char smem[];
  const int tid = threadIdx.x;
  const int lane = tid & 63;
  const int wave = __builtin_amdgcn_readfirstlane(tid >> 6);
  const int cc = lane & 15, g = lane >> 4;
  const unsigned lds0 = __builtin_amdgcn_readfirstlane((unsigned)(size_t)(lds_void_ptr)smem);
  const int bid = blockIdx.x;
  const int hd = (bid >> 3) & 1;
  const int grp = (bid & 7) | ((bid >> 4) << 3);
  const int b0 = grp * BPG + wave * EPW;

  Ctx c;
  c.lane = lane;
  c.cc = cc;
  c.g = g;
  c.prof = prof + (size_t)(blockIdx.x * WAVES + wave) * 32;
  for (int k = 0; k < 16; ++k) c.ts[k] = 0;
  STAMP(0);
  c.ebuf = lds0 + 65536 + wave * 12288;
  c.ebase = (const char*)e + (size_t)b0 * EBYTES;
  c.obase = b0 * EBYTES;
  {
    const int c0 = cc ^ g;
#pragma unroll
    for (int j = 0; j < 4; ++j) c.evoff[j] = (unsigned)((g * 256 + (c0 << 4)) ^ (j << 6));
  }
  const bool leader = wave < 4;
  c.leader = leader;
  if (wave == CODEPF) {
    unsigned long long pc;
    asm volatile("s_getpc_b64 %0" : "=s"(pc));
    const unsigned t0 = lane * 128, t1 = t0 + 8192, t2 = 16384 + (lane < CPFN ? lane : CPFN) * 128;
    asm volatile(
        "s_mov_b32 m0, %4\n\ts_nop 0\n\t"
        "global_load_lds_dword %0, %3\n\t"
        "global_load_lds_dword %1, %3\n\t"
        "global_load_lds_dword %2, %3"
        :
        : "v"(t0), "v"(t1), "v"(t2), "s"(pc), "s"(c.ebuf + 8192)
        : "memory", "m0");
  }
  if (leader) {
    dma_e<0>(c);
  } else {
    unsigned o64[4], o128[4];
    const int r8 = lane >> 3;
    const int krow = 32 * (wave - 4) + 8 * (r8 >> 2) + (r8 & 3);
    const int ch = (lane & 7) ^ (r8 & 6);
#pragma unroll
    for (int i = 0; i < 4; ++i) {
      const int k = krow + 16 * (i & 1) + 4 * (i >> 1);
      o64[i] = (unsigned)(k * 128 + ch * 16 + 4096 - i * 1024);
      o128[i] = (unsigned)(k * 256 + ch * 16 + 4096 - i * 1024);
    }
    const char* sq = (const char*)wq + hd * 16384;
    const char* sk = (const char*)wk + hd * 16384;
    const char* sv = (const char*)wv + hd * 16384;
    const char* sr = (const char*)wres + hd * 128;
    const unsigned dst = lds0 + (wave - 4) * 4096;
    glds4(o64[0], o64[1], o64[2], o64[3], sq - 4096, dst);
    glds4(o64[0], o64[1], o64[2], o64[3], sk - 4096, dst + 16384);
    glds4(o64[0], o64[1], o64[2], o64[3], sv - 4096, dst + 32768);
    glds4(o128[0], o128[1], o128[2], o128[3], sr - 4096, dst + 49152);
    dma_e<0>(c);
  }
  SBAR();

  unsigned wb[4];
  {
    const int q4 = (lane & 15) >> 2, p4 = lane & 3;
    const int r8 = (g & 1) * 4 + q4;
#pragma unroll
    for (int j = 0; j < 4; ++j)
      wb[j] = lds0 + (wave & 3) * 4096 + (g >> 1) * 1024 + r8 * 128 + ((((2 * j) ^ (r8 & 6)) + (p4 >> 1)) << 4) + (p4 & 1) * 8;
    c.wl = lds0 + lane * 16;
    asm volatile("" : "+v"(c.wl));
    c.eread = c.ebuf + cc * 256 + ((g ^ cc) << 4);
    const unsigned obuf = c.ebuf + 8192;
    c.owr = obuf + cc * 128 + (((g >> 1) ^ (cc & 7)) << 4) + (g & 1) * 8;
    c.ord = obuf + lane * 16;
    const int orow = lane >> 3, oc = (lane & 7) ^ (orow & 7);
    c.gvoff = orow * 256 + hd * 128 + oc * 16;
    c.rsrc = __builtin_amdgcn_make_buffer_rsrc((void*)out, 0, NB_TOTAL * EBYTES, 0x00020000);
  }

  St st;
  STAMP(1);
  if (leader) {
    __builtin_amdgcn_s_barrier();
  } else {
    const unsigned wlw = c.wl + (wave & 3) * 4096;
    WAITV(20); relayout(wb, wlw, 0);     __builtin_amdgcn_s_barrier(); SBAR();
    WAITV(16); relayout(wb, wlw, 16384); __builtin_amdgcn_s_barrier(); SBAR();
    WAITV(12); relayout(wb, wlw, 32768); __builtin_amdgcn_s_barrier(); SBAR();
    WAITV(8);  relayout(wb, wlw, 49152); __builtin_amdgcn_s_barrier(); SBAR();
    __builtin_amdgcn_s_setprio(1);
  }
  WAITV(0);
  SBAR();
  STAMP(2);
  read_e<2>(c, st.E);
  loadw(c, st.W[0], 0);
  loadw(c, st.W[1], 1);
  asm volatile("s_waitcnt lgkmcnt(0)" ::: "memory");
  SBAR();
  dma_e<1>(c);
  SBAR();
  STAMP(3);

  group_body<0>(c, st);
  STAMP(4);
  group_body<1>(c, st);
  STAMP(5);
  group_body<2>(c, st);
  STAMP(6);
  __builtin_amdgcn_s_setprio(0);
  group_body<3>(c, st);
  STAMP(7);
  {
    { float x_ = st.O[6][3][0]; asm volatile("v_mov_b32 %0, %0" : "+v"(x_)); st.O[6][3][0] = x_; }
    STAMP(11);
    stage_out(c, st.O[4], 0);
    STAMP(12);
    stage_out(c, st.O[5], 2048);
    STAMP(13);
    stage_out(c, st.O[6], (unsigned)-8192);
    STAMP(14);
    u32x4 ob[5];
#pragma unroll
    for (int k = 0; k < 4; ++k) ob[k] = *(lds_u32x4_ptr)(c.ord + k * 1024);
    ob[4] = *(lds_u32x4_ptr)(c.ord - 8192);
    STAMP(9);
#pragma unroll
    for (int k = 0; k < 4; ++k)
      __builtin_amdgcn_raw_buffer_store_b128(ob[k], c.rsrc, c.gvoff + (k & 1) * 2048, c.obase + (4 + (k >> 1)) * 4096, ST_AUX);
    __builtin_amdgcn_raw_buffer_store_b128(ob[4], c.rsrc, c.gvoff, c.obase + 6 * 4096, ST_AUX);
  }
  STAMP(10);
  asm volatile("s_waitcnt vmcnt(0)" ::: "memory");
  STAMP(8);
  if (c.lane == 0) {
#pragma unroll
    for (int k = 0; k < 16; ++k) c.prof[2 * k + 1] = c.ts[k];
  }
}

__global__ void diag_kernel(const unsigned long long* prof) {
  __shared__ unsigned long long smin[256];
  __shared__ double sacc[256];
  const int t = threadIdx.x;
  unsigned long long mn = ~0ull;
  for (int i = t; i < 2048; i += 256) { unsigned long long a = prof[(size_t)i * 32 + 1]; mn = a < mn ? a : mn; }
  smin[t] = mn;
  __syncthreads();
  for (int s = 128; s > 0; s >>= 1) { if (t < s) smin[t] = smin[t] < smin[t + s] ? smin[t] : smin[t + s]; __syncthreads(); }
  mn = smin[0];
  double acc = DIAGM == 2 ? 1e30 : 0;
  for (int i = t; i < 2048; i += 256) {
    double v = (double)(prof[(size_t)i * 32 + 2 * DIAGK + 1] - mn);
    if (DIAGM == 0) acc += v; else if (DIAGM == 1) acc = v > acc ? v : acc; else acc = v < acc ? v : acc;
  }
  sacc[t] = acc;
  __syncthreads();
  for (int s = 128; s > 0; s >>= 1) {
    if (t < s) { double a = sacc[t], b = sacc[t + s]; sacc[t] = (DIAGM == 0) ? a + b : DIAGM == 1 ? (a > b ? a : b) : (a < b ? a : b); }
    __syncthreads();
  }
  double v = sacc[0];
  if (DIAGM == 0) v /= 2048;
  if (v < 0) v = 0; if (v > 20000) v = 20000;
  unsigned long long dur = (unsigned long long)(v * DIAGS);
  if (t == 0) {
    unsigned long long s0, s1;
    asm volatile("s_memrealtime %0\n\ts_waitcnt lgkmcnt(0)" : "=s"(s0) :: "memory");
    for (int it = 0; it < 1000000; ++it) {
      asm volatile("s_memrealtime %0\n\ts_waitcnt lgkmcnt(0)" : "=s"(s1) :: "memory");
      if (s1 - s0 >= dur) break;
    }
  }
}

extern "C" void kernel_launch(void* const* d_in, const int* in_sizes, int n_in, void* d_out, int out_size,
                              void* d_ws, size_t ws_size, hipStream_t stream) {
  const __bf16* e = (const __bf16*)d_in[0];
  const __bf16* wq = (const __bf16*)d_in[1];
  const __bf16* wk = (const __bf16*)d_in[2];
  const __bf16* wv = (const __bf16*)d_in[3];
  const __bf16* wres = (const __bf16*)d_in[4];
  __bf16* out = (__bf16*)d_out;
  (void)hipFuncSetAttribute((const void*)autoint_kernel, hipFuncAttributeMaxDynamicSharedMemorySize, LDS_BYTES);
  autoint_kernel<<<2 * NB_TOTAL / BPG, WAVES * 64, LDS_BYTES, stream>>>(e, wq, wk, wv, wres, out, (unsigned long long*)d_ws);
  diag_kernel<<<1, 256, 0, stream>>>((const unsigned long long*)d_ws);
}
```

```cpp
#define DIAGM 0
#define DIAGS 0
#define CODEPF 3
#define CPFN 7
#include <hip/hip_runtime.h>
#include <hip/hip_bf16.h>

typedef __attribute__((ext_vector_type(8))) __bf16 bf16x8;
typedef __attribute__((ext_vector_type(2))) __bf16 bf16x2;
typedef __attribute__((ext_vector_type(4))) short s16x4;
typedef __attribute__((ext_vector_type(2))) short s16x2;
typedef __attribute__((ext_vector_type(4))) float f32x4;
typedef __attribute__((ext_vector_type(4))) unsigned u32x4;
typedef __attribute__((ext_vector_type(2))) unsigned u32x2;
typedef __attribute__((address_space(3))) s16x4* lds_s16x4_ptr;
typedef __attribute__((address_space(3))) u32x4* lds_u32x4_ptr;
typedef __attribute__((address_space(3))) u32x2* lds_u32x2_ptr;
typedef __attribute__((address_space(3))) void* lds_void_ptr;

#define DI __device__ __forceinline__
#define BF(x) __builtin_bit_cast(bf16x8, (x))
#define MFMA(a, b, c) __builtin_amdgcn_mfma_f32_16x16x32_bf16(BF(a), BF(b), (c), 0, 0, 0)

#ifndef NVS
#define NVS 3
#endif
#define PRIO 3
#define ST_AUX 17

constexpr int NB_TOTAL = 4096;
constexpr int NF = 26;
constexpr int ND = 128;
constexpr int WAVES = 8;
constexpr int EPW = 4;
constexpr int BPG = WAVES * EPW;
constexpr int EBYTES = NF * ND * 2;
constexpr int LDS_BYTES = 65536 + WAVES * 12288;

DI unsigned pack2(float lo, float hi) {
  bf16x2 v;
  v[0] = (__bf16)lo;
  v[1] = (__bf16)hi;
  return __builtin_bit_cast(unsigned, v);
}
DI unsigned relu2(float lo, float hi) {
  unsigned u = pack2(lo, hi);
  asm volatile("" : "+v"(u));
  s16x2 z = {};
  return __builtin_bit_cast(unsigned, __builtin_elementwise_max(__builtin_bit_cast(s16x2, u), z));
}

DI void glds4(unsigned v0, unsigned v1, unsigned v2, unsigned v3, const void* sbase, unsigned lds_dst) {
  asm volatile(
      "s_mov_b32 m0, %5\n\ts_nop 0\n\t"
      "global_load_lds_dwordx4 %0, %4\n\t"
      "global_load_lds_dwordx4 %1, %4 offset:1024\n\t"
      "global_load_lds_dwordx4 %2, %4 offset:2048\n\t"
      "global_load_lds_dwordx4 %3, %4 offset:3072"
      :
      : "v"(v0), "v"(v1), "v"(v2), "v"(v3), "s"(sbase), "s"(lds_dst)
      : "memory", "m0");
}
DI void glds2(unsigned v0, unsigned v1, const void* sbase, unsigned lds_dst) {
  asm volatile(
      "s_mov_b32 m0, %3\n\ts_nop 0\n\t"
      "global_load_lds_dwordx4 %0, %2\n\t"
      "global_load_lds_dwordx4 %1, %2 offset:1024"
      :
      : "v"(v0), "v"(v1), "s"(sbase), "s"(lds_dst)
      : "memory", "m0");
}

#define SGB(mask, n) __builtin_amdgcn_sched_group_barrier(mask, n, 0)
#define SBAR() __builtin_amdgcn_sched_barrier(0)
#define WAITV(n) asm volatile("s_waitcnt vmcnt(" #n ")" ::: "memory");
#define WAITW(n)                  \
  WAITV(n)                        \
  __builtin_amdgcn_s_barrier();   \
  __builtin_amdgcn_sched_barrier(0);

#define STAMP(k) if (k == 0 || k == 2 || k == 7 || k == 8) { asm volatile("s_memrealtime %0\n\ts_waitcnt lgkmcnt(0)" : "=s"(c.ts[k == 0 ? 0 : k == 2 ? 1 : k == 7 ? 2 : 3]) :: "memory"); __builtin_amdgcn_sched_barrier(0); }
#define LBAR()                                      \
  if (c.leader) __builtin_amdgcn_s_barrier();       \
  __builtin_amdgcn_sched_barrier(0);

struct Ctx {
  mutable unsigned long long ts[4];
  bool leader;
  unsigned long long* prof;
  unsigned wl;
  unsigned eread;
  unsigned ebuf;
  unsigned owr;
  unsigned ord;
  unsigned evoff[4];
  int gvoff;
  int lane, cc, g;
  const char* ebase;
  int obase;
  __amdgpu_buffer_rsrc_t rsrc;
};

struct St {
  u32x4 E[2][4];
  u32x4 W[3][4];
  u32x4 Qp[7][2], Kp[7][2];
  u32x2 Vp[7][4];
  f32x4 O[7][4];
};

DI void loadw(const Ctx& c, u32x4 (&W)[4], int n) {
  const int X = n >> 2, j = n & 3;
#pragma unroll
  for (int s = 0; s < 4; ++s) W[s] = *(lds_u32x4_ptr)(c.wl + X * 16384 + s * 4096 + j * 1024);
}
DI void relayout(const unsigned (&wb)[4], unsigned wl, int off) {
  u32x4 W[4];
#pragma unroll
  for (int j = 0; j < 4; ++j) {
    s16x4 lo = __builtin_amdgcn_ds_read_tr16_b64_v4i16((lds_s16x4_ptr)(wb[j] + off));
    s16x4 hi = __builtin_amdgcn_ds_read_tr16_b64_v4i16((lds_s16x4_ptr)(wb[j] + off + 2048));
    W[j] = __builtin_bit_cast(u32x4, __builtin_shufflevector(lo, hi, 0, 1, 2, 3, 4, 5, 6, 7));
  }
  asm volatile("s_waitcnt lgkmcnt(0)" ::: "memory");
#pragma unroll
  for (int j = 0; j < 4; ++j) *(lds_u32x4_ptr)(wl + off + j * 1024) = W[j];
  asm volatile("s_waitcnt lgkmcnt(0)" ::: "memory");
}

template <int G>
DI void dma_e(const Ctx& c) {
  const char* p = c.ebase + (size_t)G * 8192;
  if (G < 3) {
    glds4(c.evoff[0], c.evoff[1], c.evoff[2], c.evoff[3], p, c.ebuf);
    glds4(c.evoff[0], c.evoff[1], c.evoff[2], c.evoff[3], p + 4096, c.ebuf + 4096);
  } else {
    glds2(c.evoff[0], c.evoff[1], p, c.ebuf);
  }
}

template <int NT>
DI void read_e(const Ctx& c, u32x4 (&E)[2][4]) {
#pragma unroll
  for (int s = 0; s < 4; ++s) {
    const unsigned a = c.eread ^ (s << 6);
    E[0][s] = *(lds_u32x4_ptr)(a);
    if (NT > 1) E[1][s] = *(lds_u32x4_ptr)(a + 4096);
  }
}

DI void stage_out(const Ctx& c, const f32x4 (&O)[4], unsigned slot) {
#pragma unroll
  for (int j = 0; j < 4; ++j) {
    u32x2 v;
    v[0] = relu2(O[j][0], O[j][1]);
    v[1] = relu2(O[j][2], O[j][3]);
    *(lds_u32x2_ptr)((c.owr ^ (j << 5)) + slot) = v;
  }
}

DI float xmax(float m) {
  auto a = __builtin_amdgcn_permlane32_swap(__float_as_uint(m), __float_as_uint(m), false, false);
  asm("v_max_f32 %0, %1, %2" : "=v"(m) : "v"(a[0]), "v"(a[1]));
  auto b = __builtin_amdgcn_permlane16_swap(__float_as_uint(m), __float_as_uint(m), false, false);
  asm("v_max_f32 %0, %1, %2" : "=v"(m) : "v"(b[0]), "v"(b[1]));
  return m;
}
DI float xsum(float m) {
  auto a = __builtin_amdgcn_permlane32_swap(__float_as_uint(m), __float_as_uint(m), false, false);
  m = __uint_as_float(a[0]) + __uint_as_float(a[1]);
  auto b = __builtin_amdgcn_permlane16_swap(__float_as_uint(m), __float_as_uint(m), false, false);
  return __uint_as_float(b[0]) + __uint_as_float(b[1]);
}
DI u32x4 sel4(bool p, const u32x4& a, const u32x4& b) {
  u32x4 r;
#pragma unroll
  for (int i = 0; i < 4; ++i) r[i] = p ? a[i] : b[i];
  return r;
}

#define PAT(NT_, ND_, NV)                          \
  _Pragma("unroll") for (int q_ = 0; q_ < 4; ++q_) { \
    SGB(0x8, 1);                                   \
    SGB(0x100, ND_);                               \
    SGB(0x2, NV);                                  \
    if ((NT_) > 1) { SGB(0x8, 1); SGB(0x2, NV); }  \
    if ((NT_) > 2) { SGB(0x8, 1); SGB(0x2, NV); }  \
  }

struct Loc {
  f32x4 acc[2][2];
  f32x4 S[2][2];
  float sv[2][8], mx[2], sm[2];
  u32x4 Pp[2];
  u32x4 ob[4];
  u32x4 QA[2], KA[2];
  u32x2 VA[4];
};

template <int G, int n>
DI void job(const Ctx& c, St& st, Loc& L) {
  constexpr int NT = G < 3 ? 2 : 1;
  constexpr int T0 = 2 * G;
  constexpr int NM = 4 * NT;
  constexpr int NDS = 8 / NM;
  constexpr int VS = 2 / NT;
  constexpr int WB = (16 * G) % 3;
  constexpr int TA = G == 0 ? 0 : G == 1 ? 1 : G == 2 ? 3 : 4;
  constexpr int TB = G == 0 ? 1 : G == 1 ? 2 : G == 2 ? 4 : 5;
  constexpr int X = n >> 2, j = n & 3;
  const float cexp = 0.125f * 1.4426950408889634f;
  const float NINF = -__builtin_inff();
  const int g = c.g, cc = c.cc;
  if (n + 2 < 16) loadw(c, st.W[(WB + n + 2) % 3], n + 2);
  else if (G < 3) loadw(c, st.W[(WB + n + 2) % 3], n + 2 - 16);
  const u32x4(&W)[4] = st.W[(WB + n) % 3];
#pragma unroll
  for (int tt = 0; tt < NT; ++tt) L.acc[n & 1][tt] = f32x4{};
#pragma unroll
  for (int s = 0; s < 4; ++s)
#pragma unroll
    for (int tt = 0; tt < NT; ++tt)
      L.acc[n & 1][tt] = X == 2 ? MFMA(st.E[tt][s], W[s], L.acc[n & 1][tt]) : MFMA(W[s], st.E[tt][s], L.acc[n & 1][tt]);
  if (X == 3) {
#pragma unroll
    for (int tt = 0; tt < NT; ++tt) st.O[T0 + tt][j] = L.acc[n & 1][tt];
  }
  if (n > 0 && n <= 12) {
    constexpr int pX = (n - 1) >> 2, pj = (n - 1) & 3;
#pragma unroll
    for (int tt = 0; tt < NT; ++tt) {
      const f32x4 a = L.acc[(n - 1) & 1][tt];
      const unsigned lo = pack2(a[0], a[1]), hi = pack2(a[2], a[3]);
      if (pX == 0) { st.Qp[T0 + tt][pj >> 1][2 * (pj & 1)] = lo; st.Qp[T0 + tt][pj >> 1][2 * (pj & 1) + 1] = hi; }
      if (pX == 1) { st.Kp[T0 + tt][pj >> 1][2 * (pj & 1)] = lo; st.Kp[T0 + tt][pj >> 1][2 * (pj & 1) + 1] = hi; }
      if (pX == 2) { st.Vp[T0 + tt][pj][0] = lo; st.Vp[T0 + tt][pj][1] = hi; }
    }
  }
  constexpr int nv = (G > 0 && n == 0) || (G == 2 && n == 1) ? 3
                     : ((G & 1) && (n == 5 || n == 8)) ? 2
                     : (n >= 9) ? NVS : 1;
  if (G == 1 || G == 3) {
    constexpr int TO = G == 1 ? 0 : 3;
    if (n == 0) stage_out(c, st.O[TO], 0);
    if (n == 2) { L.ob[0] = *(lds_u32x4_ptr)(c.ord); L.ob[1] = *(lds_u32x4_ptr)(c.ord + 1024); }
    if (n == 4) {
      __builtin_amdgcn_raw_buffer_store_b128(L.ob[0], c.rsrc, c.gvoff, c.obase + TO * 4096, ST_AUX);
      __builtin_amdgcn_raw_buffer_store_b128(L.ob[1], c.rsrc, c.gvoff + 2048, c.obase + TO * 4096, ST_AUX);
    }
  }
  if (G == 2) {
    if (n == 0) stage_out(c, st.O[1], 0);
    if (n == 1) stage_out(c, st.O[2], 2048);
    if (n == 3) {
#pragma unroll
      for (int k = 0; k < 4; ++k) L.ob[k] = *(lds_u32x4_ptr)(c.ord + k * 1024);
    }
    if (n == 5) {
#pragma unroll
      for (int k = 0; k < 4; ++k)
        __builtin_amdgcn_raw_buffer_store_b128(L.ob[k], c.rsrc, c.gvoff + (k & 1) * 2048, c.obase + (1 + (k >> 1)) * 4096, ST_AUX);
    }
  }
  if (G == 1 || G == 3) {
    constexpr int TL = G == 1 ? 3 : 6;
    constexpr int CUT = G == 1 ? 10 : 14;
    if (n == 5) {
      L.QA[0] = sel4(cc >= CUT, st.Qp[TA][0], st.Qp[TL][0]);
      L.QA[1] = sel4(cc >= CUT, st.Qp[TA][1], st.Qp[TL][1]);
    }
    if (n == 8) {
      L.KA[0] = sel4(cc >= CUT, st.Kp[TA][0], st.Kp[TL][0]);
      L.KA[1] = sel4(cc >= CUT, st.Kp[TA][1], st.Kp[TL][1]);
    }
    if (n == 12) {
#pragma unroll
      for (int k = 0; k < 4; ++k) {
        L.VA[k][0] = (G == 1 && g == 3) ? st.Vp[TA][k][0] : st.Vp[TL][k][0];
        L.VA[k][1] = (G == 1 ? g >= 2 : g == 3) ? st.Vp[TA][k][1] : st.Vp[TL][k][1];
      }
    }
  } else {
    if (n == 5) { L.QA[0] = st.Qp[TA][0]; L.QA[1] = st.Qp[TA][1]; }
    if (n == 8) { L.KA[0] = st.Kp[TA][0]; L.KA[1] = st.Kp[TA][1]; }
    if (n == 12) {
#pragma unroll
      for (int k = 0; k < 4; ++k) L.VA[k] = st.Vp[TA][k];
    }
  }
  if (n == 9 || n == 10) {
    constexpr int qs = (n - 9) & 1;
#pragma unroll
    for (int ks = 0; ks < 2; ++ks)
#pragma unroll
      for (int i = 0; i < 2; ++i) {
        float lo = L.S[ks][qs][2 * i], hi = L.S[ks][qs][2 * i + 1];
        bool masked = false, ok = true;
        if (G == 0 && ks == 1) { masked = true; ok = i == 0 ? g < 3 : g < 2; }
        if (G == 1 && ks == 0) { masked = true; ok = i == 0 ? (g == 0 || g == 3) : g != 1; }
        if (G == 2 && ks == 0) { masked = true; ok = g >= 1; }
        if (G == 2 && ks == 1 && i == 1) { masked = true; ok = g < 3; }
        if (G == 3 && ks == 0) { masked = true; ok = i == 0 ? g < 2 : g != 2; }
        if (masked) { lo = ok ? lo : NINF; hi = ok ? hi : NINF; }
        L.sv[qs][4 * ks + 2 * i] = lo;
        L.sv[qs][4 * ks + 2 * i + 1] = hi;
      }
    float m = L.sv[qs][0];
#pragma unroll
    for (int i = 1; i < 8; ++i) m = fmaxf(m, L.sv[qs][i]);
    L.mx[qs] = xmax(m) * cexp;
  }
  if (n == 11 || n == 12) {
    constexpr int qs = (n - 11) & 1;
    float s = 0.f;
#pragma unroll
    for (int i = 0; i < 8; ++i) {
      L.sv[qs][i] = __builtin_amdgcn_exp2f(__builtin_fmaf(L.sv[qs][i], cexp, -L.mx[qs]));
      s += L.sv[qs][i];
    }
    L.sm[qs] = xsum(s);
  }
  if (n == 13 || n == 14) {
    constexpr int qs = (n - 13) & 1;
    float inv = __builtin_amdgcn_rcpf(L.sm[qs]);
    if (G == 0 && qs == 1) inv = cc < 10 ? inv : 0.f;
    if (G == 2 && qs == 0) inv = cc >= 4 ? inv : 0.f;
    if (G == 2 && qs == 1) inv = cc < 14 ? inv : 0.f;
#pragma unroll
    for (int i = 0; i < 4; ++i) L.Pp[qs][i] = pack2(L.sv[qs][2 * i] * inv, L.sv[qs][2 * i + 1] * inv);
  }
  if (n == 2 || n == 3) { PAT(NT, 2, nv * VS); }
  else { PAT(NT, 1, nv * VS); }
  if (n < 9 || n == 15) SBAR();
  if (G == 0 && (n == 1 || n == 5)) { LBAR(); }
  if (n == 8) {
    L.S[0][0] = MFMA(L.KA[0], L.QA[0], f32x4{});
    L.S[1][0] = MFMA(st.Kp[TB][0], L.QA[0], f32x4{});
    L.S[0][1] = MFMA(L.KA[0], st.Qp[TB][0], f32x4{});
    L.S[1][1] = MFMA(st.Kp[TB][0], st.Qp[TB][0], f32x4{});
    L.S[0][0] = MFMA(L.KA[1], L.QA[1], L.S[0][0]);
    L.S[1][0] = MFMA(st.Kp[TB][1], L.QA[1], L.S[1][0]);
    L.S[0][1] = MFMA(L.KA[1], st.Qp[TB][1], L.S[0][1]);
    L.S[1][1] = MFMA(st.Kp[TB][1], st.Qp[TB][1], L.S[1][1]);
    SBAR();
    if (G == 0) { LBAR(); }
  }
  if constexpr (n + 1 < 16) job<G, n + 1>(c, st, L);
}

template <int G>
DI void group_body(const Ctx& c, St& st) {
  constexpr int TA = G == 0 ? 0 : G == 1 ? 1 : G == 2 ? 3 : 4;
  constexpr int TB = G == 0 ? 1 : G == 1 ? 2 : G == 2 ? 4 : 5;
  const int cc = c.cc;
  Loc L;
  job<G, 0>(c, st, L);
  if (G < 3) {
    if (G == 0) { WAITV(0); } else if (G == 1) { WAITV(2); } else { WAITV(4); }
    SBAR();
    read_e<(G < 2 ? 2 : 1)>(c, st.E);
  }
  {
    u32x4 Vf[4];
#pragma unroll
    for (int j = 0; j < 4; ++j) {
      Vf[j][0] = L.VA[j][0]; Vf[j][1] = L.VA[j][1];
      Vf[j][2] = st.Vp[TB][j][0]; Vf[j][3] = st.Vp[TB][j][1];
    }
    const u32x4 z = {};
    if (G == 1 || G == 3) {
      constexpr int TL = G == 1 ? 3 : 6;
      constexpr int CUT = G == 1 ? 10 : 14;
      const u32x4 Ph = sel4(cc >= CUT, L.Pp[0], z);
      const u32x4 Pl = G == 1 ? sel4(cc < 4, L.Pp[0], z) : L.Pp[0];
#pragma unroll
      for (int j = 0; j < 4; ++j) st.O[TB][j] = MFMA(Vf[j], L.Pp[1], st.O[TB][j]);
#pragma unroll
      for (int j = 0; j < 4; ++j) st.O[TA][j] = MFMA(Vf[j], Ph, st.O[TA][j]);
#pragma unroll
      for (int j = 0; j < 4; ++j) st.O[TL][j] = MFMA(Vf[j], Pl, st.O[TL][j]);
    } else {
#pragma unroll
      for (int j = 0; j < 4; ++j) st.O[TA][j] = MFMA(Vf[j], L.Pp[0], st.O[TA][j]);
#pragma unroll
      for (int j = 0; j < 4; ++j) st.O[TB][j] = MFMA(Vf[j], L.Pp[1], st.O[TB][j]);
    }
  }
  SBAR();
  if (G < 2) {
    asm volatile("s_waitcnt lgkmcnt(0)" ::: "memory");
    dma_e<G + 2>(c);
    SBAR();
  }
}

__global__ __launch_bounds__(WAVES * 64, 2) void autoint_kernel(
    const __bf16* __restrict__ e, const __bf16* __restrict__ wq, const __bf16* __restrict__ wk,
    const __bf16* __restrict__ wv, const __bf16* __restrict__ wres, __bf16* __restrict__ out, unsigned long long* prof) {
  extern __shared__ __attribute__((aligned(16))) char smem[];
  const int tid = threadIdx.x;
  const int lane = tid & 63;
  const int wave = __builtin_amdgcn_readfirstlane(tid >> 6);
  const int cc = lane & 15, g = lane >> 4;
  const unsigned lds0 = __builtin_amdgcn_readfirstlane((unsigned)(size_t)(lds_void_ptr)smem);
  const int bid = blockIdx.x;
  const int hd = (bid >> 3) & 1;
  const int grp = (bid & 7) | ((bid >> 4) << 3);
  const int b0 = grp * BPG + wave * EPW;

  Ctx c;
  c.lane = lane;
  c.cc = cc;
  c.g = g;
  c.prof = prof + (size_t)(blockIdx.x * WAVES + wave) * 4;
  STAMP(0);
  c.ebuf = lds0 + 65536 + wave * 12288;
  c.ebase = (const char*)e + (size_t)b0 * EBYTES;
  c.obase = b0 * EBYTES;
  {
    const int c0 = cc ^ g;
#pragma unroll
    for (int j = 0; j < 4; ++j) c.evoff[j] = (unsigned)((g * 256 + (c0 << 4)) ^ (j << 6));
  }
  const bool leader = wave < 4;
  c.leader = leader;
  if (wave == CODEPF) {
    unsigned long long pc;
    asm volatile("s_getpc_b64 %0" : "=s"(pc));
    const unsigned t0 = lane * 128, t1 = t0 + 8192, t2 = 16384 + (lane < CPFN ? lane : CPFN) * 128;
    asm volatile(
        "s_mov_b32 m0, %4\n\ts_nop 0\n\t"
        "global_load_lds_dword %0, %3\n\t"
        "global_load_lds_dword %1, %3\n\t"
        "global_load_lds_dword %2, %3"
        :
        : "v"(t0), "v"(t1), "v"(t2), "s"(pc), "s"(c.ebuf + 8192)
        : "memory", "m0");
  }
  if (leader) {
    dma_e<0>(c);
  } else {
    unsigned o64[4], o128[4];
    const int r8 = lane >> 3;
    const int krow = 32 * (wave - 4) + 8 * (r8 >> 2) + (r8 & 3);
    const int ch = (lane & 7) ^ (r8 & 6);
#pragma unroll
    for (int i = 0; i < 4; ++i) {
      const int k = krow + 16 * (i & 1) + 4 * (i >> 1);
      o64[i] = (unsigned)(k * 128 + ch * 16 + 4096 - i * 1024);
      o128[i] = (unsigned)(k * 256 + ch * 16 + 4096 - i * 1024);
    }
    const char* sq = (const char*)wq + hd * 16384;
    const char* sk = (const char*)wk + hd * 16384;
    const char* sv = (const char*)wv + hd * 16384;
    const char* sr = (const char*)wres + hd * 128;
    const unsigned dst = lds0 + (wave - 4) * 4096;
    glds4(o64[0], o64[1], o64[2], o64[3], sq - 4096, dst);
    glds4(o64[0], o64[1], o64[2], o64[3], sk - 4096, dst + 16384);
    glds4(o64[0], o64[1], o64[2], o64[3], sv - 4096, dst + 32768);
    glds4(o128[0], o128[1], o128[2], o128[3], sr - 4096, dst + 49152);
    dma_e<0>(c);
  }
  SBAR();

  unsigned wb[4];
  {
    const int q4 = (lane & 15) >> 2, p4 = lane & 3;
    const int r8 = (g & 1) * 4 + q4;
#pragma unroll
    for (int j = 0; j < 4; ++j)
      wb[j] = lds0 + (wave & 3) * 4096 + (g >> 1) * 1024 + r8 * 128 + ((((2 * j) ^ (r8 & 6)) + (p4 >> 1)) << 4) + (p4 & 1) * 8;
    c.wl = lds0 + lane * 16;
    asm volatile("" : "+v"(c.wl));
    c.eread = c.ebuf + cc * 256 + ((g ^ cc) << 4);
    const unsigned obuf = c.ebuf + 8192;
    c.owr = obuf + cc * 128 + (((g >> 1) ^ (cc & 7)) << 4) + (g & 1) * 8;
    c.ord = obuf + lane * 16;
    const int orow = lane >> 3, oc = (lane & 7) ^ (orow & 7);
    c.gvoff = orow * 256 + hd * 128 + oc * 16;
    c.rsrc = __builtin_amdgcn_make_buffer_rsrc((void*)out, 0, NB_TOTAL * EBYTES, 0x00020000);
  }

  St st;
  STAMP(1);
  if (leader) {
    __builtin_amdgcn_s_barrier();
  } else {
    const unsigned wlw = c.wl + (wave & 3) * 4096;
    WAITV(20); relayout(wb, wlw, 0);     __builtin_amdgcn_s_barrier(); SBAR();
    WAITV(16); relayout(wb, wlw, 16384); __builtin_amdgcn_s_barrier(); SBAR();
    WAITV(12); relayout(wb, wlw, 32768); __builtin_amdgcn_s_barrier(); SBAR();
    WAITV(8);  relayout(wb, wlw, 49152); __builtin_amdgcn_s_barrier(); SBAR();
    __builtin_amdgcn_s_setprio(1);
  }
  WAITV(0);
  SBAR();
  STAMP(2);
  read_e<2>(c, st.E);
  loadw(c, st.W[0], 0);
  loadw(c, st.W[1], 1);
  asm volatile("s_waitcnt lgkmcnt(0)" ::: "memory");
  SBAR();
  dma_e<1>(c);
  SBAR();
  STAMP(3);

  group_body<0>(c, st);
  STAMP(4);
  group_body<1>(c, st);
  STAMP(5);
  group_body<2>(c, st);
  STAMP(6);
  __builtin_amdgcn_s_setprio(0);
  group_body<3>(c, st);
  STAMP(7);
  {
    stage_out(c, st.O[4], 0);
    stage_out(c, st.O[5], 2048);
    stage_out(c, st.O[6], (unsigned)-8192);
    u32x4 ob[5];
#pragma unroll
    for (int k = 0; k < 4; ++k) ob[k] = *(lds_u32x4_ptr)(c.ord + k * 1024);
    ob[4] = *(lds_u32x4_ptr)(c.ord - 8192);
#pragma unroll
    for (int k = 0; k < 4; ++k)
      __builtin_amdgcn_raw_buffer_store_b128(ob[k], c.rsrc, c.gvoff + (k & 1) * 2048, c.obase + (4 + (k >> 1)) * 4096, ST_AUX);
    __builtin_amdgcn_raw_buffer_store_b128(ob[4], c.rsrc, c.gvoff, c.obase + 6 * 4096, ST_AUX);
  }
  asm volatile("s_waitcnt vmcnt(0)" ::: "memory");
  STAMP(8);
  if (c.lane == 0) {
#pragma unroll
    for (int k = 0; k < 4; ++k) c.prof[k] = c.ts[k];
  }
}

__global__ void pre_kernel(unsigned long long* prof) {
  unsigned long long Ta;
  asm volatile("s_memrealtime %0\n\ts_waitcnt lgkmcnt(0)" : "=s"(Ta) :: "memory");
  if (threadIdx.x == 0) prof[65536] = Ta;
}
__global__ void diag_kernel(const unsigned long long* prof) {
  unsigned long long Tb;
  asm volatile("s_memrealtime %0\n\ts_waitcnt lgkmcnt(0)" : "=s"(Tb) :: "memory");
  __shared__ unsigned long long s0[256], s1[256], s2[256], s3[256], s4[256];
  const int t = threadIdx.x;
  unsigned long long mn = ~0ull, mx = 0, a1 = 0, a2 = 0, a3 = 0;
  const unsigned long long Ta = prof[65536];
  for (int i = t; i < 2048; i += 256) {
    unsigned long long a = prof[(size_t)i * 4], b = prof[(size_t)i * 4 + 3];
    mn = a < mn ? a : mn; mx = b > mx ? b : mx;
    a1 += prof[(size_t)i * 4 + 1] - Ta; a2 += prof[(size_t)i * 4 + 2] - Ta; a3 += b - Ta;
  }
  s0[t] = mn; s1[t] = mx; s2[t] = a1; s3[t] = a2; s4[t] = a3;
  __syncthreads();
  for (int s = 128; s > 0; s >>= 1) {
    if (t < s) { s0[t] = s0[t] < s0[t + s] ? s0[t] : s0[t + s]; s1[t] = s1[t] > s1[t + s] ? s1[t] : s1[t + s]; s2[t] += s2[t + s]; s3[t] += s3[t + s]; s4[t] += s4[t + s]; }
    __syncthreads();
  }
  mn = s0[0]; mx = s1[0];
  double r[8];
  r[0] = 0; r[1] = (double)(mn - Ta); r[2] = (double)(Tb - mx); r[3] = (double)(mx - mn);
  r[4] = (double)s2[0] / 2048 - r[1]; r[5] = (double)s3[0] / 2048 - r[1]; r[6] = (double)s4[0] / 2048 - r[1]; r[7] = 0;
  double v = r[DIAGM];
  asm volatile("" :: "v"(r[1]), "v"(r[2]), "v"(r[3]), "v"(r[4]), "v"(r[5]), "v"(r[6]));
  if (v < 0) v = 0; if (v > 20000) v = 20000;
  unsigned long long dur = (unsigned long long)(v * DIAGS);
  if (t == 0) {
    unsigned long long q0, q1;
    asm volatile("s_memrealtime %0\n\ts_waitcnt lgkmcnt(0)" : "=s"(q0) :: "memory");
    for (int it = 0; it < 1000000; ++it) {
      asm volatile("s_memrealtime %0\n\ts_waitcnt lgkmcnt(0)" : "=s"(q1) :: "memory");
      if (q1 - q0 >= dur) break;
    }
  }
}

extern "C" void kernel_launch(void* const* d_in, const int* in_sizes, int n_in, void* d_out, int out_size,
                              void* d_ws, size_t ws_size, hipStream_t stream) {
  const __bf16* e = (const __bf16*)d_in[0];
  const __bf16* wq = (const __bf16*)d_in[1];
  const __bf16* wk = (const __bf16*)d_in[2];
  const __bf16* wv = (const __bf16*)d_in[3];
  const __bf16* wres = (const __bf16*)d_in[4];
  __bf16* out = (__bf16*)d_out;
  (void)hipFuncSetAttribute((const void*)autoint_kernel, hipFuncAttributeMaxDynamicSharedMemorySize, LDS_BYTES);
  pre_kernel<<<1, 64, 0, stream>>>((unsigned long long*)d_ws);
  autoint_kernel<<<2 * NB_TOTAL / BPG, WAVES * 64, LDS_BYTES, stream>>>(e, wq, wk, wv, wres, out, (unsigned long long*)d_ws);
  diag_kernel<<<1, 256, 0, stream>>>((const unsigned long long*)d_ws);
}
```

```cpp
#define DIAGM 1
#define DIAGS 0
#define PGRID 256
#define PBLK 512
#define PLDS 163840
#define PVG 1
#define CODEPF 3
#define CPFN 7
#include <hip/hip_runtime.h>
#include <hip/hip_bf16.h>

typedef __attribute__((ext_vector_type(8))) __bf16 bf16x8;
typedef __attribute__((ext_vector_type(2))) __bf16 bf16x2;
typedef __attribute__((ext_vector_type(4))) short s16x4;
typedef __attribute__((ext_vector_type(2))) short s16x2;
typedef __attribute__((ext_vector_type(4))) float f32x4;
typedef __attribute__((ext_vector_type(4))) unsigned u32x4;
typedef __attribute__((ext_vector_type(2))) unsigned u32x2;
typedef __attribute__((address_space(3))) s16x4* lds_s16x4_ptr;
typedef __attribute__((address_space(3))) u32x4* lds_u32x4_ptr;
typedef __attribute__((address_space(3))) u32x2* lds_u32x2_ptr;
typedef __attribute__((address_space(3))) void* lds_void_ptr;

#define DI __device__ __forceinline__
#define BF(x) __builtin_bit_cast(bf16x8, (x))
#define MFMA(a, b, c) __builtin_amdgcn_mfma_f32_16x16x32_bf16(BF(a), BF(b), (c), 0, 0, 0)

#ifndef NVS
#define NVS 3
#endif
#define PRIO 3
#define ST_AUX 17

constexpr int NB_TOTAL = 4096;
constexpr int NF = 26;
constexpr int ND = 128;
constexpr int WAVES = 8;
constexpr int EPW = 4;
constexpr int BPG = WAVES * EPW;
constexpr int EBYTES = NF * ND * 2;
constexpr int LDS_BYTES = 65536 + WAVES * 12288;

DI unsigned pack2(float lo, float hi) {
  bf16x2 v;
  v[0] = (__bf16)lo;
  v[1] = (__bf16)hi;
  return __builtin_bit_cast(unsigned, v);
}
DI unsigned relu2(float lo, float hi) {
  unsigned u = pack2(lo, hi);
  asm volatile("" : "+v"(u));
  s16x2 z = {};
  return __builtin_bit_cast(unsigned, __builtin_elementwise_max(__builtin_bit_cast(s16x2, u), z));
}

DI void glds4(unsigned v0, unsigned v1, unsigned v2, unsigned v3, const void* sbase, unsigned lds_dst) {
  asm volatile(
      "s_mov_b32 m0, %5\n\ts_nop 0\n\t"
      "global_load_lds_dwordx4 %0, %4\n\t"
      "global_load_lds_dwordx4 %1, %4 offset:1024\n\t"
      "global_load_lds_dwordx4 %2, %4 offset:2048\n\t"
      "global_load_lds_dwordx4 %3, %4 offset:3072"
      :
      : "v"(v0), "v"(v1), "v"(v2), "v"(v3), "s"(sbase), "s"(lds_dst)
      : "memory", "m0");
}
DI void glds2(unsigned v0, unsigned v1, const void* sbase, unsigned lds_dst) {
  asm volatile(
      "s_mov_b32 m0, %3\n\ts_nop 0\n\t"
      "global_load_lds_dwordx4 %0, %2\n\t"
      "global_load_lds_dwordx4 %1, %2 offset:1024"
      :
      : "v"(v0), "v"(v1), "s"(sbase), "s"(lds_dst)
      : "memory", "m0");
}

#define SGB(mask, n) __builtin_amdgcn_sched_group_barrier(mask, n, 0)
#define SBAR() __builtin_amdgcn_sched_barrier(0)
#define WAITV(n) asm volatile("s_waitcnt vmcnt(" #n ")" ::: "memory");
#define WAITW(n)                  \
  WAITV(n)                        \
  __builtin_amdgcn_s_barrier();   \
  __builtin_amdgcn_sched_barrier(0);

#define STAMP(k)
#define LBAR()                                      \
  if (c.leader) __builtin_amdgcn_s_barrier();       \
  __builtin_amdgcn_sched_barrier(0);

struct Ctx {
  bool leader;
  unsigned long long* prof;
  unsigned wl;
  unsigned eread;
  unsigned ebuf;
  unsigned owr;
  unsigned ord;
  unsigned evoff[4];
  int gvoff;
  int lane, cc, g;
  const char* ebase;
  int obase;
  __amdgpu_buffer_rsrc_t rsrc;
};

struct St {
  u32x4 E[2][4];
  u32x4 W[3][4];
  u32x4 Qp[7][2], Kp[7][2];
  u32x2 Vp[7][4];
  f32x4 O[7][4];
};

DI void loadw(const Ctx& c, u32x4 (&W)[4], int n) {
  const int X = n >> 2, j = n & 3;
#pragma unroll
  for (int s = 0; s < 4; ++s) W[s] = *(lds_u32x4_ptr)(c.wl + X * 16384 + s * 4096 + j * 1024);
}
DI void relayout(const unsigned (&wb)[4], unsigned wl, int off) {
  u32x4 W[4];
#pragma unroll
  for (int j = 0; j < 4; ++j) {
    s16x4 lo = __builtin_amdgcn_ds_read_tr16_b64_v4i16((lds_s16x4_ptr)(wb[j] + off));
    s16x4 hi = __builtin_amdgcn_ds_read_tr16_b64_v4i16((lds_s16x4_ptr)(wb[j] + off + 2048));
    W[j] = __builtin_bit_cast(u32x4, __builtin_shufflevector(lo, hi, 0, 1, 2, 3, 4, 5, 6, 7));
  }
  asm volatile("s_waitcnt lgkmcnt(0)" ::: "memory");
#pragma unroll
  for (int j = 0; j < 4; ++j) *(lds_u32x4_ptr)(wl + off + j * 1024) = W[j];
  asm volatile("s_waitcnt lgkmcnt(0)" ::: "memory");
}

template <int G>
DI void dma_e(const Ctx& c) {
  const char* p = c.ebase + (size_t)G * 8192;
  if (G < 3) {
    glds4(c.evoff[0], c.evoff[1], c.evoff[2], c.evoff[3], p, c.ebuf);
    glds4(c.evoff[0], c.evoff[1], c.evoff[2], c.evoff[3], p + 4096, c.ebuf + 4096);
  } else {
    glds2(c.evoff[0], c.evoff[1], p, c.ebuf);
  }
}

template <int NT>
DI void read_e(const Ctx& c, u32x4 (&E)[2][4]) {
#pragma unroll
  for (int s = 0; s < 4; ++s) {
    const unsigned a = c.eread ^ (s << 6);
    E[0][s] = *(lds_u32x4_ptr)(a);
    if (NT > 1) E[1][s] = *(lds_u32x4_ptr)(a + 4096);
  }
}

DI void stage_out(const Ctx& c, const f32x4 (&O)[4], unsigned slot) {
#pragma unroll
  for (int j = 0; j < 4; ++j) {
    u32x2 v;
    v[0] = relu2(O[j][0], O[j][1]);
    v[1] = relu2(O[j][2], O[j][3]);
    *(lds_u32x2_ptr)((c.owr ^ (j << 5)) + slot) = v;
  }
}

DI float xmax(float m) {
  auto a = __builtin_amdgcn_permlane32_swap(__float_as_uint(m), __float_as_uint(m), false, false);
  asm("v_max_f32 %0, %1, %2" : "=v"(m) : "v"(a[0]), "v"(a[1]));
  auto b = __builtin_amdgcn_permlane16_swap(__float_as_uint(m), __float_as_uint(m), false, false);
  asm("v_max_f32 %0, %1, %2" : "=v"(m) : "v"(b[0]), "v"(b[1]));
  return m;
}
DI float xsum(float m) {
  auto a = __builtin_amdgcn_permlane32_swap(__float_as_uint(m), __float_as_uint(m), false, false);
  m = __uint_as_float(a[0]) + __uint_as_float(a[1]);
  auto b = __builtin_amdgcn_permlane16_swap(__float_as_uint(m), __float_as_uint(m), false, false);
  return __uint_as_float(b[0]) + __uint_as_float(b[1]);
}
DI u32x4 sel4(bool p, const u32x4& a, const u32x4& b) {
  u32x4 r;
#pragma unroll
  for (int i = 0; i < 4; ++i) r[i] = p ? a[i] : b[i];
  return r;
}

#define PAT(NT_, ND_, NV)                          \
  _Pragma("unroll") for (int q_ = 0; q_ < 4; ++q_) { \
    SGB(0x8, 1);                                   \
    SGB(0x100, ND_);                               \
    SGB(0x2, NV);                                  \
    if ((NT_) > 1) { SGB(0x8, 1); SGB(0x2, NV); }  \
    if ((NT_) > 2) { SGB(0x8, 1); SGB(0x2, NV); }  \
  }

struct Loc {
  f32x4 acc[2][2];
  f32x4 S[2][2];
  float sv[2][8], mx[2], sm[2];
  u32x4 Pp[2];
  u32x4 ob[4];
  u32x4 QA[2], KA[2];
  u32x2 VA[4];
};

template <int G, int n>
DI void job(const Ctx& c, St& st, Loc& L) {
  constexpr int NT = G < 3 ? 2 : 1;
  constexpr int T0 = 2 * G;
  constexpr int NM = 4 * NT;
  constexpr int NDS = 8 / NM;
  constexpr int VS = 2 / NT;
  constexpr int WB = (16 * G) % 3;
  constexpr int TA = G == 0 ? 0 : G == 1 ? 1 : G == 2 ? 3 : 4;
  constexpr int TB = G == 0 ? 1 : G == 1 ? 2 : G == 2 ? 4 : 5;
  constexpr int X = n >> 2, j = n & 3;
  const float cexp = 0.125f * 1.4426950408889634f;
  const float NINF = -__builtin_inff();
  const int g = c.g, cc = c.cc;
  if (n + 2 < 16) loadw(c, st.W[(WB + n + 2) % 3], n + 2);
  else if (G < 3) loadw(c, st.W[(WB + n + 2) % 3], n + 2 - 16);
  const u32x4(&W)[4] = st.W[(WB + n) % 3];
#pragma unroll
  for (int tt = 0; tt < NT; ++tt) L.acc[n & 1][tt] = f32x4{};
#pragma unroll
  for (int s = 0; s < 4; ++s)
#pragma unroll
    for (int tt = 0; tt < NT; ++tt)
      L.acc[n & 1][tt] = X == 2 ? MFMA(st.E[tt][s], W[s], L.acc[n & 1][tt]) : MFMA(W[s], st.E[tt][s], L.acc[n & 1][tt]);
  if (X == 3) {
#pragma unroll
    for (int tt = 0; tt < NT; ++tt) st.O[T0 + tt][j] = L.acc[n & 1][tt];
  }
  if (n > 0 && n <= 12) {
    constexpr int pX = (n - 1) >> 2, pj = (n - 1) & 3;
#pragma unroll
    for (int tt = 0; tt < NT; ++tt) {
      const f32x4 a = L.acc[(n - 1) & 1][tt];
      const unsigned lo = pack2(a[0], a[1]), hi = pack2(a[2], a[3]);
      if (pX == 0) { st.Qp[T0 + tt][pj >> 1][2 * (pj & 1)] = lo; st.Qp[T0 + tt][pj >> 1][2 * (pj & 1) + 1] = hi; }
      if (pX == 1) { st.Kp[T0 + tt][pj >> 1][2 * (pj & 1)] = lo; st.Kp[T0 + tt][pj >> 1][2 * (pj & 1) + 1] = hi; }
      if (pX == 2) { st.Vp[T0 + tt][pj][0] = lo; st.Vp[T0 + tt][pj][1] = hi; }
    }
  }
  constexpr int nv = (G > 0 && n == 0) || (G == 2 && n == 1) ? 3
                     : ((G & 1) && (n == 5 || n == 8)) ? 2
                     : (n >= 9) ? NVS : 1;
  if (G == 1 || G == 3) {
    constexpr int TO = G == 1 ? 0 : 3;
    if (n == 0) stage_out(c, st.O[TO], 0);
    if (n == 2) { L.ob[0] = *(lds_u32x4_ptr)(c.ord); L.ob[1] = *(lds_u32x4_ptr)(c.ord + 1024); }
    if (n == 4) {
      __builtin_amdgcn_raw_buffer_store_b128(L.ob[0], c.rsrc, c.gvoff, c.obase + TO * 4096, ST_AUX);
      __builtin_amdgcn_raw_buffer_store_b128(L.ob[1], c.rsrc, c.gvoff + 2048, c.obase + TO * 4096, ST_AUX);
    }
  }
  if (G == 2) {
    if (n == 0) stage_out(c, st.O[1], 0);
    if (n == 1) stage_out(c, st.O[2], 2048);
    if (n == 3) {
#pragma unroll
      for (int k = 0; k < 4; ++k) L.ob[k] = *(lds_u32x4_ptr)(c.ord + k * 1024);
    }
    if (n == 5) {
#pragma unroll
      for (int k = 0; k < 4; ++k)
        __builtin_amdgcn_raw_buffer_store_b128(L.ob[k], c.rsrc, c.gvoff + (k & 1) * 2048, c.obase + (1 + (k >> 1)) * 4096, ST_AUX);
    }
  }
  if (G == 1 || G == 3) {
    constexpr int TL = G == 1 ? 3 : 6;
    constexpr int CUT = G == 1 ? 10 : 14;
    if (n == 5) {
      L.QA[0] = sel4(cc >= CUT, st.Qp[TA][0], st.Qp[TL][0]);
      L.QA[1] = sel4(cc >= CUT, st.Qp[TA][1], st.Qp[TL][1]);
    }
    if (n == 8) {
      L.KA[0] = sel4(cc >= CUT, st.Kp[TA][0], st.Kp[TL][0]);
      L.KA[1] = sel4(cc >= CUT, st.Kp[TA][1], st.Kp[TL][1]);
    }
    if (n == 12) {
#pragma unroll
      for (int k = 0; k < 4; ++k) {
        L.VA[k][0] = (G == 1 && g == 3) ? st.Vp[TA][k][0] : st.Vp[TL][k][0];
        L.VA[k][1] = (G == 1 ? g >= 2 : g == 3) ? st.Vp[TA][k][1] : st.Vp[TL][k][1];
      }
    }
  } else {
    if (n == 5) { L.QA[0] = st.Qp[TA][0]; L.QA[1] = st.Qp[TA][1]; }
    if (n == 8) { L.KA[0] = st.Kp[TA][0]; L.KA[1] = st.Kp[TA][1]; }
    if (n == 12) {
#pragma unroll
      for (int k = 0; k < 4; ++k) L.VA[k] = st.Vp[TA][k];
    }
  }
  if (n == 9 || n == 10) {
    constexpr int qs = (n - 9) & 1;
#pragma unroll
    for (int ks = 0; ks < 2; ++ks)
#pragma unroll
      for (int i = 0; i < 2; ++i) {
        float lo = L.S[ks][qs][2 * i], hi = L.S[ks][qs][2 * i + 1];
        bool masked = false, ok = true;
        if (G == 0 && ks == 1) { masked = true; ok = i == 0 ? g < 3 : g < 2; }
        if (G == 1 && ks == 0) { masked = true; ok = i == 0 ? (g == 0 || g == 3) : g != 1; }
        if (G == 2 && ks == 0) { masked = true; ok = g >= 1; }
        if (G == 2 && ks == 1 && i == 1) { masked = true; ok = g < 3; }
        if (G == 3 && ks == 0) { masked = true; ok = i == 0 ? g < 2 : g != 2; }
        if (masked) { lo = ok ? lo : NINF; hi = ok ? hi : NINF; }
        L.sv[qs][4 * ks + 2 * i] = lo;
        L.sv[qs][4 * ks + 2 * i + 1] = hi;
      }
    float m = L.sv[qs][0];
#pragma unroll
    for (int i = 1; i < 8; ++i) m = fmaxf(m, L.sv[qs][i]);
    L.mx[qs] = xmax(m) * cexp;
  }
  if (n == 11 || n == 12) {
    constexpr int qs = (n - 11) & 1;
    float s = 0.f;
#pragma unroll
    for (int i = 0; i < 8; ++i) {
      L.sv[qs][i] = __builtin_amdgcn_exp2f(__builtin_fmaf(L.sv[qs][i], cexp, -L.mx[qs]));
      s += L.sv[qs][i];
    }
    L.sm[qs] = xsum(s);
  }
  if (n == 13 || n == 14) {
    constexpr int qs = (n - 13) & 1;
    float inv = __builtin_amdgcn_rcpf(L.sm[qs]);
    if (G == 0 && qs == 1) inv = cc < 10 ? inv : 0.f;
    if (G == 2 && qs == 0) inv = cc >= 4 ? inv : 0.f;
    if (G == 2 && qs == 1) inv = cc < 14 ? inv : 0.f;
#pragma unroll
    for (int i = 0; i < 4; ++i) L.Pp[qs][i] = pack2(L.sv[qs][2 * i] * inv, L.sv[qs][2 * i + 1] * inv);
  }
  if (n == 2 || n == 3) { PAT(NT, 2, nv * VS); }
  else { PAT(NT, 1, nv * VS); }
  if (n < 9 || n == 15) SBAR();
  if (G == 0 && (n == 1 || n == 5)) { LBAR(); }
  if (n == 8) {
    L.S[0][0] = MFMA(L.KA[0], L.QA[0], f32x4{});
    L.S[1][0] = MFMA(st.Kp[TB][0], L.QA[0], f32x4{});
    L.S[0][1] = MFMA(L.KA[0], st.Qp[TB][0], f32x4{});
    L.S[1][1] = MFMA(st.Kp[TB][0], st.Qp[TB][0], f32x4{});
    L.S[0][0] = MFMA(L.KA[1], L.QA[1], L.S[0][0]);
    L.S[1][0] = MFMA(st.Kp[TB][1], L.QA[1], L.S[1][0]);
    L.S[0][1] = MFMA(L.KA[1], st.Qp[TB][1], L.S[0][1]);
    L.S[1][1] = MFMA(st.Kp[TB][1], st.Qp[TB][1], L.S[1][1]);
    SBAR();
    if (G == 0) { LBAR(); }
  }
  if constexpr (n + 1 < 16) job<G, n + 1>(c, st, L);
}

template <int G>
DI void group_body(const Ctx& c, St& st) {
  constexpr int TA = G == 0 ? 0 : G == 1 ? 1 : G == 2 ? 3 : 4;
  constexpr int TB = G == 0 ? 1 : G == 1 ? 2 : G == 2 ? 4 : 5;
  const int cc = c.cc;
  Loc L;
  job<G, 0>(c, st, L);
  if (G < 3) {
    if (G == 0) { WAITV(0); } else if (G == 1) { WAITV(2); } else { WAITV(4); }
    SBAR();
    read_e<(G < 2 ? 2 : 1)>(c, st.E);
  }
  {
    u32x4 Vf[4];
#pragma unroll
    for (int j = 0; j < 4; ++j) {
      Vf[j][0] = L.VA[j][0]; Vf[j][1] = L.VA[j][1];
      Vf[j][2] = st.Vp[TB][j][0]; Vf[j][3] = st.Vp[TB][j][1];
    }
    const u32x4 z = {};
    if (G == 1 || G == 3) {
      constexpr int TL = G == 1 ? 3 : 6;
      constexpr int CUT = G == 1 ? 10 : 14;
      const u32x4 Ph = sel4(cc >= CUT, L.Pp[0], z);
      const u32x4 Pl = G == 1 ? sel4(cc < 4, L.Pp[0], z) : L.Pp[0];
#pragma unroll
      for (int j = 0; j < 4; ++j) st.O[TB][j] = MFMA(Vf[j], L.Pp[1], st.O[TB][j]);
#pragma unroll
      for (int j = 0; j < 4; ++j) st.O[TA][j] = MFMA(Vf[j], Ph, st.O[TA][j]);
#pragma unroll
      for (int j = 0; j < 4; ++j) st.O[TL][j] = MFMA(Vf[j], Pl, st.O[TL][j]);
    } else {
#pragma unroll
      for (int j = 0; j < 4; ++j) st.O[TA][j] = MFMA(Vf[j], L.Pp[0], st.O[TA][j]);
#pragma unroll
      for (int j = 0; j < 4; ++j) st.O[TB][j] = MFMA(Vf[j], L.Pp[1], st.O[TB][j]);
    }
  }
  SBAR();
  if (G < 2) {
    asm volatile("s_waitcnt lgkmcnt(0)" ::: "memory");
    dma_e<G + 2>(c);
    SBAR();
  }
}

__global__ __launch_bounds__(WAVES * 64, 2) void autoint_kernel(
    const __bf16* __restrict__ e, const __bf16* __restrict__ wq, const __bf16* __restrict__ wk,
    const __bf16* __restrict__ wv, const __bf16* __restrict__ wres, __bf16* __restrict__ out, unsigned long long* prof) {
  extern __shared__ __attribute__((aligned(16))) char smem[];
  const int tid = threadIdx.x;
  const int lane = tid & 63;
  const int wave = __builtin_amdgcn_readfirstlane(tid >> 6);
  const int cc = lane & 15, g = lane >> 4;
  const unsigned lds0 = __builtin_amdgcn_readfirstlane((unsigned)(size_t)(lds_void_ptr)smem);
  const int bid = blockIdx.x;
  const int hd = (bid >> 3) & 1;
  const int grp = (bid & 7) | ((bid >> 4) << 3);
  const int b0 = grp * BPG + wave * EPW;

  Ctx c;
  c.lane = lane;
  c.cc = cc;
  c.g = g;
  c.prof = prof + (size_t)(blockIdx.x * WAVES + wave) * 32;
  STAMP(0);
  c.ebuf = lds0 + 65536 + wave * 12288;
  c.ebase = (const char*)e + (size_t)b0 * EBYTES;
  c.obase = b0 * EBYTES;
  {
    const int c0 = cc ^ g;
#pragma unroll
    for (int j = 0; j < 4; ++j) c.evoff[j] = (unsigned)((g * 256 + (c0 << 4)) ^ (j << 6));
  }
  const bool leader = wave < 4;
  c.leader = leader;
  if (wave == CODEPF) {
    unsigned long long pc;
    asm volatile("s_getpc_b64 %0" : "=s"(pc));
    const unsigned t0 = lane * 128, t1 = t0 + 8192, t2 = 16384 + (lane < CPFN ? lane : CPFN) * 128;
    asm volatile(
        "s_mov_b32 m0, %4\n\ts_nop 0\n\t"
        "global_load_lds_dword %0, %3\n\t"
        "global_load_lds_dword %1, %3\n\t"
        "global_load_lds_dword %2, %3"
        :
        : "v"(t0), "v"(t1), "v"(t2), "s"(pc), "s"(c.ebuf + 8192)
        : "memory", "m0");
  }
  if (leader) {
    dma_e<0>(c);
  } else {
    unsigned o64[4], o128[4];
    const int r8 = lane >> 3;
    const int krow = 32 * (wave - 4) + 8 * (r8 >> 2) + (r8 & 3);
    const int ch = (lane & 7) ^ (r8 & 6);
#pragma unroll
    for (int i = 0; i < 4; ++i) {
      const int k = krow + 16 * (i & 1) + 4 * (i >> 1);
      o64[i] = (unsigned)(k * 128 + ch * 16 + 4096 - i * 1024);
      o128[i] = (unsigned)(k * 256 + ch * 16 + 4096 - i * 1024);
    }
    const char* sq = (const char*)wq + hd * 16384;
    const char* sk = (const char*)wk + hd * 16384;
    const char* sv = (const char*)wv + hd * 16384;
    const char* sr = (const char*)wres + hd * 128;
    const unsigned dst = lds0 + (wave - 4) * 4096;
    glds4(o64[0], o64[1], o64[2], o64[3], sq - 4096, dst);
    glds4(o64[0], o64[1], o64[2], o64[3], sk - 4096, dst + 16384);
    glds4(o64[0], o64[1], o64[2], o64[3], sv - 4096, dst + 32768);
    glds4(o128[0], o128[1], o128[2], o128[3], sr - 4096, dst + 49152);
    dma_e<0>(c);
  }
  SBAR();

  unsigned wb[4];
  {
    const int q4 = (lane & 15) >> 2, p4 = lane & 3;
    const int r8 = (g & 1) * 4 + q4;
#pragma unroll
    for (int j = 0; j < 4; ++j)
      wb[j] = lds0 + (wave & 3) * 4096 + (g >> 1) * 1024 + r8 * 128 + ((((2 * j) ^ (r8 & 6)) + (p4 >> 1)) << 4) + (p4 & 1) * 8;
    c.wl = lds0 + lane * 16;
    asm volatile("" : "+v"(c.wl));
    c.eread = c.ebuf + cc * 256 + ((g ^ cc) << 4);
    const unsigned obuf = c.ebuf + 8192;
    c.owr = obuf + cc * 128 + (((g >> 1) ^ (cc & 7)) << 4) + (g & 1) * 8;
    c.ord = obuf + lane * 16;
    const int orow = lane >> 3, oc = (lane & 7) ^ (orow & 7);
    c.gvoff = orow * 256 + hd * 128 + oc * 16;
    c.rsrc = __builtin_amdgcn_make_buffer_rsrc((void*)out, 0, NB_TOTAL * EBYTES, 0x00020000);
  }

  St st;
  STAMP(1);
  if (leader) {
    __builtin_amdgcn_s_barrier();
  } else {
    const unsigned wlw = c.wl + (wave & 3) * 4096;
    WAITV(20); relayout(wb, wlw, 0);     __builtin_amdgcn_s_barrier(); SBAR();
    WAITV(16); relayout(wb, wlw, 16384); __builtin_amdgcn_s_barrier(); SBAR();
    WAITV(12); relayout(wb, wlw, 32768); __builtin_amdgcn_s_barrier(); SBAR();
    WAITV(8);  relayout(wb, wlw, 49152); __builtin_amdgcn_s_barrier(); SBAR();
    __builtin_amdgcn_s_setprio(1);
  }
  WAITV(0);
  SBAR();
  STAMP(2);
  read_e<2>(c, st.E);
  loadw(c, st.W[0], 0);
  loadw(c, st.W[1], 1);
  asm volatile("s_waitcnt lgkmcnt(0)" ::: "memory");
  SBAR();
  dma_e<1>(c);
  SBAR();
  STAMP(3);

  group_body<0>(c, st);
  STAMP(4);
  group_body<1>(c, st);
  STAMP(5);
  group_body<2>(c, st);
  STAMP(6);
  __builtin_amdgcn_s_setprio(0);
  group_body<3>(c, st);
  STAMP(7);
  {
    stage_out(c, st.O[4], 0);
    stage_out(c, st.O[5], 2048);
    stage_out(c, st.O[6], (unsigned)-8192);
    u32x4 ob[5];
#pragma unroll
    for (int k = 0; k < 4; ++k) ob[k] = *(lds_u32x4_ptr)(c.ord + k * 1024);
    ob[4] = *(lds_u32x4_ptr)(c.ord - 8192);
#pragma unroll
    for (int k = 0; k < 4; ++k)
      __builtin_amdgcn_raw_buffer_store_b128(ob[k], c.rsrc, c.gvoff + (k & 1) * 2048, c.obase + (4 + (k >> 1)) * 4096, ST_AUX);
    __builtin_amdgcn_raw_buffer_store_b128(ob[4], c.rsrc, c.gvoff, c.obase + 6 * 4096, ST_AUX);
  }
  STAMP(8);
}

__global__ void pre_kernel(unsigned long long* prof) {
  unsigned long long Ta;
  asm volatile("s_memrealtime %0\n\ts_waitcnt lgkmcnt(0)" : "=s"(Ta) :: "memory");
  if (threadIdx.x == 0) prof[65536] = Ta;
}
__global__ __launch_bounds__(512, 2) void probe_kernel(unsigned long long* prof) {
  unsigned long long Ta;
  asm volatile("s_memrealtime %0\n\ts_waitcnt lgkmcnt(0)" : "=s"(Ta) :: "memory");
  asm volatile("v_mov_b32 v200, 0" ::: "v200");
  if ((threadIdx.x & 63) == 0) prof[blockIdx.x * 8 + (threadIdx.x >> 6)] = Ta;
}
__global__ void diag_kernel(const unsigned long long* prof) {
  __shared__ unsigned long long s0[256], s1[256];
  const int t = threadIdx.x;
  unsigned long long mn = ~0ull, mx = 0;
  const unsigned long long Ta = prof[65536];
  for (int i = t; i < 2048; i += 256) {
    unsigned long long a = prof[i];
    if ((i >> 3) < PGRID && (i & 7) < PBLK / 64) { mn = a < mn ? a : mn; mx = a > mx ? a : mx; }
  }
  s0[t] = mn; s1[t] = mx;
  __syncthreads();
  for (int s = 128; s > 0; s >>= 1) {
    if (t < s) { s0[t] = s0[t] < s0[t + s] ? s0[t] : s0[t + s]; s1[t] = s1[t] > s1[t + s] ? s1[t] : s1[t + s]; }
    __syncthreads();
  }
  mn = s0[0]; mx = s1[0];
  double v = DIAGM == 1 ? (double)(mn - Ta) : (double)(mx - Ta);
  if (v < 0) v = 0; if (v > 20000) v = 20000;
  unsigned long long dur = (unsigned long long)(v * DIAGS);
  if (t == 0) {
    unsigned long long q0, q1;
    asm volatile("s_memrealtime %0\n\ts_waitcnt lgkmcnt(0)" : "=s"(q0) :: "memory");
    for (int it = 0; it < 1000000; ++it) {
      asm volatile("s_memrealtime %0\n\ts_waitcnt lgkmcnt(0)" : "=s"(q1) :: "memory");
      if (q1 - q0 >= dur) break;
    }
  }
}

extern "C" void kernel_launch(void* const* d_in, const int* in_sizes, int n_in, void* d_out, int out_size,
                              void* d_ws, size_t ws_size, hipStream_t stream) {
  const __bf16* e = (const __bf16*)d_in[0];
  const __bf16* wq = (const __bf16*)d_in[1];
  const __bf16* wk = (const __bf16*)d_in[2];
  const __bf16* wv = (const __bf16*)d_in[3];
  const __bf16* wres = (const __bf16*)d_in[4];
  __bf16* out = (__bf16*)d_out;
  (void)hipFuncSetAttribute((const void*)autoint_kernel, hipFuncAttributeMaxDynamicSharedMemorySize, LDS_BYTES);
  pre_kernel<<<1, 64, 0, stream>>>((unsigned long long*)d_ws);
  (void)hipFuncSetAttribute((const void*)probe_kernel, hipFuncAttributeMaxDynamicSharedMemorySize, 163840);
  probe_kernel<<<PGRID, PBLK, PLDS, stream>>>((unsigned long long*)d_ws);
  diag_kernel<<<1, 256, 0, stream>>>((const unsigned long long*)d_ws);
  autoint_kernel<<<2 * NB_TOTAL / BPG, WAVES * 64, LDS_BYTES, stream>>>(e, wq, wk, wv, wres, out, (unsigned long long*)d_ws);
}
```

```cpp
#define DIAGM 0
#define DIAGS 0
#define NOPRE 1
#define CODEPF 3
#define CPFN 7
#include <hip/hip_runtime.h>
#include <hip/hip_bf16.h>

typedef __attribute__((ext_vector_type(8))) __bf16 bf16x8;
typedef __attribute__((ext_vector_type(2))) __bf16 bf16x2;
typedef __attribute__((ext_vector_type(4))) short s16x4;
typedef __attribute__((ext_vector_type(2))) short s16x2;
typedef __attribute__((ext_vector_type(4))) float f32x4;
typedef __attribute__((ext_vector_type(4))) unsigned u32x4;
typedef __attribute__((ext_vector_type(2))) unsigned u32x2;
typedef __attribute__((address_space(3))) s16x4* lds_s16x4_ptr;
typedef __attribute__((address_space(3))) u32x4* lds_u32x4_ptr;
typedef __attribute__((address_space(3))) u32x2* lds_u32x2_ptr;
typedef __attribute__((address_space(3))) void* lds_void_ptr;

#define DI __device__ __forceinline__
#define BF(x) __builtin_bit_cast(bf16x8, (x))
#define MFMA(a, b, c) __builtin_amdgcn_mfma_f32_16x16x32_bf16(BF(a), BF(b), (c), 0, 0, 0)

#ifndef NVS
#define NVS 3
#endif
#define PRIO 3
#define ST_AUX 17

constexpr int NB_TOTAL = 4096;
constexpr int NF = 26;
constexpr int ND = 128;
constexpr int WAVES = 8;
constexpr int EPW = 4;
constexpr int BPG = WAVES * EPW;
constexpr int EBYTES = NF * ND * 2;
constexpr int LDS_BYTES = 65536 + WAVES * 12288;

DI unsigned pack2(float lo, float hi) {
  bf16x2 v;
  v[0] = (__bf16)lo;
  v[1] = (__bf16)hi;
  return __builtin_bit_cast(unsigned, v);
}
DI unsigned relu2(float lo, float hi) {
  unsigned u = pack2(lo, hi);
  asm volatile("" : "+v"(u));
  s16x2 z = {};
  return __builtin_bit_cast(unsigned, __builtin_elementwise_max(__builtin_bit_cast(s16x2, u), z));
}

DI void glds4(unsigned v0, unsigned v1, unsigned v2, unsigned v3, const void* sbase, unsigned lds_dst) {
  asm volatile(
      "s_mov_b32 m0, %5\n\ts_nop 0\n\t"
      "global_load_lds_dwordx4 %0, %4\n\t"
      "global_load_lds_dwordx4 %1, %4 offset:1024\n\t"
      "global_load_lds_dwordx4 %2, %4 offset:2048\n\t"
      "global_load_lds_dwordx4 %3, %4 offset:3072"
      :
      : "v"(v0), "v"(v1), "v"(v2), "v"(v3), "s"(sbase), "s"(lds_dst)
      : "memory", "m0");
}
DI void glds2(unsigned v0, unsigned v1, const void* sbase, unsigned lds_dst) {
  asm volatile(
      "s_mov_b32 m0, %3\n\ts_nop 0\n\t"
      "global_load_lds_dwordx4 %0, %2\n\t"
      "global_load_lds_dwordx4 %1, %2 offset:1024"
      :
      : "v"(v0), "v"(v1), "s"(sbase), "s"(lds_dst)
      : "memory", "m0");
}

#define SGB(mask, n) __builtin_amdgcn_sched_group_barrier(mask, n, 0)
#define SBAR() __builtin_amdgcn_sched_barrier(0)
#define WAITV(n) asm volatile("s_waitcnt vmcnt(" #n ")" ::: "memory");
#define WAITW(n)                  \
  WAITV(n)                        \
  __builtin_amdgcn_s_barrier();   \
  __builtin_amdgcn_sched_barrier(0);

#define STAMP(k) if (k == 0 || k == 2 || k == 7 || k == 8) { asm volatile("s_memrealtime %0\n\ts_waitcnt lgkmcnt(0)" : "=s"(c.ts[k == 0 ? 0 : k == 2 ? 1 : k == 7 ? 2 : 3]) :: "memory"); __builtin_amdgcn_sched_barrier(0); }
#define LBAR()                                      \
  if (c.leader) __builtin_amdgcn_s_barrier();       \
  __builtin_amdgcn_sched_barrier(0);

struct Ctx {
  mutable unsigned long long ts[4];
  bool leader;
  unsigned long long* prof;
  unsigned wl;
  unsigned eread;
  unsigned ebuf;
  unsigned owr;
  unsigned ord;
  unsigned evoff[4];
  int gvoff;
  int lane, cc, g;
  const char* ebase;
  int obase;
  __amdgpu_buffer_rsrc_t rsrc;
};

struct St {
  u32x4 E[2][4];
  u32x4 W[3][4];
  u32x4 Qp[7][2], Kp[7][2];
  u32x2 Vp[7][4];
  f32x4 O[7][4];
};

DI void loadw(const Ctx& c, u32x4 (&W)[4], int n) {
  const int X = n >> 2, j = n & 3;
#pragma unroll
  for (int s = 0; s < 4; ++s) W[s] = *(lds_u32x4_ptr)(c.wl + X * 16384 + s * 4096 + j * 1024);
}
DI void relayout(const unsigned (&wb)[4], unsigned wl, int off) {
  u32x4 W[4];
#pragma unroll
  for (int j = 0; j < 4; ++j) {
    s16x4 lo = __builtin_amdgcn_ds_read_tr16_b64_v4i16((lds_s16x4_ptr)(wb[j] + off));
    s16x4 hi = __builtin_amdgcn_ds_read_tr16_b64_v4i16((lds_s16x4_ptr)(wb[j] + off + 2048));
    W[j] = __builtin_bit_cast(u32x4, __builtin_shufflevector(lo, hi, 0, 1, 2, 3, 4, 5, 6, 7));
  }
  asm volatile("s_waitcnt lgkmcnt(0)" ::: "memory");
#pragma unroll
  for (int j = 0; j < 4; ++j) *(lds_u32x4_ptr)(wl + off + j * 1024) = W[j];
  asm volatile("s_waitcnt lgkmcnt(0)" ::: "memory");
}

template <int G>
DI void dma_e(const Ctx& c) {
  const char* p = c.ebase + (size_t)G * 8192;
  if (G < 3) {
    glds4(c.evoff[0], c.evoff[1], c.evoff[2], c.evoff[3], p, c.ebuf);
    glds4(c.evoff[0], c.evoff[1], c.evoff[2], c.evoff[3], p + 4096, c.ebuf + 4096);
  } else {
    glds2(c.evoff[0], c.evoff[1], p, c.ebuf);
  }
}

template <int NT>
DI void read_e(const Ctx& c, u32x4 (&E)[2][4]) {
#pragma unroll
  for (int s = 0; s < 4; ++s) {
    const unsigned a = c.eread ^ (s << 6);
    E[0][s] = *(lds_u32x4_ptr)(a);
    if (NT > 1) E[1][s] = *(lds_u32x4_ptr)(a + 4096);
  }
}

DI void stage_out(const Ctx& c, const f32x4 (&O)[4], unsigned slot) {
#pragma unroll
  for (int j = 0; j < 4; ++j) {
    u32x2 v;
    v[0] = relu2(O[j][0], O[j][1]);
    v[1] = relu2(O[j][2], O[j][3]);
    *(lds_u32x2_ptr)((c.owr ^ (j << 5)) + slot) = v;
  }
}

DI float xmax(float m) {
  auto a = __builtin_amdgcn_permlane32_swap(__float_as_uint(m), __float_as_uint(m), false, false);
  asm("v_max_f32 %0, %1, %2" : "=v"(m) : "v"(a[0]), "v"(a[1]));
  auto b = __builtin_amdgcn_permlane16_swap(__float_as_uint(m), __float_as_uint(m), false, false);
  asm("v_max_f32 %0, %1, %2" : "=v"(m) : "v"(b[0]), "v"(b[1]));
  return m;
}
DI float xsum(float m) {
  auto a = __builtin_amdgcn_permlane32_swap(__float_as_uint(m), __float_as_uint(m), false, false);
  m = __uint_as_float(a[0]) + __uint_as_float(a[1]);
  auto b = __builtin_amdgcn_permlane16_swap(__float_as_uint(m), __float_as_uint(m), false, false);
  return __uint_as_float(b[0]) + __uint_as_float(b[1]);
}
DI u32x4 sel4(bool p, const u32x4& a, const u32x4& b) {
  u32x4 r;
#pragma unroll
  for (int i = 0; i < 4; ++i) r[i] = p ? a[i] : b[i];
  return r;
}

#define PAT(NT_, ND_, NV)                          \
  _Pragma("unroll") for (int q_ = 0; q_ < 4; ++q_) { \
    SGB(0x8, 1);                                   \
    SGB(0x100, ND_);                               \
    SGB(0x2, NV);                                  \
    if ((NT_) > 1) { SGB(0x8, 1); SGB(0x2, NV); }  \
    if ((NT_) > 2) { SGB(0x8, 1); SGB(0x2, NV); }  \
  }

struct Loc {
  f32x4 acc[2][2];
  f32x4 S[2][2];
  float sv[2][8], mx[2], sm[2];
  u32x4 Pp[2];
  u32x4 ob[4];
  u32x4 QA[2], KA[2];
  u32x2 VA[4];
};

template <int G, int n>
DI void job(const Ctx& c, St& st, Loc& L) {
  constexpr int NT = G < 3 ? 2 : 1;
  constexpr int T0 = 2 * G;
  constexpr int NM = 4 * NT;
  constexpr int NDS = 8 / NM;
  constexpr int VS = 2 / NT;
  constexpr int WB = (16 * G) % 3;
  constexpr int TA = G == 0 ? 0 : G == 1 ? 1 : G == 2 ? 3 : 4;
  constexpr int TB = G == 0 ? 1 : G == 1 ? 2 : G == 2 ? 4 : 5;
  constexpr int X = n >> 2, j = n & 3;
  const float cexp = 0.125f * 1.4426950408889634f;
  const float NINF = -__builtin_inff();
  const int g = c.g, cc = c.cc;
  if (n + 2 < 16) loadw(c, st.W[(WB + n + 2) % 3], n + 2);
  else if (G < 3) loadw(c, st.W[(WB + n + 2) % 3], n + 2 - 16);
  const u32x4(&W)[4] = st.W[(WB + n) % 3];
#pragma unroll
  for (int tt = 0; tt < NT; ++tt) L.acc[n & 1][tt] = f32x4{};
#pragma unroll
  for (int s = 0; s < 4; ++s)
#pragma unroll
    for (int tt = 0; tt < NT; ++tt)
      L.acc[n & 1][tt] = X == 2 ? MFMA(st.E[tt][s], W[s], L.acc[n & 1][tt]) : MFMA(W[s], st.E[tt][s], L.acc[n & 1][tt]);
  if (X == 3) {
#pragma unroll
    for (int tt = 0; tt < NT; ++tt) st.O[T0 + tt][j] = L.acc[n & 1][tt];
  }
  if (n > 0 && n <= 12) {
    constexpr int pX = (n - 1) >> 2, pj = (n - 1) & 3;
#pragma unroll
    for (int tt = 0; tt < NT; ++tt) {
      const f32x4 a = L.acc[(n - 1) & 1][tt];
      const unsigned lo = pack2(a[0], a[1]), hi = pack2(a[2], a[3]);
      if (pX == 0) { st.Qp[T0 + tt][pj >> 1][2 * (pj & 1)] = lo; st.Qp[T0 + tt][pj >> 1][2 * (pj & 1) + 1] = hi; }
      if (pX == 1) { st.Kp[T0 + tt][pj >> 1][2 * (pj & 1)] = lo; st.Kp[T0 + tt][pj >> 1][2 * (pj & 1) + 1] = hi; }
      if (pX == 2) { st.Vp[T0 + tt][pj][0] = lo; st.Vp[T0 + tt][pj][1] = hi; }
    }
  }
  constexpr int nv = (G > 0 && n == 0) || (G == 2 && n == 1) ? 3
                     : ((G & 1) && (n == 5 || n == 8)) ? 2
                     : (n >= 9) ? NVS : 1;
  if (G == 1 || G == 3) {
    constexpr int TO = G == 1 ? 0 : 3;
    if (n == 0) stage_out(c, st.O[TO], 0);
    if (n == 2) { L.ob[0] = *(lds_u32x4_ptr)(c.ord); L.ob[1] = *(lds_u32x4_ptr)(c.ord + 1024); }
    if (n == 4) {
      __builtin_amdgcn_raw_buffer_store_b128(L.ob[0], c.rsrc, c.gvoff, c.obase + TO * 4096, ST_AUX);
      __builtin_amdgcn_raw_buffer_store_b128(L.ob[1], c.rsrc, c.gvoff + 2048, c.obase + TO * 4096, ST_AUX);
    }
  }
  if (G == 2) {
    if (n == 0) stage_out(c, st.O[1], 0);
    if (n == 1) stage_out(c, st.O[2], 2048);
    if (n == 3) {
#pragma unroll
      for (int k = 0; k < 4; ++k) L.ob[k] = *(lds_u32x4_ptr)(c.ord + k * 1024);
    }
    if (n == 5) {
#pragma unroll
      for (int k = 0; k < 4; ++k)
        __builtin_amdgcn_raw_buffer_store_b128(L.ob[k], c.rsrc, c.gvoff + (k & 1) * 2048, c.obase + (1 + (k >> 1)) * 4096, ST_AUX);
    }
  }
  if (G == 1 || G == 3) {
    constexpr int TL = G == 1 ? 3 : 6;
    constexpr int CUT = G == 1 ? 10 : 14;
    if (n == 5) {
      L.QA[0] = sel4(cc >= CUT, st.Qp[TA][0], st.Qp[TL][0]);
      L.QA[1] = sel4(cc >= CUT, st.Qp[TA][1], st.Qp[TL][1]);
    }
    if (n == 8) {
      L.KA[0] = sel4(cc >= CUT, st.Kp[TA][0], st.Kp[TL][0]);
      L.KA[1] = sel4(cc >= CUT, st.Kp[TA][1], st.Kp[TL][1]);
    }
    if (n == 12) {
#pragma unroll
      for (int k = 0; k < 4; ++k) {
        L.VA[k][0] = (G == 1 && g == 3) ? st.Vp[TA][k][0] : st.Vp[TL][k][0];
        L.VA[k][1] = (G == 1 ? g >= 2 : g == 3) ? st.Vp[TA][k][1] : st.Vp[TL][k][1];
      }
    }
  } else {
    if (n == 5) { L.QA[0] = st.Qp[TA][0]; L.QA[1] = st.Qp[TA][1]; }
    if (n == 8) { L.KA[0] = st.Kp[TA][0]; L.KA[1] = st.Kp[TA][1]; }
    if (n == 12) {
#pragma unroll
      for (int k = 0; k < 4; ++k) L.VA[k] = st.Vp[TA][k];
    }
  }
  if (n == 9 || n == 10) {
    constexpr int qs = (n - 9) & 1;
#pragma unroll
    for (int ks = 0; ks < 2; ++ks)
#pragma unroll
      for (int i = 0; i < 2; ++i) {
        float lo = L.S[ks][qs][2 * i], hi = L.S[ks][qs][2 * i + 1];
        bool masked = false, ok = true;
        if (G == 0 && ks == 1) { masked = true; ok = i == 0 ? g < 3 : g < 2; }
        if (G == 1 && ks == 0) { masked = true; ok = i == 0 ? (g == 0 || g == 3) : g != 1; }
        if (G == 2 && ks == 0) { masked = true; ok = g >= 1; }
        if (G == 2 && ks == 1 && i == 1) { masked = true; ok = g < 3; }
        if (G == 3 && ks == 0) { masked = true; ok = i == 0 ? g < 2 : g != 2; }
        if (masked) { lo = ok ? lo : NINF; hi = ok ? hi : NINF; }
        L.sv[qs][4 * ks + 2 * i] = lo;
        L.sv[qs][4 * ks + 2 * i + 1] = hi;
      }
    float m = L.sv[qs][0];
#pragma unroll
    for (int i = 1; i < 8; ++i) m = fmaxf(m, L.sv[qs][i]);
    L.mx[qs] = xmax(m) * cexp;
  }
  if (n == 11 || n == 12) {
    constexpr int qs = (n - 11) & 1;
    float s = 0.f;
#pragma unroll
    for (int i = 0; i < 8; ++i) {
      L.sv[qs][i] = __builtin_amdgcn_exp2f(__builtin_fmaf(L.sv[qs][i], cexp, -L.mx[qs]));
      s += L.sv[qs][i];
    }
    L.sm[qs] = xsum(s);
  }
  if (n == 13 || n == 14) {
    constexpr int qs = (n - 13) & 1;
    float inv = __builtin_amdgcn_rcpf(L.sm[qs]);
    if (G == 0 && qs == 1) inv = cc < 10 ? inv : 0.f;
    if (G == 2 && qs == 0) inv = cc >= 4 ? inv : 0.f;
    if (G == 2 && qs == 1) inv = cc < 14 ? inv : 0.f;
#pragma unroll
    for (int i = 0; i < 4; ++i) L.Pp[qs][i] = pack2(L.sv[qs][2 * i] * inv, L.sv[qs][2 * i + 1] * inv);
  }
  if (n == 2 || n == 3) { PAT(NT, 2, nv * VS); }
  else { PAT(NT, 1, nv * VS); }
  if (n < 9 || n == 15) SBAR();
  if (G == 0 && (n == 1 || n == 5)) { LBAR(); }
  if (n == 8) {
    L.S[0][0] = MFMA(L.KA[0], L.QA[0], f32x4{});
    L.S[1][0] = MFMA(st.Kp[TB][0], L.QA[0], f32x4{});
    L.S[0][1] = MFMA(L.KA[0], st.Qp[TB][0], f32x4{});
    L.S[1][1] = MFMA(st.Kp[TB][0], st.Qp[TB][0], f32x4{});
    L.S[0][0] = MFMA(L.KA[1], L.QA[1], L.S[0][0]);
    L.S[1][0] = MFMA(st.Kp[TB][1], L.QA[1], L.S[1][0]);
    L.S[0][1] = MFMA(L.KA[1], st.Qp[TB][1], L.S[0][1]);
    L.S[1][1] = MFMA(st.Kp[TB][1], st.Qp[TB][1], L.S[1][1]);
    SBAR();
    if (G == 0) { LBAR(); }
  }
  if constexpr (n + 1 < 16) job<G, n + 1>(c, st, L);
}

template <int G>
DI void group_body(const Ctx& c, St& st) {
  constexpr int TA = G == 0 ? 0 : G == 1 ? 1 : G == 2 ? 3 : 4;
  constexpr int TB = G == 0 ? 1 : G == 1 ? 2 : G == 2 ? 4 : 5;
  const int cc = c.cc;
  Loc L;
  job<G, 0>(c, st, L);
  if (G < 3) {
    if (G == 0) { WAITV(0); } else if (G == 1) { WAITV(2); } else { WAITV(4); }
    SBAR();
    read_e<(G < 2 ? 2 : 1)>(c, st.E);
  }
  {
    u32x4 Vf[4];
#pragma unroll
    for (int j = 0; j < 4; ++j) {
      Vf[j][0] = L.VA[j][0]; Vf[j][1] = L.VA[j][1];
      Vf[j][2] = st.Vp[TB][j][0]; Vf[j][3] = st.Vp[TB][j][1];
    }
    const u32x4 z = {};
    if (G == 1 || G == 3) {
      constexpr int TL = G == 1 ? 3 : 6;
      constexpr int CUT = G == 1 ? 10 : 14;
      const u32x4 Ph = sel4(cc >= CUT, L.Pp[0], z);
      const u32x4 Pl = G == 1 ? sel4(cc < 4, L.Pp[0], z) : L.Pp[0];
#pragma unroll
      for (int j = 0; j < 4; ++j) st.O[TB][j] = MFMA(Vf[j], L.Pp[1], st.O[TB][j]);
#pragma unroll
      for (int j = 0; j < 4; ++j) st.O[TA][j] = MFMA(Vf[j], Ph, st.O[TA][j]);
#pragma unroll
      for (int j = 0; j < 4; ++j) st.O[TL][j] = MFMA(Vf[j], Pl, st.O[TL][j]);
    } else {
#pragma unroll
      for (int j = 0; j < 4; ++j) st.O[TA][j] = MFMA(Vf[j], L.Pp[0], st.O[TA][j]);
#pragma unroll
      for (int j = 0; j < 4; ++j) st.O[TB][j] = MFMA(Vf[j], L.Pp[1], st.O[TB][j]);
    }
  }
  SBAR();
  if (G < 2) {
    asm volatile("s_waitcnt lgkmcnt(0)" ::: "memory");
    dma_e<G + 2>(c);
    SBAR();
  }
}

__global__ __launch_bounds__(WAVES * 64, 2) void autoint_kernel(
    const __bf16* __restrict__ e, const __bf16* __restrict__ wq, const __bf16* __restrict__ wk,
    const __bf16* __restrict__ wv, const __bf16* __restrict__ wres, __bf16* __restrict__ out, unsigned long long* prof) {
  extern __shared__ __attribute__((aligned(16))) char smem[];
  const int tid = threadIdx.x;
  const int lane = tid & 63;
  const int wave = __builtin_amdgcn_readfirstlane(tid >> 6);
  const int cc = lane & 15, g = lane >> 4;
  const unsigned lds0 = __builtin_amdgcn_readfirstlane((unsigned)(size_t)(lds_void_ptr)smem);
  const int bid = blockIdx.x;
  const int hd = (bid >> 3) & 1;
  const int grp = (bid & 7) | ((bid >> 4) << 3);
  const int b0 = grp * BPG + wave * EPW;

  Ctx c;
  c.lane = lane;
  c.cc = cc;
  c.g = g;
  c.prof = prof + (size_t)(blockIdx.x * WAVES + wave) * 4;
  STAMP(0);
  c.ebuf = lds0 + 65536 + wave * 12288;
  c.ebase = (const char*)e + (size_t)b0 * EBYTES;
  c.obase = b0 * EBYTES;
  {
    const int c0 = cc ^ g;
#pragma unroll
    for (int j = 0; j < 4; ++j) c.evoff[j] = (unsigned)((g * 256 + (c0 << 4)) ^ (j << 6));
  }
  const bool leader = wave < 4;
  c.leader = leader;
  if (wave == CODEPF) {
    unsigned long long pc;
    asm volatile("s_getpc_b64 %0" : "=s"(pc));
    const unsigned t0 = lane * 128, t1 = t0 + 8192, t2 = 16384 + (lane < CPFN ? lane : CPFN) * 128;
    asm volatile(
        "s_mov_b32 m0, %4\n\ts_nop 0\n\t"
        "global_load_lds_dword %0, %3\n\t"
        "global_load_lds_dword %1, %3\n\t"
        "global_load_lds_dword %2, %3"
        :
        : "v"(t0), "v"(t1), "v"(t2), "s"(pc), "s"(c.ebuf + 8192)
        : "memory", "m0");
  }
  if (leader) {
    dma_e<0>(c);
  } else {
    unsigned o64[4], o128[4];
    const int r8 = lane >> 3;
    const int krow = 32 * (wave - 4) + 8 * (r8 >> 2) + (r8 & 3);
    const int ch = (lane & 7) ^ (r8 & 6);
#pragma unroll
    for (int i = 0; i < 4; ++i) {
      const int k = krow + 16 * (i & 1) + 4 * (i >> 1);
      o64[i] = (unsigned)(k * 128 + ch * 16 + 4096 - i * 1024);
      o128[i] = (unsigned)(k * 256 + ch * 16 + 4096 - i * 1024);
    }
    const char* sq = (const char*)wq + hd * 16384;
    const char* sk = (const char*)wk + hd * 16384;
    const char* sv = (const char*)wv + hd * 16384;
    const char* sr = (const char*)wres + hd * 128;
    const unsigned dst = lds0 + (wave - 4) * 4096;
    glds4(o64[0], o64[1], o64[2], o64[3], sq - 4096, dst);
    glds4(o64[0], o64[1], o64[2], o64[3], sk - 4096, dst + 16384);
    glds4(o64[0], o64[1], o64[2], o64[3], sv - 4096, dst + 32768);
    glds4(o128[0], o128[1], o128[2], o128[3], sr - 4096, dst + 49152);
    dma_e<0>(c);
  }
  SBAR();

  unsigned wb[4];
  {
    const int q4 = (lane & 15) >> 2, p4 = lane & 3;
    const int r8 = (g & 1) * 4 + q4;
#pragma unroll
    for (int j = 0; j < 4; ++j)
      wb[j] = lds0 + (wave & 3) * 4096 + (g >> 1) * 1024 + r8 * 128 + ((((2 * j) ^ (r8 & 6)) + (p4 >> 1)) << 4) + (p4 & 1) * 8;
    c.wl = lds0 + lane * 16;
    asm volatile("" : "+v"(c.wl));
    c.eread = c.ebuf + cc * 256 + ((g ^ cc) << 4);
    const unsigned obuf = c.ebuf + 8192;
    c.owr = obuf + cc * 128 + (((g >> 1) ^ (cc & 7)) << 4) + (g & 1) * 8;
    c.ord = obuf + lane * 16;
    const int orow = lane >> 3, oc = (lane & 7) ^ (orow & 7);
    c.gvoff = orow * 256 + hd * 128 + oc * 16;
    c.rsrc = __builtin_amdgcn_make_buffer_rsrc((void*)out, 0, NB_TOTAL * EBYTES, 0x00020000);
  }

  St st;
  STAMP(1);
  if (leader) {
    __builtin_amdgcn_s_barrier();
  } else {
    const unsigned wlw = c.wl + (wave & 3) * 4096;
    WAITV(20); relayout(wb, wlw, 0);     __builtin_amdgcn_s_barrier(); SBAR();
    WAITV(16); relayout(wb, wlw, 16384); __builtin_amdgcn_s_barrier(); SBAR();
    WAITV(12); relayout(wb, wlw, 32768); __builtin_amdgcn_s_barrier(); SBAR();
    WAITV(8);  relayout(wb, wlw, 49152); __builtin_amdgcn_s_barrier(); SBAR();
    __builtin_amdgcn_s_setprio(1);
  }
  WAITV(0);
  SBAR();
  STAMP(2);
  read_e<2>(c, st.E);
  loadw(c, st.W[0], 0);
  loadw(c, st.W[1], 1);
  asm volatile("s_waitcnt lgkmcnt(0)" ::: "memory");
  SBAR();
  dma_e<1>(c);
  SBAR();
  STAMP(3);

  group_body<0>(c, st);
  STAMP(4);
  group_body<1>(c, st);
  STAMP(5);
  group_body<2>(c, st);
  STAMP(6);
  __builtin_amdgcn_s_setprio(0);
  group_body<3>(c, st);
  STAMP(7);
  {
    stage_out(c, st.O[4], 0);
    stage_out(c, st.O[5], 2048);
    stage_out(c, st.O[6], (unsigned)-8192);
    u32x4 ob[5];
#pragma unroll
    for (int k = 0; k < 4; ++k) ob[k] = *(lds_u32x4_ptr)(c.ord + k * 1024);
    ob[4] = *(lds_u32x4_ptr)(c.ord - 8192);
#pragma unroll
    for (int k = 0; k < 4; ++k)
      __builtin_amdgcn_raw_buffer_store_b128(ob[k], c.rsrc, c.gvoff + (k & 1) * 2048, c.obase + (4 + (k >> 1)) * 4096, ST_AUX);
    __builtin_amdgcn_raw_buffer_store_b128(ob[4], c.rsrc, c.gvoff, c.obase + 6 * 4096, ST_AUX);
  }
  asm volatile("s_waitcnt vmcnt(0)" ::: "memory");
  STAMP(8);
  if (c.lane == 0) {
#pragma unroll
    for (int k = 0; k < 4; ++k) c.prof[k] = c.ts[k];
  }
}

__global__ void pre_kernel(unsigned long long* prof) {
  unsigned long long Ta;
  asm volatile("s_memrealtime %0\n\ts_waitcnt lgkmcnt(0)" : "=s"(Ta) :: "memory");
  if (threadIdx.x == 0) prof[65536] = Ta;
}
__global__ void diag_kernel(const unsigned long long* prof) {
  unsigned long long Tb;
  asm volatile("s_memrealtime %0\n\ts_waitcnt lgkmcnt(0)" : "=s"(Tb) :: "memory");
  __shared__ unsigned long long s0[256], s1[256], s2[256], s3[256], s4[256];
  const int t = threadIdx.x;
  unsigned long long mn = ~0ull, mx = 0, a1 = 0, a2 = 0, a3 = 0;
  const unsigned long long Ta = prof[0] - 100000;
  for (int i = t; i < 2048; i += 256) {
    unsigned long long a = prof[(size_t)i * 4], b = prof[(size_t)i * 4 + 3];
    mn = a < mn ? a : mn; mx = b > mx ? b : mx;
    a1 += prof[(size_t)i * 4 + 1] - Ta; a2 += prof[(size_t)i * 4 + 2] - Ta; a3 += b - Ta;
  }
  s0[t] = mn; s1[t] = mx; s2[t] = a1; s3[t] = a2; s4[t] = a3;
  __syncthreads();
  for (int s = 128; s > 0; s >>= 1) {
    if (t < s) { s0[t] = s0[t] < s0[t + s] ? s0[t] : s0[t + s]; s1[t] = s1[t] > s1[t + s] ? s1[t] : s1[t + s]; s2[t] += s2[t + s]; s3[t] += s3[t + s]; s4[t] += s4[t + s]; }
    __syncthreads();
  }
  mn = s0[0]; mx = s1[0];
  double r[8];
  r[0] = 0; r[1] = (double)(mn - Ta); r[2] = (double)(Tb - mx); r[3] = (double)(mx - mn);
  r[4] = (double)s2[0] / 2048 - r[1]; r[5] = (double)s3[0] / 2048 - r[1]; r[6] = (double)s4[0] / 2048 - r[1]; r[7] = 0;
  double v = r[DIAGM];
  asm volatile("" :: "v"(r[1]), "v"(r[2]), "v"(r[3]), "v"(r[4]), "v"(r[5]), "v"(r[6]));
  if (v < 0) v = 0; if (v > 20000) v = 20000;
  unsigned long long dur = (unsigned long long)(v * DIAGS);
  if (t == 0) {
    unsigned long long q0, q1;
    asm volatile("s_memrealtime %0\n\ts_waitcnt lgkmcnt(0)" : "=s"(q0) :: "memory");
    for (int it = 0; it < 1000000; ++it) {
      asm volatile("s_memrealtime %0\n\ts_waitcnt lgkmcnt(0)" : "=s"(q1) :: "memory");
      if (q1 - q0 >= dur) break;
    }
  }
}

extern "C" void kernel_launch(void* const* d_in, const int* in_sizes, int n_in, void* d_out, int out_size,
                              void* d_ws, size_t ws_size, hipStream_t stream) {
  const __bf16* e = (const __bf16*)d_in[0];
  const __bf16* wq = (const __bf16*)d_in[1];
  const __bf16* wk = (const __bf16*)d_in[2];
  const __bf16* wv = (const __bf16*)d_in[3];
  const __bf16* wres = (const __bf16*)d_in[4];
  __bf16* out = (__bf16*)d_out;
  (void)hipFuncSetAttribute((const void*)autoint_kernel, hipFuncAttributeMaxDynamicSharedMemorySize, LDS_BYTES);
  autoint_kernel<<<2 * NB_TOTAL / BPG, WAVES * 64, LDS_BYTES, stream>>>(e, wq, wk, wv, wres, out, (unsigned long long*)d_ws);
  diag_kernel<<<1, 256, 0, stream>>>((const unsigned long long*)d_ws);
}
```

```cpp
#define XMODE 0
#define DIAGS 0
#define CODEPF 3
#define CPFN 7
#include <hip/hip_runtime.h>
#include <hip/hip_bf16.h>

typedef __attribute__((ext_vector_type(8))) __bf16 bf16x8;
typedef __attribute__((ext_vector_type(2))) __bf16 bf16x2;
typedef __attribute__((ext_vector_type(4))) short s16x4;
typedef __attribute__((ext_vector_type(2))) short s16x2;
typedef __attribute__((ext_vector_type(4))) float f32x4;
typedef __attribute__((ext_vector_type(4))) unsigned u32x4;
typedef __attribute__((ext_vector_type(2))) unsigned u32x2;
typedef __attribute__((address_space(3))) s16x4* lds_s16x4_ptr;
typedef __attribute__((address_space(3))) u32x4* lds_u32x4_ptr;
typedef __attribute__((address_space(3))) u32x2* lds_u32x2_ptr;
typedef __attribute__((address_space(3))) void* lds_void_ptr;

#define DI __device__ __forceinline__
#define BF(x) __builtin_bit_cast(bf16x8, (x))
#define MFMA(a, b, c) __builtin_amdgcn_mfma_f32_16x16x32_bf16(BF(a), BF(b), (c), 0, 0, 0)

#ifndef NVS
#define NVS 3
#endif
#define PRIO 3
#define ST_AUX 17

constexpr int NB_TOTAL = 4096;
constexpr int NF = 26;
constexpr int ND = 128;
constexpr int WAVES = 8;
constexpr int EPW = 4;
constexpr int BPG = WAVES * EPW;
constexpr int EBYTES = NF * ND * 2;
constexpr int LDS_BYTES = 65536 + WAVES * 12288;

DI unsigned pack2(float lo, float hi) {
  bf16x2 v;
  v[0] = (__bf16)lo;
  v[1] = (__bf16)hi;
  return __builtin_bit_cast(unsigned, v);
}
DI unsigned relu2(float lo, float hi) {
  unsigned u = pack2(lo, hi);
  asm volatile("" : "+v"(u));
  s16x2 z = {};
  return __builtin_bit_cast(unsigned, __builtin_elementwise_max(__builtin_bit_cast(s16x2, u), z));
}

DI void glds4(unsigned v0, unsigned v1, unsigned v2, unsigned v3, const void* sbase, unsigned lds_dst) {
  asm volatile(
      "s_mov_b32 m0, %5\n\ts_nop 0\n\t"
      "global_load_lds_dwordx4 %0, %4\n\t"
      "global_load_lds_dwordx4 %1, %4 offset:1024\n\t"
      "global_load_lds_dwordx4 %2, %4 offset:2048\n\t"
      "global_load_lds_dwordx4 %3, %4 offset:3072"
      :
      : "v"(v0), "v"(v1), "v"(v2), "v"(v3), "s"(sbase), "s"(lds_dst)
      : "memory", "m0");
}
DI void glds2(unsigned v0, unsigned v1, const void* sbase, unsigned lds_dst) {
  asm volatile(
      "s_mov_b32 m0, %3\n\ts_nop 0\n\t"
      "global_load_lds_dwordx4 %0, %2\n\t"
      "global_load_lds_dwordx4 %1, %2 offset:1024"
      :
      : "v"(v0), "v"(v1), "s"(sbase), "s"(lds_dst)
      : "memory", "m0");
}

#define SGB(mask, n) __builtin_amdgcn_sched_group_barrier(mask, n, 0)
#define SBAR() __builtin_amdgcn_sched_barrier(0)
#define WAITV(n) asm volatile("s_waitcnt vmcnt(" #n ")" ::: "memory");
#define WAITW(n)                  \
  WAITV(n)                        \
  __builtin_amdgcn_s_barrier();   \
  __builtin_amdgcn_sched_barrier(0);

#define STAMP(k)
#define LBAR()                                      \
  if (c.leader) __builtin_amdgcn_s_barrier();       \
  __builtin_amdgcn_sched_barrier(0);

struct Ctx {
  bool leader;
  unsigned long long* prof;
  unsigned wl;
  unsigned eread;
  unsigned ebuf;
  unsigned owr;
  unsigned ord;
  unsigned evoff[4];
  int gvoff;
  int lane, cc, g;
  const char* ebase;
  int obase;
  __amdgpu_buffer_rsrc_t rsrc;
};

struct St {
  u32x4 E[2][4];
  u32x4 W[3][4];
  u32x4 Qp[7][2], Kp[7][2];
  u32x2 Vp[7][4];
  f32x4 O[7][4];
};

DI void loadw(const Ctx& c, u32x4 (&W)[4], int n) {
  const int X = n >> 2, j = n & 3;
#pragma unroll
  for (int s = 0; s < 4; ++s) W[s] = *(lds_u32x4_ptr)(c.wl + X * 16384 + s * 4096 + j * 1024);
}
DI void relayout(const unsigned (&wb)[4], unsigned wl, int off) {
  u32x4 W[4];
#pragma unroll
  for (int j = 0; j < 4; ++j) {
    s16x4 lo = __builtin_amdgcn_ds_read_tr16_b64_v4i16((lds_s16x4_ptr)(wb[j] + off));
    s16x4 hi = __builtin_amdgcn_ds_read_tr16_b64_v4i16((lds_s16x4_ptr)(wb[j] + off + 2048));
    W[j] = __builtin_bit_cast(u32x4, __builtin_shufflevector(lo, hi, 0, 1, 2, 3, 4, 5, 6, 7));
  }
  asm volatile("s_waitcnt lgkmcnt(0)" ::: "memory");
#pragma unroll
  for (int j = 0; j < 4; ++j) *(lds_u32x4_ptr)(wl + off + j * 1024) = W[j];
  asm volatile("s_waitcnt lgkmcnt(0)" ::: "memory");
}

template <int G>
DI void dma_e(const Ctx& c) {
  const char* p = c.ebase + (size_t)G * 8192;
  if (G < 3) {
    glds4(c.evoff[0], c.evoff[1], c.evoff[2], c.evoff[3], p, c.ebuf);
    glds4(c.evoff[0], c.evoff[1], c.evoff[2], c.evoff[3], p + 4096, c.ebuf + 4096);
  } else {
    glds2(c.evoff[0], c.evoff[1], p, c.ebuf);
  }
}

template <int NT>
DI void read_e(const Ctx& c, u32x4 (&E)[2][4]) {
#pragma unroll
  for (int s = 0; s < 4; ++s) {
    const unsigned a = c.eread ^ (s << 6);
    E[0][s] = *(lds_u32x4_ptr)(a);
    if (NT > 1) E[1][s] = *(lds_u32x4_ptr)(a + 4096);
  }
}

DI void stage_out(const Ctx& c, const f32x4 (&O)[4], unsigned slot) {
#pragma unroll
  for (int j = 0; j < 4; ++j) {
    u32x2 v;
    v[0] = relu2(O[j][0], O[j][1]);
    v[1] = relu2(O[j][2], O[j][3]);
    *(lds_u32x2_ptr)((c.owr ^ (j << 5)) + slot) = v;
  }
}

DI float xmax(float m) {
  auto a = __builtin_amdgcn_permlane32_swap(__float_as_uint(m), __float_as_uint(m), false, false);
  asm("v_max_f32 %0, %1, %2" : "=v"(m) : "v"(a[0]), "v"(a[1]));
  auto b = __builtin_amdgcn_permlane16_swap(__float_as_uint(m), __float_as_uint(m), false, false);
  asm("v_max_f32 %0, %1, %2" : "=v"(m) : "v"(b[0]), "v"(b[1]));
  return m;
}
DI float xsum(float m) {
  auto a = __builtin_amdgcn_permlane32_swap(__float_as_uint(m), __float_as_uint(m), false, false);
  m = __uint_as_float(a[0]) + __uint_as_float(a[1]);
  auto b = __builtin_amdgcn_permlane16_swap(__float_as_uint(m), __float_as_uint(m), false, false);
  return __uint_as_float(b[0]) + __uint_as_float(b[1]);
}
DI u32x4 sel4(bool p, const u32x4& a, const u32x4& b) {
  u32x4 r;
#pragma unroll
  for (int i = 0; i < 4; ++i) r[i] = p ? a[i] : b[i];
  return r;
}

#define PAT(NT_, ND_, NV)                          \
  _Pragma("unroll") for (int q_ = 0; q_ < 4; ++q_) { \
    SGB(0x8, 1);                                   \
    SGB(0x100, ND_);                               \
    SGB(0x2, NV);                                  \
    if ((NT_) > 1) { SGB(0x8, 1); SGB(0x2, NV); }  \
    if ((NT_) > 2) { SGB(0x8, 1); SGB(0x2, NV); }  \
  }

struct Loc {
  f32x4 acc[2][2];
  f32x4 S[2][2];
  float sv[2][8], mx[2], sm[2];
  u32x4 Pp[2];
  u32x4 ob[4];
  u32x4 QA[2], KA[2];
  u32x2 VA[4];
};

template <int G, int n>
DI void job(const Ctx& c, St& st, Loc& L) {
  constexpr int NT = G < 3 ? 2 : 1;
  constexpr int T0 = 2 * G;
  constexpr int NM = 4 * NT;
  constexpr int NDS = 8 / NM;
  constexpr int VS = 2 / NT;
  constexpr int WB = (16 * G) % 3;
  constexpr int TA = G == 0 ? 0 : G == 1 ? 1 : G == 2 ? 3 : 4;
  constexpr int TB = G == 0 ? 1 : G == 1 ? 2 : G == 2 ? 4 : 5;
  constexpr int X = n >> 2, j = n & 3;
  const float cexp = 0.125f * 1.4426950408889634f;
  const float NINF = -__builtin_inff();
  const int g = c.g, cc = c.cc;
  if (n + 2 < 16) loadw(c, st.W[(WB + n + 2) % 3], n + 2);
  else if (G < 3) loadw(c, st.W[(WB + n + 2) % 3], n + 2 - 16);
  const u32x4(&W)[4] = st.W[(WB + n) % 3];
#pragma unroll
  for (int tt = 0; tt < NT; ++tt) L.acc[n & 1][tt] = f32x4{};
#pragma unroll
  for (int s = 0; s < 4; ++s)
#pragma unroll
    for (int tt = 0; tt < NT; ++tt)
      L.acc[n & 1][tt] = X == 2 ? MFMA(st.E[tt][s], W[s], L.acc[n & 1][tt]) : MFMA(W[s], st.E[tt][s], L.acc[n & 1][tt]);
  if (X == 3) {
#pragma unroll
    for (int tt = 0; tt < NT; ++tt) st.O[T0 + tt][j] = L.acc[n & 1][tt];
  }
  if (n > 0 && n <= 12) {
    constexpr int pX = (n - 1) >> 2, pj = (n - 1) & 3;
#pragma unroll
    for (int tt = 0; tt < NT; ++tt) {
      const f32x4 a = L.acc[(n - 1) & 1][tt];
      const unsigned lo = pack2(a[0], a[1]), hi = pack2(a[2], a[3]);
      if (pX == 0) { st.Qp[T0 + tt][pj >> 1][2 * (pj & 1)] = lo; st.Qp[T0 + tt][pj >> 1][2 * (pj & 1) + 1] = hi; }
      if (pX == 1) { st.Kp[T0 + tt][pj >> 1][2 * (pj & 1)] = lo; st.Kp[T0 + tt][pj >> 1][2 * (pj & 1) + 1] = hi; }
      if (pX == 2) { st.Vp[T0 + tt][pj][0] = lo; st.Vp[T0 + tt][pj][1] = hi; }
    }
  }
  constexpr int nv = (G > 0 && n == 0) || (G == 2 && n == 1) ? 3
                     : ((G & 1) && (n == 5 || n == 8)) ? 2
                     : (n >= 9) ? NVS : 1;
  if (G == 1 || G == 3) {
    constexpr int TO = G == 1 ? 0 : 3;
    if (n == 0) stage_out(c, st.O[TO], 0);
    if (n == 2) { L.ob[0] = *(lds_u32x4_ptr)(c.ord); L.ob[1] = *(lds_u32x4_ptr)(c.ord + 1024); }
    if (n == 4) {
      __builtin_amdgcn_raw_buffer_store_b128(L.ob[0], c.rsrc, c.gvoff, c.obase + TO * 4096, ST_AUX);
      __builtin_amdgcn_raw_buffer_store_b128(L.ob[1], c.rsrc, c.gvoff + 2048, c.obase + TO * 4096, ST_AUX);
    }
  }
  if (G == 2) {
    if (n == 0) stage_out(c, st.O[1], 0);
    if (n == 1) stage_out(c, st.O[2], 2048);
    if (n == 3) {
#pragma unroll
      for (int k = 0; k < 4; ++k) L.ob[k] = *(lds_u32x4_ptr)(c.ord + k * 1024);
    }
    if (n == 5) {
#pragma unroll
      for (int k = 0; k < 4; ++k)
        __builtin_amdgcn_raw_buffer_store_b128(L.ob[k], c.rsrc, c.gvoff + (k & 1) * 2048, c.obase + (1 + (k >> 1)) * 4096, ST_AUX);
    }
  }
  if (G == 1 || G == 3) {
    constexpr int TL = G == 1 ? 3 : 6;
    constexpr int CUT = G == 1 ? 10 : 14;
    if (n == 5) {
      L.QA[0] = sel4(cc >= CUT, st.Qp[TA][0], st.Qp[TL][0]);
      L.QA[1] = sel4(cc >= CUT, st.Qp[TA][1], st.Qp[TL][1]);
    }
    if (n == 8) {
      L.KA[0] = sel4(cc >= CUT, st.Kp[TA][0], st.Kp[TL][0]);
      L.KA[1] = sel4(cc >= CUT, st.Kp[TA][1], st.Kp[TL][1]);
    }
    if (n == 12) {
#pragma unroll
      for (int k = 0; k < 4; ++k) {
        L.VA[k][0] = (G == 1 && g == 3) ? st.Vp[TA][k][0] : st.Vp[TL][k][0];
        L.VA[k][1] = (G == 1 ? g >= 2 : g == 3) ? st.Vp[TA][k][1] : st.Vp[TL][k][1];
      }
    }
  } else {
    if (n == 5) { L.QA[0] = st.Qp[TA][0]; L.QA[1] = st.Qp[TA][1]; }
    if (n == 8) { L.KA[0] = st.Kp[TA][0]; L.KA[1] = st.Kp[TA][1]; }
    if (n == 12) {
#pragma unroll
      for (int k = 0; k < 4; ++k) L.VA[k] = st.Vp[TA][k];
    }
  }
  if (n == 9 || n == 10) {
    constexpr int qs = (n - 9) & 1;
#pragma unroll
    for (int ks = 0; ks < 2; ++ks)
#pragma unroll
      for (int i = 0; i < 2; ++i) {
        float lo = L.S[ks][qs][2 * i], hi = L.S[ks][qs][2 * i + 1];
        bool masked = false, ok = true;
        if (G == 0 && ks == 1) { masked = true; ok = i == 0 ? g < 3 : g < 2; }
        if (G == 1 && ks == 0) { masked = true; ok = i == 0 ? (g == 0 || g == 3) : g != 1; }
        if (G == 2 && ks == 0) { masked = true; ok = g >= 1; }
        if (G == 2 && ks == 1 && i == 1) { masked = true; ok = g < 3; }
        if (G == 3 && ks == 0) { masked = true; ok = i == 0 ? g < 2 : g != 2; }
        if (masked) { lo = ok ? lo : NINF; hi = ok ? hi : NINF; }
        L.sv[qs][4 * ks + 2 * i] = lo;
        L.sv[qs][4 * ks + 2 * i + 1] = hi;
      }
    float m = L.sv[qs][0];
#pragma unroll
    for (int i = 1; i < 8; ++i) m = fmaxf(m, L.sv[qs][i]);
    L.mx[qs] = xmax(m) * cexp;
  }
  if (n == 11 || n == 12) {
    constexpr int qs = (n - 11) & 1;
    float s = 0.f;
#pragma unroll
    for (int i = 0; i < 8; ++i) {
      L.sv[qs][i] = __builtin_amdgcn_exp2f(__builtin_fmaf(L.sv[qs][i], cexp, -L.mx[qs]));
      s += L.sv[qs][i];
    }
    L.sm[qs] = xsum(s);
  }
  if (n == 13 || n == 14) {
    constexpr int qs = (n - 13) & 1;
    float inv = __builtin_amdgcn_rcpf(L.sm[qs]);
    if (G == 0 && qs == 1) inv = cc < 10 ? inv : 0.f;
    if (G == 2 && qs == 0) inv = cc >= 4 ? inv : 0.f;
    if (G == 2 && qs == 1) inv = cc < 14 ? inv : 0.f;
#pragma unroll
    for (int i = 0; i < 4; ++i) L.Pp[qs][i] = pack2(L.sv[qs][2 * i] * inv, L.sv[qs][2 * i + 1] * inv);
  }
  if (n == 2 || n == 3) { PAT(NT, 2, nv * VS); }
  else { PAT(NT, 1, nv * VS); }
  if (n < 9 || n == 15) SBAR();
  if (G == 0 && (n == 1 || n == 5)) { LBAR(); }
  if (n == 8) {
    L.S[0][0] = MFMA(L.KA[0], L.QA[0], f32x4{});
    L.S[1][0] = MFMA(st.Kp[TB][0], L.QA[0], f32x4{});
    L.S[0][1] = MFMA(L.KA[0], st.Qp[TB][0], f32x4{});
    L.S[1][1] = MFMA(st.Kp[TB][0], st.Qp[TB][0], f32x4{});
    L.S[0][0] = MFMA(L.KA[1], L.QA[1], L.S[0][0]);
    L.S[1][0] = MFMA(st.Kp[TB][1], L.QA[1], L.S[1][0]);
    L.S[0][1] = MFMA(L.KA[1], st.Qp[TB][1], L.S[0][1]);
    L.S[1][1] = MFMA(st.Kp[TB][1], st.Qp[TB][1], L.S[1][1]);
    SBAR();
    if (G == 0) { LBAR(); }
  }
  if constexpr (n + 1 < 16) job<G, n + 1>(c, st, L);
}

template <int G>
DI void group_body(const Ctx& c, St& st) {
  constexpr int TA = G == 0 ? 0 : G == 1 ? 1 : G == 2 ? 3 : 4;
  constexpr int TB = G == 0 ? 1 : G == 1 ? 2 : G == 2 ? 4 : 5;
  const int cc = c.cc;
  Loc L;
  job<G, 0>(c, st, L);
  if (G < 3) {
    if (G == 0) { WAITV(0); } else if (G == 1) { WAITV(2); } else { WAITV(4); }
    SBAR();
    read_e<(G < 2 ? 2 : 1)>(c, st.E);
  }
  {
    u32x4 Vf[4];
#pragma unroll
    for (int j = 0; j < 4; ++j) {
      Vf[j][0] = L.VA[j][0]; Vf[j][1] = L.VA[j][1];
      Vf[j][2] = st.Vp[TB][j][0]; Vf[j][3] = st.Vp[TB][j][1];
    }
    const u32x4 z = {};
    if (G == 1 || G == 3) {
      constexpr int TL = G == 1 ? 3 : 6;
      constexpr int CUT = G == 1 ? 10 : 14;
      const u32x4 Ph = sel4(cc >= CUT, L.Pp[0], z);
      const u32x4 Pl = G == 1 ? sel4(cc < 4, L.Pp[0], z) : L.Pp[0];
#pragma unroll
      for (int j = 0; j < 4; ++j) st.O[TB][j] = MFMA(Vf[j], L.Pp[1], st.O[TB][j]);
#pragma unroll
      for (int j = 0; j < 4; ++j) st.O[TA][j] = MFMA(Vf[j], Ph, st.O[TA][j]);
#pragma unroll
      for (int j = 0; j < 4; ++j) st.O[TL][j] = MFMA(Vf[j], Pl, st.O[TL][j]);
    } else {
#pragma unroll
      for (int j = 0; j < 4; ++j) st.O[TA][j] = MFMA(Vf[j], L.Pp[0], st.O[TA][j]);
#pragma unroll
      for (int j = 0; j < 4; ++j) st.O[TB][j] = MFMA(Vf[j], L.Pp[1], st.O[TB][j]);
    }
  }
  SBAR();
  if (G < 2) {
    asm volatile("s_waitcnt lgkmcnt(0)" ::: "memory");
    dma_e<G + 2>(c);
    SBAR();
  }
}

__global__ __launch_bounds__(WAVES * 64, 2) void autoint_kernel(
    const __bf16* __restrict__ e, const __bf16* __restrict__ wq, const __bf16* __restrict__ wk,
    const __bf16* __restrict__ wv, const __bf16* __restrict__ wres, __bf16* __restrict__ out, unsigned long long* prof) {
  extern __shared__ __attribute__((aligned(16))) char smem[];
  const int tid = threadIdx.x;
  const int lane = tid & 63;
  const int wave = __builtin_amdgcn_readfirstlane(tid >> 6);
  const int cc = lane & 15, g = lane >> 4;
  const unsigned lds0 = __builtin_amdgcn_readfirstlane((unsigned)(size_t)(lds_void_ptr)smem);
  const int bid = blockIdx.x;
  const int hd = (bid >> 3) & 1;
  const int grp = (bid & 7) | ((bid >> 4) << 3);
  const int b0 = grp * BPG + wave * EPW;

  Ctx c;
  c.lane = lane;
  c.cc = cc;
  c.g = g;
  c.prof = prof + (size_t)(blockIdx.x * WAVES + wave) * 32;
  STAMP(0);
  c.ebuf = lds0 + 65536 + wave * 12288;
  c.ebase = (const char*)e + (size_t)b0 * EBYTES;
  c.obase = b0 * EBYTES;
  {
    const int c0 = cc ^ g;
#pragma unroll
    for (int j = 0; j < 4; ++j) c.evoff[j] = (unsigned)((g * 256 + (c0 << 4)) ^ (j << 6));
  }
  const bool leader = wave < 4;
  c.leader = leader;
  if (wave == CODEPF) {
    unsigned long long pc;
    asm volatile("s_getpc_b64 %0" : "=s"(pc));
    const unsigned t0 = lane * 128, t1 = t0 + 8192, t2 = 16384 + (lane < CPFN ? lane : CPFN) * 128;
    asm volatile(
        "s_mov_b32 m0, %4\n\ts_nop 0\n\t"
        "global_load_lds_dword %0, %3\n\t"
        "global_load_lds_dword %1, %3\n\t"
        "global_load_lds_dword %2, %3"
        :
        : "v"(t0), "v"(t1), "v"(t2), "s"(pc), "s"(c.ebuf + 8192)
        : "memory", "m0");
  }
  if (leader) {
    dma_e<0>(c);
  } else {
    unsigned o64[4], o128[4];
    const int r8 = lane >> 3;
    const int krow = 32 * (wave - 4) + 8 * (r8 >> 2) + (r8 & 3);
    const int ch = (lane & 7) ^ (r8 & 6);
#pragma unroll
    for (int i = 0; i < 4; ++i) {
      const int k = krow + 16 * (i & 1) + 4 * (i >> 1);
      o64[i] = (unsigned)(k * 128 + ch * 16 + 4096 - i * 1024);
      o128[i] = (unsigned)(k * 256 + ch * 16 + 4096 - i * 1024);
    }
    const char* sq = (const char*)wq + hd * 16384;
    const char* sk = (const char*)wk + hd * 16384;
    const char* sv = (const char*)wv + hd * 16384;
    const char* sr = (const char*)wres + hd * 128;
    const unsigned dst = lds0 + (wave - 4) * 4096;
    glds4(o64[0], o64[1], o64[2], o64[3], sq - 4096, dst);
    glds4(o64[0], o64[1], o64[2], o64[3], sk - 4096, dst + 16384);
    glds4(o64[0], o64[1], o64[2], o64[3], sv - 4096, dst + 32768);
    glds4(o128[0], o128[1], o128[2], o128[3], sr - 4096, dst + 49152);
    dma_e<0>(c);
  }
  SBAR();

  unsigned wb[4];
  {
    const int q4 = (lane & 15) >> 2, p4 = lane & 3;
    const int r8 = (g & 1) * 4 + q4;
#pragma unroll
    for (int j = 0; j < 4; ++j)
      wb[j] = lds0 + (wave & 3) * 4096 + (g >> 1) * 1024 + r8 * 128 + ((((2 * j) ^ (r8 & 6)) + (p4 >> 1)) << 4) + (p4 & 1) * 8;
    c.wl = lds0 + lane * 16;
    asm volatile("" : "+v"(c.wl));
    c.eread = c.ebuf + cc * 256 + ((g ^ cc) << 4);
    const unsigned obuf = c.ebuf + 8192;
    c.owr = obuf + cc * 128 + (((g >> 1) ^ (cc & 7)) << 4) + (g & 1) * 8;
    c.ord = obuf + lane * 16;
    const int orow = lane >> 3, oc = (lane & 7) ^ (orow & 7);
    c.gvoff = orow * 256 + hd * 128 + oc * 16;
    c.rsrc = __builtin_amdgcn_make_buffer_rsrc((void*)out, 0, NB_TOTAL * EBYTES, 0x00020000);
  }

  St st;
  STAMP(1);
  if (leader) {
    __builtin_amdgcn_s_barrier();
  } else {
    const unsigned wlw = c.wl + (wave & 3) * 4096;
    WAITV(20); relayout(wb, wlw, 0);     __builtin_amdgcn_s_barrier(); SBAR();
    WAITV(16); relayout(wb, wlw, 16384); __builtin_amdgcn_s_barrier(); SBAR();
    WAITV(12); relayout(wb, wlw, 32768); __builtin_amdgcn_s_barrier(); SBAR();
    WAITV(8);  relayout(wb, wlw, 49152); __builtin_amdgcn_s_barrier(); SBAR();
    __builtin_amdgcn_s_setprio(1);
  }
  WAITV(0);
  SBAR();
  STAMP(2);
  read_e<2>(c, st.E);
  loadw(c, st.W[0], 0);
  loadw(c, st.W[1], 1);
  asm volatile("s_waitcnt lgkmcnt(0)" ::: "memory");
  SBAR();
  dma_e<1>(c);
  SBAR();
  STAMP(3);

  group_body<0>(c, st);
  STAMP(4);
  group_body<1>(c, st);
  STAMP(5);
  group_body<2>(c, st);
  STAMP(6);
  __builtin_amdgcn_s_setprio(0);
  group_body<3>(c, st);
  STAMP(7);
  {
    stage_out(c, st.O[4], 0);
    stage_out(c, st.O[5], 2048);
    stage_out(c, st.O[6], (unsigned)-8192);
    u32x4 ob[5];
#pragma unroll
    for (int k = 0; k < 4; ++k) ob[k] = *(lds_u32x4_ptr)(c.ord + k * 1024);
    ob[4] = *(lds_u32x4_ptr)(c.ord - 8192);
#pragma unroll
    for (int k = 0; k < 4; ++k)
      __builtin_amdgcn_raw_buffer_store_b128(ob[k], c.rsrc, c.gvoff + (k & 1) * 2048, c.obase + (4 + (k >> 1)) * 4096, ST_AUX);
    __builtin_amdgcn_raw_buffer_store_b128(ob[4], c.rsrc, c.gvoff, c.obase + 6 * 4096, ST_AUX);
  }
  STAMP(8);
}

__global__ __launch_bounds__(512) void exp_kernel(const char* e, unsigned long long* prof) {
  const int lane = threadIdx.x & 63, wave = threadIdx.x >> 6, bid = blockIdx.x;
  unsigned long long t0, t1;
  asm volatile("s_memrealtime %0\n\ts_waitcnt lgkmcnt(0)" : "=s"(t0) :: "memory");
  u32x4 acc = {};
  if (XMODE == 0 || XMODE == 3 || XMODE == 4) {
    const char* p = e + (size_t)(bid * 8 + wave) * 13312 + lane * 16;
    u32x4 a[13];
#pragma unroll
    for (int k = 0; k < 13; ++k) {
      a[k] = *(const u32x4*)(p + k * 1024);
      if ((XMODE == 3 && (k & 3) == 3) || (XMODE == 4 && k == 7)) { asm volatile("s_waitcnt vmcnt(0)" ::: "memory"); __builtin_amdgcn_sched_barrier(0); }
    }
#pragma unroll
    for (int k = 0; k < 13; ++k) acc ^= a[k];
  } else {
    const int grp = (bid & 7) | ((bid >> 4) << 3);
    const char* p = e + (size_t)(grp * 8 + wave) * 26624 + lane * 16;
    u32x4 a[26];
#pragma unroll
    for (int k = 0; k < 26; ++k) {
      a[k] = *(const u32x4*)(p + k * 1024);
      if (XMODE == 2 && (k & 7) == 7) { asm volatile("s_waitcnt vmcnt(0)" ::: "memory"); __builtin_amdgcn_sched_barrier(0); }
    }
#pragma unroll
    for (int k = 0; k < 26; ++k) acc ^= a[k];
  }
  asm volatile("" :: "v"(acc));
  asm volatile("s_waitcnt vmcnt(0)\n\ts_memrealtime %0\n\ts_waitcnt lgkmcnt(0)" : "=s"(t1) :: "memory");
  if (lane == 0) { prof[(bid * 8 + wave) * 2] = t0; prof[(bid * 8 + wave) * 2 + 1] = t1; }
}
__global__ void diag_kernel(const unsigned long long* prof) {
  __shared__ unsigned long long s0[256], s1[256];
  const int t = threadIdx.x;
  unsigned long long mn = ~0ull, mx = 0;
  for (int i = t; i < 2048; i += 256) {
    unsigned long long a = prof[2 * i], b = prof[2 * i + 1];
    mn = a < mn ? a : mn; mx = b > mx ? b : mx;
  }
  s0[t] = mn; s1[t] = mx;
  __syncthreads();
  for (int s = 128; s > 0; s >>= 1) {
    if (t < s) { s0[t] = s0[t] < s0[t + s] ? s0[t] : s0[t + s]; s1[t] = s1[t] > s1[t + s] ? s1[t] : s1[t + s]; }
    __syncthreads();
  }
  double v = (double)(s1[0] - s0[0]);
  if (v < 0) v = 0; if (v > 20000) v = 20000;
  unsigned long long dur = (unsigned long long)(v * DIAGS);
  if (t == 0) {
    unsigned long long q0, q1;
    asm volatile("s_memrealtime %0\n\ts_waitcnt lgkmcnt(0)" : "=s"(q0) :: "memory");
    for (int it = 0; it < 1000000; ++it) {
      asm volatile("s_memrealtime %0\n\ts_waitcnt lgkmcnt(0)" : "=s"(q1) :: "memory");
      if (q1 - q0 >= dur) break;
    }
  }
}

extern "C" void kernel_launch(void* const* d_in, const int* in_sizes, int n_in, void* d_out, int out_size,
                              void* d_ws, size_t ws_size, hipStream_t stream) {
  const __bf16* e = (const __bf16*)d_in[0];
  const __bf16* wq = (const __bf16*)d_in[1];
  const __bf16* wk = (const __bf16*)d_in[2];
  const __bf16* wv = (const __bf16*)d_in[3];
  const __bf16* wres = (const __bf16*)d_in[4];
  __bf16* out = (__bf16*)d_out;
  (void)hipFuncSetAttribute((const void*)autoint_kernel, hipFuncAttributeMaxDynamicSharedMemorySize, LDS_BYTES);
  exp_kernel<<<256, 512, 0, stream>>>((const char*)d_in[0], (unsigned long long*)d_ws);
  diag_kernel<<<1, 256, 0, stream>>>((const unsigned long long*)d_ws);
  autoint_kernel<<<2 * NB_TOTAL / BPG, WAVES * 64, LDS_BYTES, stream>>>(e, wq, wk, wv, wres, out, (unsigned long long*)d_ws);
}
```

```cpp
#define DIAGM 0
#define DIAGS 10
#define KSTAMP c.ts[0] = tA_; c.ts[1] = tB_; c.ts[2] = tA_;
#define CODEPF 7
#define CPFN 7
#include <hip/hip_runtime.h>
#include <hip/hip_bf16.h>

typedef __attribute__((ext_vector_type(8))) __bf16 bf16x8;
typedef __attribute__((ext_vector_type(2))) __bf16 bf16x2;
typedef __attribute__((ext_vector_type(4))) short s16x4;
typedef __attribute__((ext_vector_type(2))) short s16x2;
typedef __attribute__((ext_vector_type(4))) float f32x4;
typedef __attribute__((ext_vector_type(4))) unsigned u32x4;
typedef __attribute__((ext_vector_type(2))) unsigned u32x2;
typedef __attribute__((address_space(3))) s16x4* lds_s16x4_ptr;
typedef __attribute__((address_space(3))) u32x4* lds_u32x4_ptr;
typedef __attribute__((address_space(3))) u32x2* lds_u32x2_ptr;
typedef __attribute__((address_space(3))) void* lds_void_ptr;

#define DI __device__ __forceinline__
#define BF(x) __builtin_bit_cast(bf16x8, (x))
#define MFMA(a, b, c) __builtin_amdgcn_mfma_f32_16x16x32_bf16(BF(a), BF(b), (c), 0, 0, 0)

#define DJ 2
#define HORD 2
#define LPRIO 2
#ifndef NVS
#define NVS 3
#endif
#define PRIO 3
#define ST_AUX 17

constexpr int NB_TOTAL = 4096;
constexpr int NF = 26;
constexpr int ND = 128;
constexpr int WAVES = 8;
constexpr int EPW = 4;
constexpr int BPG = WAVES * EPW;
constexpr int EBYTES = NF * ND * 2;
constexpr int LDS_BYTES = 65536 + WAVES * 12288;

DI unsigned pack2(float lo, float hi) {
  bf16x2 v;
  v[0] = (__bf16)lo;
  v[1] = (__bf16)hi;
  return __builtin_bit_cast(unsigned, v);
}
DI unsigned relu2(float lo, float hi) {
  unsigned u = pack2(lo, hi);
  asm volatile("" : "+v"(u));
  s16x2 z = {};
  return __builtin_bit_cast(unsigned, __builtin_elementwise_max(__builtin_bit_cast(s16x2, u), z));
}

DI void glds4(unsigned v0, unsigned v1, unsigned v2, unsigned v3, const void* sbase, unsigned lds_dst) {
  asm volatile(
      "s_mov_b32 m0, %5\n\ts_nop 0\n\t"
      "global_load_lds_dwordx4 %0, %4\n\t"
      "global_load_lds_dwordx4 %1, %4 offset:1024\n\t"
      "global_load_lds_dwordx4 %2, %4 offset:2048\n\t"
      "global_load_lds_dwordx4 %3, %4 offset:3072"
      :
      : "v"(v0), "v"(v1), "v"(v2), "v"(v3), "s"(sbase), "s"(lds_dst)
      : "memory", "m0");
}
DI void glds2(unsigned v0, unsigned v1, const void* sbase, unsigned lds_dst) {
  asm volatile(
      "s_mov_b32 m0, %3\n\ts_nop 0\n\t"
      "global_load_lds_dwordx4 %0, %2\n\t"
      "global_load_lds_dwordx4 %1, %2 offset:1024"
      :
      : "v"(v0), "v"(v1), "s"(sbase), "s"(lds_dst)
      : "memory", "m0");
}

#define SGB(mask, n) __builtin_amdgcn_sched_group_barrier(mask, n, 0)
#define SBAR() __builtin_amdgcn_sched_barrier(0)
#define WAITV(n) asm volatile("s_waitcnt vmcnt(" #n ")" ::: "memory");
#define WAITW(n)                  \
  WAITV(n)                        \
  __builtin_amdgcn_s_barrier();   \
  __builtin_amdgcn_sched_barrier(0);

#define STAMP(k) if (k == 0 || k == 2 || k == 7 || k == 8) { asm volatile("s_memrealtime %0\n\ts_waitcnt lgkmcnt(0)" : "=s"(c.ts[k == 0 ? 0 : k == 2 ? 1 : k == 7 ? 2 : 3]) :: "memory"); __builtin_amdgcn_sched_barrier(0); }
DI void poll4(unsigned v, unsigned a) {
  unsigned s_, t_;
  asm volatile(
      "v_readfirstlane_b32 %0, %2\n\t"
      "s_cmp_ge_u32 %0, 4\n\t"
      "s_cbranch_scc1 2f\n"
      "1:\n\t"
      "ds_read_b32 %1, %3\n\t"
      "s_waitcnt lgkmcnt(0)\n\t"
      "v_readfirstlane_b32 %0, %1\n\t"
      "s_cmp_ge_u32 %0, 4\n\t"
      "s_cbranch_scc0 1b\n"
      "2:"
      : "=&s"(s_), "=&v"(t_)
      : "v"(v), "v"(a)
      : "memory", "scc");
}
typedef volatile __attribute__((address_space(3))) unsigned* lds_vu32_ptr;

struct Ctx {
  mutable unsigned long long ts[4];
  bool leader;
  unsigned flag;
  unsigned long long* prof;
  unsigned wl;
  unsigned eread;
  unsigned ebuf;
  unsigned owr;
  unsigned ord;
  unsigned evoff[4];
  int gvoff;
  int lane, cc, g;
  const char* ebase;
  int obase;
  __amdgpu_buffer_rsrc_t rsrc;
};

struct St {
  u32x4 E[2][4];
  u32x4 W[3][4];
  u32x4 Qp[7][2], Kp[7][2];
  u32x2 Vp[7][4];
  f32x4 O[7][4];
};

DI void loadw(const Ctx& c, u32x4 (&W)[4], int n) {
  const int X = n >> 2, j = n & 3;
#pragma unroll
  for (int s = 0; s < 4; ++s) W[s] = *(lds_u32x4_ptr)(c.wl + X * 16384 + s * 4096 + j * 1024);
}
template <int NP>
DI void relayout(const unsigned (&wb)[4], unsigned wl, int off) {
  u32x4 W[NP][4];
#pragma unroll
  for (int p = 0; p < NP; ++p)
#pragma unroll
    for (int j = 0; j < 4; ++j) {
      s16x4 lo = __builtin_amdgcn_ds_read_tr16_b64_v4i16((lds_s16x4_ptr)(wb[j] + off + p * 16384));
      s16x4 hi = __builtin_amdgcn_ds_read_tr16_b64_v4i16((lds_s16x4_ptr)(wb[j] + off + p * 16384 + 2048));
      W[p][j] = __builtin_bit_cast(u32x4, __builtin_shufflevector(lo, hi, 0, 1, 2, 3, 4, 5, 6, 7));
    }
  asm volatile("s_waitcnt lgkmcnt(0)" ::: "memory");
#pragma unroll
  for (int p = 0; p < NP; ++p)
#pragma unroll
    for (int j = 0; j < 4; ++j) *(lds_u32x4_ptr)(wl + off + p * 16384 + j * 1024) = W[p][j];
}
DI void signal(unsigned fa, unsigned one, int k) {
  asm volatile("ds_add_u32 %0, %1 offset:%2" : : "v"(fa), "v"(one), "n"(k * 4) : "memory");
}

template <int G>
DI void dma_e(const Ctx& c) {
  const char* p = c.ebase + (size_t)G * 8192;
  if (G < 3) {
    glds4(c.evoff[0], c.evoff[1], c.evoff[2], c.evoff[3], p, c.ebuf);
    glds4(c.evoff[0], c.evoff[1], c.evoff[2], c.evoff[3], p + 4096, c.ebuf + 4096);
  } else {
    glds2(c.evoff[0], c.evoff[1], p, c.ebuf);
  }
}

template <int NT>
DI void read_e(const Ctx& c, u32x4 (&E)[2][4]) {
#pragma unroll
  for (int s = 0; s < 4; ++s) {
    const unsigned a = c.eread ^ (s << 6);
    E[0][s] = *(lds_u32x4_ptr)(a);
    if (NT > 1) E[1][s] = *(lds_u32x4_ptr)(a + 4096);
  }
}

DI void stage_out(const Ctx& c, const f32x4 (&O)[4], unsigned slot) {
#pragma unroll
  for (int j = 0; j < 4; ++j) {
    u32x2 v;
    v[0] = relu2(O[j][0], O[j][1]);
    v[1] = relu2(O[j][2], O[j][3]);
    *(lds_u32x2_ptr)((c.owr ^ (j << 5)) + slot) = v;
  }
}

DI float xmax(float m) {
  auto a = __builtin_amdgcn_permlane32_swap(__float_as_uint(m), __float_as_uint(m), false, false);
  asm("v_max_f32 %0, %1, %2" : "=v"(m) : "v"(a[0]), "v"(a[1]));
  auto b = __builtin_amdgcn_permlane16_swap(__float_as_uint(m), __float_as_uint(m), false, false);
  asm("v_max_f32 %0, %1, %2" : "=v"(m) : "v"(b[0]), "v"(b[1]));
  return m;
}
DI float xsum(float m) {
  auto a = __builtin_amdgcn_permlane32_swap(__float_as_uint(m), __float_as_uint(m), false, false);
  m = __uint_as_float(a[0]) + __uint_as_float(a[1]);
  auto b = __builtin_amdgcn_permlane16_swap(__float_as_uint(m), __float_as_uint(m), false, false);
  return __uint_as_float(b[0]) + __uint_as_float(b[1]);
}
DI u32x4 sel4(bool p, const u32x4& a, const u32x4& b) {
  u32x4 r;
#pragma unroll
  for (int i = 0; i < 4; ++i) r[i] = p ? a[i] : b[i];
  return r;
}

#define PAT(NT_, ND_, NV)                          \
  _Pragma("unroll") for (int q_ = 0; q_ < 4; ++q_) { \
    SGB(0x8, 1);                                   \
    SGB(0x100, ND_);                               \
    SGB(0x2, NV);                                  \
    if ((NT_) > 1) { SGB(0x8, 1); SGB(0x2, NV); }  \
    if ((NT_) > 2) { SGB(0x8, 1); SGB(0x2, NV); }  \
  }

struct Loc {
  unsigned fl;
  f32x4 acc[2][2];
  f32x4 S[2][2];
  float sv[2][8], mx[2], sm[2];
  u32x4 Pp[2];
  u32x4 ob[4];
  u32x4 QA[2], KA[2];
  u32x2 VA[4];
};

template <int G, int n>
DI void job(const Ctx& c, St& st, Loc& L) {
  constexpr int NT = G < 3 ? 2 : 1;
  constexpr int T0 = 2 * G;
  constexpr int NM = 4 * NT;
  constexpr int NDS = 8 / NM;
  constexpr int VS = 2 / NT;
  constexpr int WB = (16 * G) % 3;
  constexpr int TA = G == 0 ? 0 : G == 1 ? 1 : G == 2 ? 3 : 4;
  constexpr int TB = G == 0 ? 1 : G == 1 ? 2 : G == 2 ? 4 : 5;
  constexpr int X = n >> 2, j = n & 3;
  const float cexp = 0.125f * 1.4426950408889634f;
  const float NINF = -__builtin_inff();
  const int g = c.g, cc = c.cc;
  if (G == 0 && n == 6) { poll4(L.fl, c.flag + 4); SBAR(); }
  if (G == 0 && n == 10) { poll4(L.fl, c.flag + 8); SBAR(); }
  if (G == 0 && n == 5) L.fl = *(lds_vu32_ptr)(c.flag + 4);
  if (G == 0 && n == 9) L.fl = *(lds_vu32_ptr)(c.flag + 8);
  if (n + 2 < 16) loadw(c, st.W[(WB + n + 2) % 3], n + 2);
  else if (G < 3) loadw(c, st.W[(WB + n + 2) % 3], n + 2 - 16);
  const u32x4(&W)[4] = st.W[(WB + n) % 3];
#pragma unroll
  for (int tt = 0; tt < NT; ++tt) L.acc[n & 1][tt] = f32x4{};
#pragma unroll
  for (int s = 0; s < 4; ++s)
#pragma unroll
    for (int tt = 0; tt < NT; ++tt)
      L.acc[n & 1][tt] = X == 2 ? MFMA(st.E[tt][s], W[s], L.acc[n & 1][tt]) : MFMA(W[s], st.E[tt][s], L.acc[n & 1][tt]);
  if (X == 3) {
#pragma unroll
    for (int tt = 0; tt < NT; ++tt) st.O[T0 + tt][j] = L.acc[n & 1][tt];
  }
  if (n > 0 && n <= 12) {
    constexpr int pX = (n - 1) >> 2, pj = (n - 1) & 3;
#pragma unroll
    for (int tt = 0; tt < NT; ++tt) {
      const f32x4 a = L.acc[(n - 1) & 1][tt];
      const unsigned lo = pack2(a[0], a[1]), hi = pack2(a[2], a[3]);
      if (pX == 0) { st.Qp[T0 + tt][pj >> 1][2 * (pj & 1)] = lo; st.Qp[T0 + tt][pj >> 1][2 * (pj & 1) + 1] = hi; }
      if (pX == 1) { st.Kp[T0 + tt][pj >> 1][2 * (pj & 1)] = lo; st.Kp[T0 + tt][pj >> 1][2 * (pj & 1) + 1] = hi; }
      if (pX == 2) { st.Vp[T0 + tt][pj][0] = lo; st.Vp[T0 + tt][pj][1] = hi; }
    }
  }
  constexpr int nv = (G > 0 && n == 0) || (G == 2 && n == 1) ? 3
                     : ((G & 1) && (n == 5 || n == 8)) ? 2
                     : (n >= 9) ? NVS : 1;
  if (G == 1 || G == 3) {
    constexpr int TO = G == 1 ? 0 : 3;
    if (n == 0) stage_out(c, st.O[TO], 0);
    if (n == 2) { L.ob[0] = *(lds_u32x4_ptr)(c.ord); L.ob[1] = *(lds_u32x4_ptr)(c.ord + 1024); }
    if (n == 4) {
      __builtin_amdgcn_raw_buffer_store_b128(L.ob[0], c.rsrc, c.gvoff, c.obase + TO * 4096, ST_AUX);
      __builtin_amdgcn_raw_buffer_store_b128(L.ob[1], c.rsrc, c.gvoff + 2048, c.obase + TO * 4096, ST_AUX);
    }
  }
  if (G == 2) {
    if (n == 0) stage_out(c, st.O[1], 0);
    if (n == 1) stage_out(c, st.O[2], 2048);
    if (n == 3) {
#pragma unroll
      for (int k = 0; k < 4; ++k) L.ob[k] = *(lds_u32x4_ptr)(c.ord + k * 1024);
    }
    if (n == 5) {
#pragma unroll
      for (int k = 0; k < 4; ++k)
        __builtin_amdgcn_raw_buffer_store_b128(L.ob[k], c.rsrc, c.gvoff + (k & 1) * 2048, c.obase + (1 + (k >> 1)) * 4096, ST_AUX);
    }
  }
  if (G == 1 || G == 3) {
    constexpr int TL = G == 1 ? 3 : 6;
    constexpr int CUT = G == 1 ? 10 : 14;
    if (n == 5) {
      L.QA[0] = sel4(cc >= CUT, st.Qp[TA][0], st.Qp[TL][0]);
      L.QA[1] = sel4(cc >= CUT, st.Qp[TA][1], st.Qp[TL][1]);
    }
    if (n == 8) {
      L.KA[0] = sel4(cc >= CUT, st.Kp[TA][0], st.Kp[TL][0]);
      L.KA[1] = sel4(cc >= CUT, st.Kp[TA][1], st.Kp[TL][1]);
    }
    if (n == 12) {
#pragma unroll
      for (int k = 0; k < 4; ++k) {
        L.VA[k][0] = (G == 1 && g == 3) ? st.Vp[TA][k][0] : st.Vp[TL][k][0];
        L.VA[k][1] = (G == 1 ? g >= 2 : g == 3) ? st.Vp[TA][k][1] : st.Vp[TL][k][1];
      }
    }
  } else {
    if (n == 5) { L.QA[0] = st.Qp[TA][0]; L.QA[1] = st.Qp[TA][1]; }
    if (n == 8) { L.KA[0] = st.Kp[TA][0]; L.KA[1] = st.Kp[TA][1]; }
    if (n == 12) {
#pragma unroll
      for (int k = 0; k < 4; ++k) L.VA[k] = st.Vp[TA][k];
    }
  }
  if (n == 9 || n == 10) {
    constexpr int qs = (n - 9) & 1;
#pragma unroll
    for (int ks = 0; ks < 2; ++ks)
#pragma unroll
      for (int i = 0; i < 2; ++i) {
        float lo = L.S[ks][qs][2 * i], hi = L.S[ks][qs][2 * i + 1];
        bool masked = false, ok = true;
        if (G == 0 && ks == 1) { masked = true; ok = i == 0 ? g < 3 : g < 2; }
        if (G == 1 && ks == 0) { masked = true; ok = i == 0 ? (g == 0 || g == 3) : g != 1; }
        if (G == 2 && ks == 0) { masked = true; ok = g >= 1; }
        if (G == 2 && ks == 1 && i == 1) { masked = true; ok = g < 3; }
        if (G == 3 && ks == 0) { masked = true; ok = i == 0 ? g < 2 : g != 2; }
        if (masked) { lo = ok ? lo : NINF; hi = ok ? hi : NINF; }
        L.sv[qs][4 * ks + 2 * i] = lo;
        L.sv[qs][4 * ks + 2 * i + 1] = hi;
      }
    float m = L.sv[qs][0];
#pragma unroll
    for (int i = 1; i < 8; ++i) m = fmaxf(m, L.sv[qs][i]);
    L.mx[qs] = xmax(m) * cexp;
  }
  if (n == 11 || n == 12) {
    constexpr int qs = (n - 11) & 1;
    float s = 0.f;
#pragma unroll
    for (int i = 0; i < 8; ++i) {
      L.sv[qs][i] = __builtin_amdgcn_exp2f(__builtin_fmaf(L.sv[qs][i], cexp, -L.mx[qs]));
      s += L.sv[qs][i];
    }
    L.sm[qs] = xsum(s);
  }
  if (n == 13 || n == 14) {
    constexpr int qs = (n - 13) & 1;
    float inv = __builtin_amdgcn_rcpf(L.sm[qs]);
    if (G == 0 && qs == 1) inv = cc < 10 ? inv : 0.f;
    if (G == 2 && qs == 0) inv = cc >= 4 ? inv : 0.f;
    if (G == 2 && qs == 1) inv = cc < 14 ? inv : 0.f;
#pragma unroll
    for (int i = 0; i < 4; ++i) L.Pp[qs][i] = pack2(L.sv[qs][2 * i] * inv, L.sv[qs][2 * i + 1] * inv);
  }
  if (n == 2 || n == 3) { PAT(NT, 2, nv * VS); }
  else { PAT(NT, 1, nv * VS); }
  if (n < 9 || n == 15 || (G == 0 && n == 9)) SBAR();
  if (G == 0 && n == DJ) { dma_e<1>(c); SBAR(); }
  if (n == 8) {
    L.S[0][0] = MFMA(L.KA[0], L.QA[0], f32x4{});
    L.S[1][0] = MFMA(st.Kp[TB][0], L.QA[0], f32x4{});
    L.S[0][1] = MFMA(L.KA[0], st.Qp[TB][0], f32x4{});
    L.S[1][1] = MFMA(st.Kp[TB][0], st.Qp[TB][0], f32x4{});
    L.S[0][0] = MFMA(L.KA[1], L.QA[1], L.S[0][0]);
    L.S[1][0] = MFMA(st.Kp[TB][1], L.QA[1], L.S[1][0]);
    L.S[0][1] = MFMA(L.KA[1], st.Qp[TB][1], L.S[0][1]);
    L.S[1][1] = MFMA(st.Kp[TB][1], st.Qp[TB][1], L.S[1][1]);
    SBAR();
  }
  if constexpr (n + 1 < 16) job<G, n + 1>(c, st, L);
}

template <int G>
DI void group_body(const Ctx& c, St& st) {
  constexpr int TA = G == 0 ? 0 : G == 1 ? 1 : G == 2 ? 3 : 4;
  constexpr int TB = G == 0 ? 1 : G == 1 ? 2 : G == 2 ? 4 : 5;
  const int cc = c.cc;
  Loc L;
  job<G, 0>(c, st, L);
  if (G < 3) {
    if (G == 0) { WAITV(0); } else if (G == 1) { WAITV(2); } else { WAITV(4); }
    SBAR();
    read_e<(G < 2 ? 2 : 1)>(c, st.E);
  }
  {
    u32x4 Vf[4];
#pragma unroll
    for (int j = 0; j < 4; ++j) {
      Vf[j][0] = L.VA[j][0]; Vf[j][1] = L.VA[j][1];
      Vf[j][2] = st.Vp[TB][j][0]; Vf[j][3] = st.Vp[TB][j][1];
    }
    const u32x4 z = {};
    if (G == 1 || G == 3) {
      constexpr int TL = G == 1 ? 3 : 6;
      constexpr int CUT = G == 1 ? 10 : 14;
      const u32x4 Ph = sel4(cc >= CUT, L.Pp[0], z);
      const u32x4 Pl = G == 1 ? sel4(cc < 4, L.Pp[0], z) : L.Pp[0];
#pragma unroll
      for (int j = 0; j < 4; ++j) st.O[TB][j] = MFMA(Vf[j], L.Pp[1], st.O[TB][j]);
#pragma unroll
      for (int j = 0; j < 4; ++j) st.O[TA][j] = MFMA(Vf[j], Ph, st.O[TA][j]);
#pragma unroll
      for (int j = 0; j < 4; ++j) st.O[TL][j] = MFMA(Vf[j], Pl, st.O[TL][j]);
    } else {
#pragma unroll
      for (int j = 0; j < 4; ++j) st.O[TA][j] = MFMA(Vf[j], L.Pp[0], st.O[TA][j]);
#pragma unroll
      for (int j = 0; j < 4; ++j) st.O[TB][j] = MFMA(Vf[j], L.Pp[1], st.O[TB][j]);
    }
  }
  SBAR();
  if (G < 2) {
    asm volatile("s_waitcnt lgkmcnt(0)" ::: "memory");
    dma_e<G + 2>(c);
    SBAR();
  }
}

__global__ __launch_bounds__(WAVES * 64, 2) void autoint_kernel(
    const __bf16* __restrict__ e_, const __bf16* __restrict__ wq_, const __bf16* __restrict__ wk_,
    const __bf16* __restrict__ wv_, const __bf16* __restrict__ wres_, __bf16* __restrict__ out_, unsigned long long* prof_) {
  extern __shared__ __attribute__((aligned(16))) char smem[];
  unsigned long long tA_, tB_;
  typedef __attribute__((ext_vector_type(4))) unsigned long long u64x4;
  typedef __attribute__((ext_vector_type(2))) unsigned long long u64x2;
  u64x4 k0_; u64x2 k1_; unsigned long long k2_;
  asm volatile("s_memrealtime %0\n\ts_waitcnt lgkmcnt(0)\n\t"
     "s_load_dwordx8 %2, %5, 0x0\n\ts_load_dwordx4 %3, %5, 0x20\n\ts_load_dwordx2 %4, %5, 0x30\n\ts_waitcnt lgkmcnt(0)\n\t"
     "s_memrealtime %1\n\ts_waitcnt lgkmcnt(0)"
     : "=&s"(tA_), "=&s"(tB_), "=&s"(k0_), "=&s"(k1_), "=&s"(k2_) : "s"(__builtin_amdgcn_kernarg_segment_ptr()) : "memory");
  const __bf16* __restrict__ e = (const __bf16*)k0_[0]; const __bf16* __restrict__ wq = (const __bf16*)k0_[1];
  const __bf16* __restrict__ wk = (const __bf16*)k0_[2]; const __bf16* __restrict__ wv = (const __bf16*)k0_[3];
  const __bf16* __restrict__ wres = (const __bf16*)k1_[0]; __bf16* __restrict__ out = (__bf16*)k1_[1];
  unsigned long long* prof = (unsigned long long*)k2_;
  const int tid = threadIdx.x;
  const int lane = tid & 63;
  const int wave = __builtin_amdgcn_readfirstlane(tid >> 6);
  const int cc = lane & 15, g = lane >> 4;
  const unsigned lds0 = __builtin_amdgcn_readfirstlane((unsigned)(size_t)(lds_void_ptr)smem);
  const int bid = blockIdx.x;
  const int hd = (bid >> 3) & 1;
  const int grp = (bid & 7) | ((bid >> 4) << 3);
  const int b0 = grp * BPG + wave * EPW;

  asm volatile("" : : "s"(e), "s"(wq), "s"(wk), "s"(wv), "s"(wres), "s"(out));
  Ctx c;
  c.lane = lane;
  c.cc = cc;
  c.g = g;
  c.prof = prof + (size_t)(blockIdx.x * WAVES + wave) * 4;
  KSTAMP;
  c.ebuf = lds0 + 65536 + wave * 12288;
  c.ebase = (const char*)e + (size_t)b0 * EBYTES;
  c.obase = b0 * EBYTES;
  {
    const int c0 = cc ^ g;
#pragma unroll
    for (int j = 0; j < 4; ++j) c.evoff[j] = (unsigned)((g * 256 + (c0 << 4)) ^ (j << 6));
  }
  const bool leader = wave < 4;
  c.leader = leader;
  if (wave == CODEPF) {
    unsigned long long pc;
    asm volatile("s_getpc_b64 %0" : "=s"(pc));
    const unsigned t0 = lane * 128, t1 = t0 + 8192, t2 = 16384 + (lane < CPFN ? lane : CPFN) * 128;
    asm volatile(
        "s_mov_b32 m0, %4\n\ts_nop 0\n\t"
        "global_load_lds_dword %0, %3\n\t"
        "global_load_lds_dword %1, %3\n\t"
        "global_load_lds_dword %2, %3"
        :
        : "v"(t0), "v"(t1), "v"(t2), "s"(pc), "s"(c.ebuf + 8192)
        : "memory", "m0");
  }
  c.flag = c.ebuf + 8192 + 512;
  {
    const u32x4 z = {};
    *(lds_u32x4_ptr)(c.flag) = z;
  }
  {
    unsigned o64[4], o128[4];
    const int r8 = lane >> 3;
    const int krow = 32 * (wave & 3) + 8 * (r8 >> 2) + (r8 & 3);
    const int ch = (lane & 7) ^ (r8 & 6);
#pragma unroll
    for (int i = 0; i < 4; ++i) {
      const int k = krow + 16 * (i & 1) + 4 * (i >> 1);
      o64[i] = (unsigned)(k * 128 + ch * 16 + 4096 - i * 1024);
      o128[i] = (unsigned)(k * 256 + ch * 16 + 4096 - i * 1024);
    }
    const char* sq = (const char*)wq + hd * 16384;
    const char* sk = (const char*)wk + hd * 16384;
    const char* sv = (const char*)wv + hd * 16384;
    const char* sr = (const char*)wres + hd * 128;
    const unsigned dst = lds0 + (wave & 3) * 4096;
    if (leader) {
      __builtin_amdgcn_s_setprio(LPRIO);
      glds4(o64[0], o64[1], o64[2], o64[3], sq - 4096, dst);
      asm volatile("s_waitcnt lgkmcnt(0)" ::: "memory");
      __builtin_amdgcn_s_barrier();
      glds4(o64[0], o64[1], o64[2], o64[3], sk - 4096, dst + 16384);
      dma_e<0>(c);
    } else {
      asm volatile("s_waitcnt lgkmcnt(0)" ::: "memory");
      __builtin_amdgcn_s_barrier();
      if (HORD == 2) dma_e<0>(c);
      glds4(o64[0], o64[1], o64[2], o64[3], sv - 4096, dst + 32768);
      if (HORD == 1) dma_e<0>(c);
      glds4(o128[0], o128[1], o128[2], o128[3], sr - 4096, dst + 49152);
      if (HORD == 0) dma_e<0>(c);
    }
  }
  SBAR();

  unsigned wb[4];
  {
    const int q4 = (lane & 15) >> 2, p4 = lane & 3;
    const int r8 = (g & 1) * 4 + q4;
#pragma unroll
    for (int j = 0; j < 4; ++j)
      wb[j] = lds0 + (wave & 3) * 4096 + (g >> 1) * 1024 + r8 * 128 + ((((2 * j) ^ (r8 & 6)) + (p4 >> 1)) << 4) + (p4 & 1) * 8;
    c.wl = lds0 + lane * 16;
    asm volatile("" : "+v"(c.wl));
    c.eread = c.ebuf + cc * 256 + ((g ^ cc) << 4);
    const unsigned obuf = c.ebuf + 8192;
    c.owr = obuf + cc * 128 + (((g >> 1) ^ (cc & 7)) << 4) + (g & 1) * 8;
    c.ord = obuf + lane * 16;
    const int orow = lane >> 3, oc = (lane & 7) ^ (orow & 7);
    c.gvoff = orow * 256 + hd * 128 + oc * 16;
    c.rsrc = __builtin_amdgcn_make_buffer_rsrc((void*)out, 0, NB_TOTAL * EBYTES, 0x00020000);
  }

  St st;
  STAMP(1);
  {
    const unsigned wlw = c.wl + (wave & 3) * 4096;
    const unsigned fa = lds0 + 65536 + 8192 + 512 + (lane & 7) * 12288, one = lane < 8 ? 1u : 0u;
    if (leader) {
      WAITV(8); relayout<2>(wb, wlw, 0); signal(fa, one, 0); SBAR();
      __builtin_amdgcn_s_setprio(0);
    } else {
      if (HORD == 0) { WAITV(12); } else if (HORD == 1) { WAITV(12); } else { WAITV(4); }
      relayout<1>(wb, wlw, 32768); signal(fa, one, 1); SBAR();
      if (HORD == 0) { WAITV(8); } else { WAITV(0); }
      relayout<1>(wb, wlw, 49152); signal(fa, one, 2); SBAR();
      __builtin_amdgcn_s_setprio(1);
    }
  }
  WAITV(0);
  SBAR();
  read_e<2>(c, st.E);
  poll4(*(lds_vu32_ptr)(c.flag), c.flag);
  SBAR();
  loadw(c, st.W[0], 0);
  loadw(c, st.W[1], 1);
  SBAR();
  SBAR();
  STAMP(3);

  group_body<0>(c, st);
  STAMP(4);
  group_body<1>(c, st);
  STAMP(5);
  group_body<2>(c, st);
  STAMP(6);
  __builtin_amdgcn_s_setprio(0);
  group_body<3>(c, st);
  {
    stage_out(c, st.O[4], 0);
    stage_out(c, st.O[5], 2048);
    stage_out(c, st.O[6], (unsigned)-8192);
    u32x4 ob[5];
#pragma unroll
    for (int k = 0; k < 4; ++k) ob[k] = *(lds_u32x4_ptr)(c.ord + k * 1024);
    ob[4] = *(lds_u32x4_ptr)(c.ord - 8192);
#pragma unroll
    for (int k = 0; k < 4; ++k)
      __builtin_amdgcn_raw_buffer_store_b128(ob[k], c.rsrc, c.gvoff + (k & 1) * 2048, c.obase + (4 + (k >> 1)) * 4096, ST_AUX);
    __builtin_amdgcn_raw_buffer_store_b128(ob[4], c.rsrc, c.gvoff, c.obase + 6 * 4096, ST_AUX);
  }
  asm volatile("s_waitcnt vmcnt(0)" ::: "memory");
  STAMP(8);
  if (c.lane == 0) {
#pragma unroll
    for (int k = 0; k < 4; ++k) c.prof[k] = c.ts[k];
  }
}

__global__ void pre_kernel(unsigned long long* prof) {
  unsigned long long Ta;
  asm volatile("s_memrealtime %0\n\ts_waitcnt lgkmcnt(0)" : "=s"(Ta) :: "memory");
  if (threadIdx.x == 0) prof[65536] = Ta;
}
__global__ void diag_kernel(const unsigned long long* prof) {
  unsigned long long Tb;
  asm volatile("s_memrealtime %0\n\ts_waitcnt lgkmcnt(0)" : "=s"(Tb) :: "memory");
  __shared__ unsigned long long s0[256], s1[256], s2[256], s3[256], s4[256];
  const int t = threadIdx.x;
  unsigned long long mn = ~0ull, mx = 0, a1 = 0, a2 = 0, a3 = 0;
  const unsigned long long Ta = prof[65536];
  for (int i = t; i < 2048; i += 256) {
    unsigned long long a = prof[(size_t)i * 4], b = prof[(size_t)i * 4 + 3];
    mn = a < mn ? a : mn; mx = b > mx ? b : mx;
    a1 += prof[(size_t)i * 4 + 1] - Ta; a2 += prof[(size_t)i * 4 + 2] - Ta; a3 += b - Ta;
  }
  s0[t] = mn; s1[t] = mx; s2[t] = a1; s3[t] = a2; s4[t] = a3;
  __syncthreads();
  for (int s = 128; s > 0; s >>= 1) {
    if (t < s) { s0[t] = s0[t] < s0[t + s] ? s0[t] : s0[t + s]; s1[t] = s1[t] > s1[t + s] ? s1[t] : s1[t + s]; s2[t] += s2[t + s]; s3[t] += s3[t + s]; s4[t] += s4[t + s]; }
    __syncthreads();
  }
  mn = s0[0]; mx = s1[0];
  double r[8];
  r[0] = 0; r[1] = (double)(mn - Ta); r[2] = (double)(Tb - mx); r[3] = (double)(mx - mn);
  r[4] = (double)s2[0] / 2048 - r[1]; r[5] = (double)s3[0] / 2048 - r[1]; r[6] = (double)s4[0] / 2048 - r[1]; r[7] = 0;
  double v = r[DIAGM];
  asm volatile("" :: "v"(r[1]), "v"(r[2]), "v"(r[3]), "v"(r[4]), "v"(r[5]), "v"(r[6]));
  if (v < 0) v = 0; if (v > 20000) v = 20000;
  unsigned long long dur = (unsigned long long)(v * DIAGS);
  if (t == 0) {
    unsigned long long q0, q1;
    asm volatile("s_memrealtime %0\n\ts_waitcnt lgkmcnt(0)" : "=s"(q0) :: "memory");
    for (int it = 0; it < 1000000; ++it) {
      asm volatile("s_memrealtime %0\n\ts_waitcnt lgkmcnt(0)" : "=s"(q1) :: "memory");
      if (q1 - q0 >= dur) break;
    }
  }
}

extern "C" void kernel_launch(void* const* d_in, const int* in_sizes, int n_in, void* d_out, int out_size,
                              void* d_ws, size_t ws_size, hipStream_t stream) {
  const __bf16* e = (const __bf16*)d_in[0];
  const __bf16* wq = (const __bf16*)d_in[1];
  const __bf16* wk = (const __bf16*)d_in[2];
  const __bf16* wv = (const __bf16*)d_in[3];
  const __bf16* wres = (const __bf16*)d_in[4];
  __bf16* out = (__bf16*)d_out;
  (void)hipFuncSetAttribute((const void*)autoint_kernel, hipFuncAttributeMaxDynamicSharedMemorySize, LDS_BYTES);
  pre_kernel<<<1, 64, 0, stream>>>((unsigned long long*)d_ws);
  autoint_kernel<<<2 * NB_TOTAL / BPG, WAVES * 64, LDS_BYTES, stream>>>(e, wq, wk, wv, wres, out, (unsigned long long*)d_ws);
  diag_kernel<<<1, 256, 0, stream>>>((const unsigned long long*)d_ws);
}
```

```cpp
#include <hip/hip_runtime.h>
#include <hip/hip_bf16.h>


__global__ __launch_bounds__(256, 1) __attribute__((aligned(4096))) void autoint_kernel(
    const __bf16* __restrict__ e, const __bf16* __restrict__ wq, const __bf16* __restrict__ wk,
    const __bf16* __restrict__ wv, const __bf16* __restrict__ wres, __bf16* __restrict__ out, unsigned long long* prof) {
  unsigned tid = threadIdx.x;
  asm volatile(
      "s_load_dwordx8 s[4:11], s[0:1], 0x0\n"
      "s_load_dwordx4 s[12:15], s[0:1], 0x20\n"
      "s_getpc_b64 s[24:25]\n"
      ".Lpc%=:\n"
      "v_readfirstlane_b32 s18, v0\n"
      "v_and_b32 v1, 63, v0\n"
      "v_lshlrev_b32 v2, 7, v0\n"
      "s_lshr_b32 s18, s18, 6\n"
      "v_min_u32 v2, .Lend%=-.Lpc%=-4, v2\n"
      "s_mul_i32 s19, s18, 40960\n"
      "s_add_u32 m0, s19, 32768\n"
      "v_lshrrev_b32 v3, 3, v1\n"
      "global_load_lds_dword v2, s[24:25]\n"
      "v_and_b32 v4, 3, v3\n"
      "v_lshrrev_b32 v5, 5, v1\n"
      "v_lshl_or_b32 v4, v5, 3, v4\n"
      "v_and_b32 v5, 6, v3\n"
      "v_xor_b32 v5, v5, v1\n"
      "v_and_b32 v5, 7, v5\n"
      "v_lshlrev_b32 v5, 4, v5\n"
      "v_lshl_or_b32 v6, v4, 7, v5\n"
      "v_add_u32 v7, 4096, v6\n"
      "v_add_u32 v8, 5120, v6\n"
      "v_add_u32 v9, 2560, v6\n"
      "v_add_u32 v2, 3584, v6\n"
      "v_lshl_or_b32 v6, v4, 8, v5\n"
      "v_add_u32 v10, 4096, v6\n"
      "v_add_u32 v11, 7168, v6\n"
      "v_add_u32 v12, 3072, v6\n"
      "v_add_u32 v13, 6144, v6\n"
      "v_lshrrev_b32 v14, 4, v1\n"
      "v_and_b32 v15, 15, v1\n"
      "v_xor_b32 v16, v15, v14\n"
      "v_lshlrev_b32 v16, 4, v16\n"
      "v_lshl_or_b32 v17, v14, 10, v16\n"
      "v_add_u32 v18, 2304, v17\n"
      "v_xor_b32 v19, 64, v17\n"
      "v_add_u32 v19, 1536, v19\n"
      "v_xor_b32 v20, 128, v17\n"
      "v_add_u32 v20, 768, v20\n"
      "v_xor_b32 v17, 192, v17\n"
      "s_lshr_b32 s21, s2, 4\n"
      "s_and_b32 s20, s2, 7\n"
      "s_lshl_b32 s21, s21, 3\n"
      "s_or_b32 s20, s20, s21\n"
      "s_lshl_b32 s20, s20, 2\n"
      "s_add_u32 s20, s20, s18\n"
      "s_mul_i32 s20, s20, 53248\n"
      "s_bfe_u32 s3, s2, 0x10003\n"
      "s_lshl_b32 s21, s3, 14\n"
      "s_lshl_b32 s26, s18, 12\n"
      "s_add_u32 s21, s21, s26\n"
      "s_sub_u32 s21, s21, 4096\n"
      "s_ashr_i32 s27, s21, 31\n"
      "s_lshl_b32 s26, s18, 13\n"
      "s_lshl_b32 s34, s3, 7\n"
      "s_add_u32 s26, s26, s34\n"
      "s_sub_u32 s26, s26, 4096\n"
      "s_ashr_i32 s33, s26, 31\n"
      "s_sub_u32 s35, s20, 2304\n"
      "s_ashr_i32 s24, s35, 31\n"
      "s_waitcnt lgkmcnt(0)\n"
      "s_add_u32 s6, s6, s21\n"
      "s_addc_u32 s7, s7, s27\n"
      "s_add_u32 s8, s8, s21\n"
      "s_addc_u32 s9, s9, s27\n"
      "s_add_u32 s22, s4, s35\n"
      "s_addc_u32 s23, s5, s24\n"
      "s_add_u32 s10, s10, s21\n"
      "s_addc_u32 s11, s11, s27\n"
      "s_add_u32 s12, s12, s26\n"
      "s_addc_u32 s13, s13, s33\n"
      "s_add_u32 m0, s19, 0\n"
      "s_nop 0\n"
      "global_load_lds_dwordx4 v7, s[6:7]\n"
      "global_load_lds_dwordx4 v8, s[6:7] offset:1024\n"
      "global_load_lds_dwordx4 v9, s[6:7] offset:2048\n"
      "global_load_lds_dwordx4 v2, s[6:7] offset:3072\n"
      "v_lshrrev_b32 v3, 2, v15\n"
      "v_and_b32 v4, 1, v14\n"
      "v_lshl_or_b32 v3, v4, 2, v3\n"
      "v_and_b32 v4, 6, v3\n"
      "v_bfe_u32 v5, v1, 1, 1\n"
      "v_or_b32 v4, v4, v5\n"
      "s_add_u32 m0, s19, 36864\n"
      "s_nop 0\n"
      "global_load_lds_dwordx4 v18, s[22:23] sc0 sc1 nt\n"
      "v_lshlrev_b32 v4, 4, v4\n"
      "v_lshl_or_b32 v4, v3, 7, v4\n"
      "v_lshrrev_b32 v5, 1, v14\n"
      "v_lshl_or_b32 v4, v5, 10, v4\n"
      "v_and_b32 v6, 1, v1\n"
      "v_lshl_or_b32 v21, v6, 3, v4\n"
      "global_load_lds_dwordx4 v19, s[22:23] offset:1024 sc0 sc1 nt\n"
      "v_xor_b32 v22, 32, v21\n"
      "v_xor_b32 v23, 64, v21\n"
      "v_xor_b32 v24, 96, v21\n"
      "v_add_u32 v25, 81920, v21\n"
      "v_add_u32 v26, 81920, v22\n"
      "v_add_u32 v27, 81920, v23\n"
      "global_load_lds_dwordx4 v20, s[22:23] offset:2048 sc0 sc1 nt\n"
      "v_add_u32 v28, 81920, v24\n"
      "v_add_u32 v5, s19, v21\n"
      "v_add_u32 v29, s19, v22\n"
      "v_add_u32 v30, s19, v23\n"
      "v_add_u32 v31, s19, v24\n"
      "v_and_b32 v4, 3, v15\n"
      "global_load_lds_dwordx4 v17, s[22:23] offset:3072 sc0 sc1 nt\n"
      "v_lshrrev_b32 v6, 2, v15\n"
      "v_lshlrev_b32 v6, 4, v6\n"
      "v_lshl_or_b32 v6, v14, 8, v6\n"
      "v_lshl_or_b32 v6, v4, 10, v6\n"
      "v_lshlrev_b32 v4, 5, v4\n"
      "v_xor_b32 v6, v6, v4\n"
      "s_add_u32 m0, s19, 4096\n"
      "s_nop 0\n"
      "global_load_lds_dwordx4 v7, s[8:9]\n"
      "v_add_u32 v32, s19, v6\n"
      "v_xor_b32 v33, 64, v32\n"
      "v_lshlrev_b32 v34, 4, v1\n"
      "v_xor_b32 v35, 32, v34\n"
      "v_xor_b32 v36, 64, v34\n"
      "v_xor_b32 v37, 96, v34\n"
      "global_load_lds_dwordx4 v8, s[8:9] offset:1024\n"
      "v_add_u32 v38, 81920, v34\n"
      "v_add_u32 v39, 81920, v35\n"
      "v_add_u32 v40, 81920, v36\n"
      "v_add_u32 v41, 81920, v37\n"
      "v_lshl_or_b32 v3, v15, 8, v16\n"
      "v_add_u32 v6, s19, v3\n"
      "global_load_lds_dwordx4 v9, s[8:9] offset:2048\n"
      "v_xor_b32 v1, 64, v6\n"
      "v_xor_b32 v42, 128, v6\n"
      "v_xor_b32 v43, 192, v6\n"
      "v_lshlrev_b32 v4, 3, v15\n"
      "v_lshl_or_b32 v4, v14, 8, v4\n"
      "v_add_u32 v0, s34, v4\n"
      "global_load_lds_dwordx4 v2, s[8:9] offset:3072\n"
      "v_mov_b32 v44, 0xff800000\n"
      "s_mov_b32 s28, s14\n"
      "s_mov_b32 s29, s15\n"
      "s_mov_b32 s30, 27262976\n"
      "s_mov_b32 s31, 0x20000\n"
      "s_mov_b32 s32, 0x3e38aa3b\n"
      "s_mov_b32 s36, 0xffffffff\n"
      "s_mov_b32 s37, 0x0\n"
      "s_mov_b32 s38, 0xffffffff\n"
      "s_mov_b32 s39, 0x33773377\n"
      "s_mov_b32 s40, 0xdd99dd99\n"
      "s_mov_b32 s41, 0xffffffff\n"
      "s_mov_b32 s42, 0xeeeeeeee\n"
      "s_mov_b32 s43, 0x77ff77ff\n"
      "s_mov_b32 s44, 0xbb33bb33\n"
      "s_mov_b32 s45, 0xffffffff\n"
      "s_mov_b32 s46, 0xccddccdd\n"
      "s_mov_b32 s47, 0xffffffff\n"
      "s_mov_b32 s48, 0xffeeffee\n"
      "s_mov_b32 s49, 0x77777777\n"
      "s_mov_b32 s50, 0x99bb99bb\n"
      "s_mov_b32 s51, 0xffffffff\n"
      "s_mov_b32 s52, 0xeecceecc\n"
      "s_mov_b32 s53, 0xffffffff\n"
      "s_mov_b32 vcc_lo, 0x33333333\n"
      "s_mov_b32 vcc_hi, 0x33333333\n"
      "v_mov_b32 v48, 0\n"
      "v_mov_b32 v49, 0\n"
      "v_mov_b32 v50, 0\n"
      "s_add_u32 s24, s22, 4096\n"
      "s_addc_u32 s25, s23, 0\n"
      "s_add_u32 m0, s19, 16384\n"
      "s_nop 0\n"
      "global_load_lds_dwordx4 v18, s[24:25] sc0 sc1 nt\n"
      "v_cndmask_b32_e64 v51, v44, 0, s[36:37]\n"
      "v_cndmask_b32_e64 v52, 0, v44, s[36:37]\n"
      "v_mov_b32 v53, 0\n"
      "v_mov_b32 v54, 0\n"
      "v_mov_b32 v55, 0\n"
      "v_cndmask_b32_e64 v56, v44, 0, s[36:37]\n"
      "global_load_lds_dwordx4 v19, s[24:25] offset:1024 sc0 sc1 nt\n"
      "v_cndmask_b32_e64 v57, 0, v44, s[36:37]\n"
      "v_mov_b32 v58, 0\n"
      "v_mov_b32 v59, 0\n"
      "global_load_lds_dwordx4 v20, s[24:25] offset:2048 sc0 sc1 nt\n"
      "global_load_lds_dwordx4 v17, s[24:25] offset:3072 sc0 sc1 nt\n"
      "s_waitcnt vmcnt(12)\n"
      "s_barrier\n"
      "ds_read_b64_tr_b16 a[0:1], v21 offset:0\n"
      "ds_read_b64_tr_b16 a[2:3], v21 offset:2048\n"
      "s_mov_b64 exec, 0\n"
      "s_cmp_eq_u32 s18, 1\n"
      "s_cbranch_scc1 .Lpad2%=\n"
      "s_cmp_eq_u32 s18, 2\n"
      "s_cbranch_scc1 .Lpad3%=\n"
      "s_cmp_eq_u32 s18, 3\n"
      "s_cbranch_scc1 .Lpad4%=\n"
      "s_branch .Lpad1%=\n"
      ".Lret0%=:\n"
      "s_mov_b64 exec, -1\n"
      "s_waitcnt vmcnt(8)\n"
      "ds_read_b128 v[60:63], v6 offset:36864\n"
      "ds_read_b128 v[64:67], v1 offset:36864\n"
      "ds_read_b64_tr_b16 a[4:5], v21 offset:40960\n"
      "ds_read_b64_tr_b16 a[6:7], v21 offset:43008\n"
      "ds_read_b128 v[44:47], v42 offset:36864\n"
      "ds_read_b64_tr_b16 a[8:9], v25 offset:0\n"
      "ds_read_b64_tr_b16 a[10:11], v25 offset:2048\n"
      "ds_read_b128 v[68:71], v43 offset:36864\n"
      "ds_read_b64_tr_b16 a[12:13], v25 offset:40960\n"
      "ds_read_b64_tr_b16 a[14:15], v25 offset:43008\n"
      "ds_read_b64_tr_b16 a[16:17], v22 offset:0\n"
      "ds_read_b64_tr_b16 a[18:19], v22 offset:2048\n"
      "ds_read_b64_tr_b16 a[20:21], v22 offset:40960\n"
      "ds_read_b64_tr_b16 a[22:23], v22 offset:43008\n"
      "ds_read_b64_tr_b16 a[24:25], v26 offset:0\n"
      "ds_read_b64_tr_b16 a[26:27], v26 offset:2048\n"
      "ds_read_b64_tr_b16 a[28:29], v26 offset:40960\n"
      "ds_read_b64_tr_b16 a[30:31], v26 offset:43008\n"
      "s_waitcnt lgkmcnt(14)\n"
      "v_mfma_f32_16x16x32_bf16 v[72:75], a[0:3], v[60:63], 0\n"
      "s_add_u32 m0, s19, 8192\n"
      "s_add_u32 s24, s22, 8192\n"
      "s_addc_u32 s25, s23, 0\n"
      "ds_read_b64_tr_b16 a[32:33], v23 offset:0\n"
      "v_mfma_f32_16x16x32_bf16 v[72:75], a[4:7], v[64:67], v[72:75]\n"
      "global_load_lds_dwordx4 v7, s[10:11]\n"
      "s_waitcnt lgkmcnt(12)\n"
      "ds_read_b64_tr_b16 a[34:35], v23 offset:2048\n"
      "v_mfma_f32_16x16x32_bf16 v[72:75], a[8:11], v[44:47], v[72:75]\n"
      "s_waitcnt lgkmcnt(10)\n"
      "ds_read_b64_tr_b16 a[36:37], v23 offset:40960\n"
      "ds_read_b64_tr_b16 a[38:39], v23 offset:43008\n"
      "v_mfma_f32_16x16x32_bf16 v[72:75], a[12:15], v[68:71], v[72:75]\n"
      "s_waitcnt lgkmcnt(10)\n"
      "ds_read_b64_tr_b16 a[40:41], v27 offset:0\n"
      "ds_read_b64_tr_b16 a[42:43], v27 offset:2048\n"
      "v_mfma_f32_16x16x32_bf16 v[76:79], a[16:19], v[60:63], 0\n"
      "s_waitcnt lgkmcnt(10)\n"
      "ds_read_b64_tr_b16 a[44:45], v27 offset:40960\n"
      "ds_read_b64_tr_b16 a[46:47], v27 offset:43008\n"
      "v_mfma_f32_16x16x32_bf16 v[76:79], a[20:23], v[64:67], v[76:79]\n"
      "global_load_lds_dwordx4 v8, s[10:11] offset:1024\n"
      "s_waitcnt lgkmcnt(8)\n"
      "ds_read_b64_tr_b16 a[48:49], v24 offset:0\n"
      "v_mfma_f32_16x16x32_bf16 v[76:79], a[24:27], v[44:47], v[76:79]\n"
      "ds_read_b64_tr_b16 a[50:51], v24 offset:2048\n"
      "ds_read_b64_tr_b16 a[52:53], v24 offset:40960\n"
      "v_mfma_f32_16x16x32_bf16 v[76:79], a[28:31], v[68:71], v[76:79]\n"
      "s_waitcnt lgkmcnt(9)\n"
      "ds_read_b64_tr_b16 a[54:55], v24 offset:43008\n"
      "ds_read_b64_tr_b16 a[56:57], v28 offset:0\n"
      "v_mfma_f32_16x16x32_bf16 v[80:83], a[32:35], v[60:63], 0\n"
      "s_waitcnt lgkmcnt(9)\n"
      "s_waitcnt vmcnt(6)\n"
      "s_barrier\n"
      "ds_read_b64_tr_b16 a[58:59], v28 offset:2048\n"
      "ds_read_b64_tr_b16 a[60:61], v28 offset:40960\n"
      "v_mfma_f32_16x16x32_bf16 v[80:83], a[36:39], v[64:67], v[80:83]\n"
      "global_load_lds_dwordx4 v9, s[10:11] offset:2048\n"
      "s_waitcnt lgkmcnt(9)\n"
      "ds_read_b64_tr_b16 a[62:63], v28 offset:43008\n"
      "v_mfma_f32_16x16x32_bf16 v[80:83], a[40:43], v[44:47], v[80:83]\n"
      "s_waitcnt lgkmcnt(8)\n"
      "ds_read_b64_tr_b16 a[64:65], v21 offset:4096\n"
      "ds_read_b64_tr_b16 a[66:67], v21 offset:6144\n"
      "v_mfma_f32_16x16x32_bf16 v[80:83], a[44:47], v[68:71], v[80:83]\n"
      "s_waitcnt lgkmcnt(8)\n"
      "ds_read_b64_tr_b16 a[68:69], v21 offset:45056\n"
      "ds_read_b64_tr_b16 a[70:71], v21 offset:47104\n"
      "v_cvt_pk_bf16_f32 v84, v72, v73\n"
      "v_cvt_pk_bf16_f32 v85, v74, v75\n"
      "v_mfma_f32_16x16x32_bf16 v[88:91], a[48:51], v[60:63], 0\n"
      "s_waitcnt lgkmcnt(8)\n"
      "ds_read_b64_tr_b16 a[72:73], v25 offset:4096\n"
      "ds_read_b64_tr_b16 a[74:75], v25 offset:6144\n"
      "v_mfma_f32_16x16x32_bf16 v[88:91], a[52:55], v[64:67], v[88:91]\n"
      "global_load_lds_dwordx4 v2, s[10:11] offset:3072\n"
      "s_waitcnt lgkmcnt(8)\n"
      "ds_read_b64_tr_b16 a[76:77], v25 offset:45056\n"
      "ds_read_b64_tr_b16 a[78:79], v25 offset:47104\n"
      "v_mfma_f32_16x16x32_bf16 v[88:91], a[56:59], v[44:47], v[88:91]\n"
      "s_waitcnt lgkmcnt(8)\n"
      "s_add_u32 m0, s19, 12288\n"
      "ds_read_b64_tr_b16 a[80:81], v22 offset:4096\n"
      "ds_read_b64_tr_b16 a[82:83], v22 offset:6144\n"
      "v_mfma_f32_16x16x32_bf16 v[88:91], a[60:63], v[68:71], v[88:91]\n"
      "s_waitcnt lgkmcnt(8)\n"
      "ds_read_b64_tr_b16 a[84:85], v22 offset:45056\n"
      "ds_read_b64_tr_b16 a[86:87], v22 offset:47104\n"
      "v_cvt_pk_bf16_f32 v86, v76, v77\n"
      "v_cvt_pk_bf16_f32 v87, v78, v79\n"
      "v_mfma_f32_16x16x32_bf16 v[72:75], a[64:67], v[60:63], 0\n"
      "s_waitcnt lgkmcnt(8)\n"
      "ds_read_b64_tr_b16 a[88:89], v26 offset:4096\n"
      "ds_read_b64_tr_b16 a[90:91], v26 offset:6144\n"
      "v_mfma_f32_16x16x32_bf16 v[72:75], a[68:71], v[64:67], v[72:75]\n"
      "global_load_lds_dwordx4 v10, s[12:13]\n"
      "s_waitcnt lgkmcnt(8)\n"
      "ds_read_b64_tr_b16 a[92:93], v26 offset:45056\n"
      "ds_read_b64_tr_b16 a[94:95], v26 offset:47104\n"
      "v_mfma_f32_16x16x32_bf16 v[72:75], a[72:75], v[44:47], v[72:75]\n"
      "s_waitcnt lgkmcnt(8)\n"
      "ds_read_b64_tr_b16 a[96:97], v23 offset:4096\n"
      "ds_read_b64_tr_b16 a[98:99], v23 offset:6144\n"
      "v_mfma_f32_16x16x32_bf16 v[72:75], a[76:79], v[68:71], v[72:75]\n"
      "s_waitcnt lgkmcnt(8)\n"
      "ds_read_b64_tr_b16 a[100:101], v23 offset:45056\n"
      "ds_read_b64_tr_b16 a[102:103], v23 offset:47104\n"
      "v_cvt_pk_bf16_f32 v76, v80, v81\n"
      "v_cvt_pk_bf16_f32 v77, v82, v83\n"
      "v_mfma_f32_16x16x32_bf16 v[92:95], a[80:83], v[60:63], 0\n"
      "s_waitcnt lgkmcnt(8)\n"
      "ds_read_b64_tr_b16 a[104:105], v27 offset:4096\n"
      "ds_read_b64_tr_b16 a[106:107], v27 offset:6144\n"
      "v_mfma_f32_16x16x32_bf16 v[92:95], a[84:87], v[64:67], v[92:95]\n"
      "global_load_lds_dwordx4 v11, s[12:13] offset:1024\n"
      "s_waitcnt lgkmcnt(8)\n"
      "ds_read_b64_tr_b16 a[108:109], v27 offset:45056\n"
      "ds_read_b64_tr_b16 a[110:111], v27 offset:47104\n"
      "v_mfma_f32_16x16x32_bf16 v[92:95], a[88:91], v[44:47], v[92:95]\n"
      "s_waitcnt lgkmcnt(8)\n"
      "ds_read_b64_tr_b16 a[112:113], v24 offset:4096\n"
      "ds_read_b64_tr_b16 a[114:115], v24 offset:6144\n"
      "v_mfma_f32_16x16x32_bf16 v[92:95], a[92:95], v[68:71], v[92:95]\n"
      "s_waitcnt lgkmcnt(8)\n"
      "ds_read_b64_tr_b16 a[116:117], v24 offset:45056\n"
      "ds_read_b64_tr_b16 a[118:119], v24 offset:47104\n"
      "v_cvt_pk_bf16_f32 v78, v88, v89\n"
      "v_cvt_pk_bf16_f32 v79, v90, v91\n"
      "v_mfma_f32_16x16x32_bf16 v[8:11], a[96:99], v[60:63], 0\n"
      "s_waitcnt lgkmcnt(8)\n"
      "ds_read_b64_tr_b16 a[120:121], v28 offset:4096\n"
      "ds_read_b64_tr_b16 a[122:123], v28 offset:6144\n"
      "v_mfma_f32_16x16x32_bf16 v[8:11], a[100:103], v[64:67], v[8:11]\n"
      "global_load_lds_dwordx4 v12, s[12:13] offset:2048\n"
      "s_waitcnt lgkmcnt(8)\n"
      "ds_read_b64_tr_b16 a[124:125], v28 offset:45056\n"
      "ds_read_b64_tr_b16 a[126:127], v28 offset:47104\n"
      "v_mfma_f32_16x16x32_bf16 v[8:11], a[104:107], v[44:47], v[8:11]\n"
      "s_waitcnt vmcnt(7) lgkmcnt(8)\n"
      "ds_read_b128 v[24:27], v6 offset:16384\n"
      "ds_read_b128 v[80:83], v1 offset:16384\n"
      "v_mfma_f32_16x16x32_bf16 v[8:11], a[108:111], v[68:71], v[8:11]\n"
      "s_waitcnt lgkmcnt(8)\n"
      "ds_read_b128 v[88:91], v42 offset:16384\n"
      "v_cvt_pk_bf16_f32 v96, v72, v73\n"
      "v_cvt_pk_bf16_f32 v97, v74, v75\n"
      "v_mfma_f32_16x16x32_bf16 v[100:103], a[112:115], v[60:63], 0\n"
      "s_waitcnt lgkmcnt(7)\n"
      "ds_read_b128 v[104:107], v43 offset:16384\n"
      "v_cvt_pk_bf16_f32 v98, v92, v93\n"
      "v_cvt_pk_bf16_f32 v99, v94, v95\n"
      "v_mfma_f32_16x16x32_bf16 v[100:103], a[116:119], v[64:67], v[100:103]\n"
      "global_load_lds_dwordx4 v13, s[12:13] offset:3072\n"
      "s_waitcnt lgkmcnt(6)\n"
      "v_mfma_f32_16x16x32_bf16 v[100:103], a[120:123], v[44:47], v[100:103]\n"
      "s_waitcnt lgkmcnt(4)\n"
      "s_add_u32 m0, s19, 20480\n"
      "v_cvt_pk_bf16_f32 v72, v8, v9\n"
      "v_cvt_pk_bf16_f32 v73, v10, v11\n"
      "v_mfma_f32_16x16x32_bf16 v[100:103], a[124:127], v[68:71], v[100:103]\n"
      "s_waitcnt lgkmcnt(3)\n"
      "v_mfma_f32_16x16x32_bf16 v[108:111], a[0:3], v[24:27], 0\n"
      "s_waitcnt lgkmcnt(2)\n"
      "v_mfma_f32_16x16x32_bf16 v[108:111], a[4:7], v[80:83], v[108:111]\n"
      "s_waitcnt lgkmcnt(1)\n"
      "v_cvt_pk_bf16_f32 v74, v100, v101\n"
      "v_cvt_pk_bf16_f32 v75, v102, v103\n"
      "v_mfma_f32_16x16x32_bf16 v[108:111], a[8:11], v[88:91], v[108:111]\n"
      "s_waitcnt lgkmcnt(0)\n"
      "v_mfma_f32_16x16x32_bf16 v[108:111], a[12:15], v[104:107], v[108:111]\n"
      "v_mfma_f32_16x16x32_bf16 v[8:11], a[16:19], v[24:27], 0\n"
      "v_mfma_f32_16x16x32_bf16 v[8:11], a[20:23], v[80:83], v[8:11]\n"
      "v_cvt_pk_bf16_f32 v12, v108, v109\n"
      "v_cvt_pk_bf16_f32 v13, v110, v111\n"
      "v_mfma_f32_16x16x32_bf16 v[8:11], a[24:27], v[88:91], v[8:11]\n"
      "v_mfma_f32_16x16x32_bf16 v[8:11], a[28:31], v[104:107], v[8:11]\n"
      "v_mfma_f32_16x16x32_bf16 v[92:95], a[32:35], v[24:27], 0\n"
      "v_mfma_f32_16x16x32_bf16 v[92:95], a[36:39], v[80:83], v[92:95]\n"
      "v_cvt_pk_bf16_f32 v14, v8, v9\n"
      "v_cvt_pk_bf16_f32 v15, v10, v11\n"
      "v_mfma_f32_16x16x32_bf16 v[92:95], a[40:43], v[88:91], v[92:95]\n"
      "v_mfma_f32_16x16x32_bf16 v[92:95], a[44:47], v[104:107], v[92:95]\n"
      "v_mfma_f32_16x16x32_bf16 v[100:103], a[48:51], v[24:27], 0\n"
      "v_mfma_f32_16x16x32_bf16 v[100:103], a[52:55], v[80:83], v[100:103]\n"
      "v_cvt_pk_bf16_f32 v112, v92, v93\n"
      "v_cvt_pk_bf16_f32 v113, v94, v95\n"
      "v_mfma_f32_16x16x32_bf16 v[100:103], a[56:59], v[88:91], v[100:103]\n"
      "s_waitcnt vmcnt(4)\n"
      "ds_read_b64_tr_b16 v[108:109], v5 offset:8192\n"
      "ds_read_b64_tr_b16 v[110:111], v5 offset:10240\n"
      "v_mfma_f32_16x16x32_bf16 v[100:103], a[60:63], v[104:107], v[100:103]\n"
      "ds_read_b64_tr_b16 v[8:9], v29 offset:8192\n"
      "ds_read_b64_tr_b16 v[10:11], v29 offset:10240\n"
      "v_mfma_f32_16x16x32_bf16 v[116:119], a[64:67], v[24:27], 0\n"
      "ds_read_b64_tr_b16 v[120:121], v30 offset:8192\n"
      "ds_read_b64_tr_b16 v[122:123], v30 offset:10240\n"
      "v_mfma_f32_16x16x32_bf16 v[116:119], a[68:71], v[80:83], v[116:119]\n"
      "ds_read_b64_tr_b16 v[92:93], v31 offset:8192\n"
      "ds_read_b64_tr_b16 v[94:95], v31 offset:10240\n"
      "v_mfma_f32_16x16x32_bf16 v[116:119], a[72:75], v[88:91], v[116:119]\n"
      "v_cvt_pk_bf16_f32 v114, v100, v101\n"
      "v_cvt_pk_bf16_f32 v115, v102, v103\n"
      "v_mfma_f32_16x16x32_bf16 v[116:119], a[76:79], v[104:107], v[116:119]\n"
      "v_mfma_f32_16x16x32_bf16 v[124:127], a[80:83], v[24:27], 0\n"
      "s_waitcnt lgkmcnt(6)\n"
      "ds_write_b128 v32, v[108:111] offset:8192\n"
      "v_mfma_f32_16x16x32_bf16 v[124:127], a[84:87], v[80:83], v[124:127]\n"
      "v_cvt_pk_bf16_f32 v128, v116, v117\n"
      "v_cvt_pk_bf16_f32 v129, v118, v119\n"
      "v_mfma_f32_16x16x32_bf16 v[124:127], a[88:91], v[88:91], v[124:127]\n"
      "v_mfma_f32_16x16x32_bf16 v[124:127], a[92:95], v[104:107], v[124:127]\n"
      "s_waitcnt lgkmcnt(1)\n"
      "ds_write_b128 v33, v[8:11] offset:8192\n"
      "v_mfma_f32_16x16x32_bf16 v[100:103], a[96:99], v[24:27], 0\n"
      "v_mfma_f32_16x16x32_bf16 v[100:103], a[100:103], v[80:83], v[100:103]\n"
      "v_cvt_pk_bf16_f32 v130, v124, v125\n"
      "v_cvt_pk_bf16_f32 v131, v126, v127\n"
      "v_mfma_f32_16x16x32_bf16 v[100:103], a[104:107], v[88:91], v[100:103]\n"
      "ds_write_b128 v32, v[120:123] offset:8320\n"
      "v_mfma_f32_16x16x32_bf16 v[100:103], a[108:111], v[104:107], v[100:103]\n"
      "v_mfma_f32_16x16x32_bf16 v[108:111], a[112:115], v[24:27], 0\n"
      "v_mfma_f32_16x16x32_bf16 v[108:111], a[116:119], v[80:83], v[108:111]\n"
      "ds_write_b128 v33, v[92:95] offset:8320\n"
      "v_mfma_f32_16x16x32_bf16 v[108:111], a[120:123], v[88:91], v[108:111]\n"
      "v_cvt_pk_bf16_f32 v8, v100, v101\n"
      "v_cvt_pk_bf16_f32 v9, v102, v103\n"
      "v_mfma_f32_16x16x32_bf16 v[108:111], a[124:127], v[104:107], v[108:111]\n"
      "v_mfma_f32_16x16x32_bf16 v[116:119], v[96:99], v[84:87], 0\n"
      "v_mfma_f32_16x16x32_bf16 v[116:119], v[72:75], v[76:79], v[116:119]\n"
      "s_waitcnt lgkmcnt(0)\n"
      "s_barrier\n"
      "ds_read_b128 a[128:131], v34 offset:8192\n"
      "v_cvt_pk_bf16_f32 v10, v108, v109\n"
      "v_cvt_pk_bf16_f32 v11, v110, v111\n"
      "v_mfma_f32_16x16x32_bf16 v[92:95], v[128:131], v[84:87], v[54:57]\n"
      "ds_read_b128 a[132:135], v34 offset:49152\n"
      "ds_read_b128 a[136:139], v38 offset:8192\n"
      "v_mfma_f32_16x16x32_bf16 v[92:95], v[8:11], v[76:79], v[92:95]\n"
      "ds_read_b128 a[140:143], v38 offset:49152\n"
      "ds_read_b128 a[144:147], v35 offset:9216\n"
      "v_mfma_f32_16x16x32_bf16 v[100:103], v[96:99], v[12:15], 0\n"
      "ds_read_b128 a[148:151], v35 offset:50176\n"
      "ds_read_b128 a[152:155], v39 offset:9216\n"
      "v_mfma_f32_16x16x32_bf16 v[100:103], v[72:75], v[112:115], v[100:103]\n"
      "ds_read_b128 a[156:159], v39 offset:50176\n"
      "ds_read_b128 a[160:163], v36 offset:10240\n"
      "v_mfma_f32_16x16x32_bf16 v[84:87], v[128:131], v[12:15], v[54:57]\n"
      "ds_read_b128 a[164:167], v36 offset:51200\n"
      "ds_read_b128 a[168:171], v40 offset:10240\n"
      "v_mfma_f32_16x16x32_bf16 v[84:87], v[8:11], v[112:115], v[84:87]\n"
      "s_waitcnt lgkmcnt(10)\n"
      "ds_read_b128 a[172:175], v40 offset:51200\n"
      "ds_read_b128 a[176:179], v37 offset:11264\n"
      "v_mfma_f32_16x16x32_bf16 v[76:79], v[60:63], a[128:131], 0\n"
      "s_waitcnt vmcnt(0) lgkmcnt(10)\n"
      "ds_read_b64_tr_b16 v[72:73], v5 offset:12288\n"
      "ds_read_b64_tr_b16 v[74:75], v5 offset:14336\n"
      "v_mfma_f32_16x16x32_bf16 v[76:79], v[64:67], a[132:135], v[76:79]\n"
      "ds_read_b64_tr_b16 v[96:97], v29 offset:12288\n"
      "ds_read_b64_tr_b16 v[98:99], v29 offset:14336\n"
      "v_mfma_f32_16x16x32_bf16 v[76:79], v[44:47], a[136:139], v[76:79]\n"
      "s_waitcnt lgkmcnt(10)\n"
      "ds_read_b64_tr_b16 v[108:109], v30 offset:12288\n"
      "ds_read_b64_tr_b16 v[110:111], v30 offset:14336\n"
      "v_mfma_f32_16x16x32_bf16 v[76:79], v[68:71], a[140:143], v[76:79]\n"
      "ds_read_b64_tr_b16 v[120:121], v31 offset:12288\n"
      "ds_read_b64_tr_b16 v[122:123], v31 offset:14336\n"
      "v_mfma_f32_16x16x32_bf16 v[124:127], v[60:63], a[144:147], 0\n"
      "global_load_lds_dwordx4 v18, s[24:25] sc0 sc1 nt\n"
      "ds_read_b128 a[180:183], v37 offset:52224\n"
      "v_mfma_f32_16x16x32_bf16 v[124:127], v[64:67], a[148:151], v[124:127]\n"
      "global_load_lds_dwordx4 v19, s[24:25] offset:1024 sc0 sc1 nt\n"
      "ds_read_b128 a[184:187], v41 offset:11264\n"
      "v_mfma_f32_16x16x32_bf16 v[124:127], v[44:47], a[152:155], v[124:127]\n"
      "s_waitcnt lgkmcnt(8)\n"
      "ds_write_b128 v32, v[72:75] offset:12288\n"
      "v_mfma_f32_16x16x32_bf16 v[124:127], v[68:71], a[156:159], v[124:127]\n"
      "global_load_lds_dwordx4 v20, s[24:25] offset:2048 sc0 sc1 nt\n"
      "ds_read_b128 a[188:191], v41 offset:52224\n"
      "v_mfma_f32_16x16x32_bf16 v[28:31], v[60:63], a[160:163], 0\n"
      "global_load_lds_dwordx4 v17, s[24:25] offset:3072 sc0 sc1 nt\n"
      "s_add_u32 m0, s19, 24576\n"
      "v_mfma_f32_16x16x32_bf16 v[28:31], v[64:67], a[164:167], v[28:31]\n"
      "s_waitcnt lgkmcnt(4)\n"
      "ds_write_b128 v33, v[96:99] offset:12288\n"
      "v_mfma_f32_16x16x32_bf16 v[28:31], v[44:47], a[168:171], v[28:31]\n"
      "s_add_u32 s24, s22, 12288\n"
      "s_addc_u32 s25, s23, 0\n"
      "v_cvt_pk_bf16_f32 v250, v76, v77\n"
      "v_cvt_pk_bf16_f32 v251, v78, v79\n"
      "v_mfma_f32_16x16x32_bf16 v[28:31], v[68:71], a[172:175], v[28:31]\n"
      "v_cvt_pk_bf16_f32 v244, v124, v125\n"
      "v_cvt_pk_bf16_f32 v245, v126, v127\n"
      "v_max3_f32 v2, v116, v117, v118\n"
      "v_mfma_f32_16x16x32_bf16 v[72:75], v[60:63], a[176:179], 0\n"
      "s_waitcnt lgkmcnt(2)\n"
      "ds_write_b128 v32, v[108:111] offset:12416\n"
      "v_mfma_f32_16x16x32_bf16 v[72:75], v[64:67], a[180:183], v[72:75]\n"
      "v_cvt_pk_bf16_f32 v238, v28, v29\n"
      "v_cvt_pk_bf16_f32 v239, v30, v31\n"
      "v_max3_f32 v3, v100, v101, v102\n"
      "v_mfma_f32_16x16x32_bf16 v[72:75], v[44:47], a[184:187], v[72:75]\n"
      "s_waitcnt lgkmcnt(2)\n"
      "v_max3_f32 v2, v2, v119, v92\n"
      "v_max3_f32 v3, v3, v103, v84\n"
      "v_mfma_f32_16x16x32_bf16 v[72:75], v[68:71], a[188:191], v[72:75]\n"
      "ds_write_b128 v33, v[120:123] offset:12416\n"
      "v_mfma_f32_16x16x32_bf16 v[76:79], v[24:27], a[128:131], 0\n"
      "v_max3_f32 v2, v2, v93, v94\n"
      "v_max3_f32 v3, v3, v85, v86\n"
      "v_mfma_f32_16x16x32_bf16 v[76:79], v[80:83], a[132:135], v[76:79]\n"
      "v_cvt_pk_bf16_f32 v232, v72, v73\n"
      "v_cvt_pk_bf16_f32 v233, v74, v75\n"
      "v_mfma_f32_16x16x32_bf16 v[76:79], v[88:91], a[136:139], v[76:79]\n"
      "v_permlane32_swap_b32 v2, v3\n"
      "v_mfma_f32_16x16x32_bf16 v[76:79], v[104:107], a[140:143], v[76:79]\n"
      "s_waitcnt lgkmcnt(0)\n"
      "s_barrier\n"
      "ds_read_b128 a[192:195], v34 offset:12288\n"
      "v_max_f32 v4, v2, v3\n"
      "v_mfma_f32_16x16x32_bf16 v[28:31], v[24:27], a[144:147], 0\n"
      "ds_read_b128 a[196:199], v34 offset:53248\n"
      "ds_read_b128 a[200:203], v38 offset:12288\n"
      "v_mfma_f32_16x16x32_bf16 v[28:31], v[80:83], a[148:151], v[28:31]\n"
      "ds_read_b128 a[204:207], v38 offset:53248\n"
      "v_cvt_pk_bf16_f32 v252, v76, v77\n"
      "v_cvt_pk_bf16_f32 v253, v78, v79\n"
      "v_mfma_f32_16x16x32_bf16 v[28:31], v[88:91], a[152:155], v[28:31]\n"
      "ds_read_b128 a[208:211], v35 offset:13312\n"
      "v_max_f32 v5, v2, v3\n"
      "v_mfma_f32_16x16x32_bf16 v[28:31], v[104:107], a[156:159], v[28:31]\n"
      "ds_read_b128 a[212:215], v35 offset:54272\n"
      "v_permlane16_swap_b32 v4, v5\n"
      "v_mfma_f32_16x16x32_bf16 v[72:75], v[24:27], a[160:163], 0\n"
      "ds_read_b128 a[216:219], v39 offset:13312\n"
      "v_max_f32 v4, v4, v5\n"
      "v_mfma_f32_16x16x32_bf16 v[72:75], v[80:83], a[164:167], v[72:75]\n"
      "ds_read_b128 a[220:223], v39 offset:54272\n"
      "v_cvt_pk_bf16_f32 v246, v28, v29\n"
      "v_cvt_pk_bf16_f32 v247, v30, v31\n"
      "v_mfma_f32_16x16x32_bf16 v[72:75], v[88:91], a[168:171], v[72:75]\n"
      "ds_read_b128 a[224:227], v36 offset:14336\n"
      "v_mul_f32 v2, s32, v4\n"
      "v_mul_f32 v3, s32, v4\n"
      "v_mfma_f32_16x16x32_bf16 v[72:75], v[104:107], a[172:175], v[72:75]\n"
      "ds_read_b128 a[228:231], v36 offset:55296\n"
      "v_permlane32_swap_b32 v2, v3\n"
      "v_mfma_f32_16x16x32_bf16 v[32:35], v[24:27], a[176:179], 0\n"
      "ds_read_b128 a[232:235], v40 offset:14336\n"
      "v_fma_f32 v116, v116, s32, -v2\n"
      "v_mfma_f32_16x16x32_bf16 v[32:35], v[80:83], a[180:183], v[32:35]\n"
      "v_cvt_pk_bf16_f32 v240, v72, v73\n"
      "v_cvt_pk_bf16_f32 v241, v74, v75\n"
      "v_mfma_f32_16x16x32_bf16 v[32:35], v[88:91], a[184:187], v[32:35]\n"
      "ds_read_b128 a[236:239], v40 offset:55296\n"
      "v_fma_f32 v117, v117, s32, -v2\n"
      "v_mfma_f32_16x16x32_bf16 v[32:35], v[104:107], a[188:191], v[32:35]\n"
      "s_waitcnt lgkmcnt(4)\n"
      "ds_read_b128 a[240:243], v37 offset:15360\n"
      "v_fma_f32 v100, v100, s32, -v3\n"
      "v_mfma_f32_16x16x32_bf16 v[28:31], v[60:63], a[192:195], 0\n"
      "ds_read_b128 a[244:247], v37 offset:56320\n"
      "v_fma_f32 v101, v101, s32, -v3\n"
      "v_mfma_f32_16x16x32_bf16 v[28:31], v[64:67], a[196:199], v[28:31]\n"
      "ds_read_b128 a[248:251], v41 offset:15360\n"
      "v_cvt_pk_bf16_f32 v234, v32, v33\n"
      "v_mfma_f32_16x16x32_bf16 v[28:31], v[44:47], a[200:203], v[28:31]\n"
      "ds_read_b128 a[252:255], v41 offset:56320\n"
      "v_cvt_pk_bf16_f32 v235, v34, v35\n"
      "v_mfma_f32_16x16x32_bf16 v[28:31], v[68:71], a[204:207], v[28:31]\n"
      "v_exp_f32 v116, v116\n"
      "v_mfma_f32_16x16x32_bf16 v[72:75], v[60:63], a[208:211], 0\n"
      "v_exp_f32 v117, v117\n"
      "v_mfma_f32_16x16x32_bf16 v[72:75], v[64:67], a[212:215], v[72:75]\n"
      "v_fma_f32 v118, v118, s32, -v2\n"
      "v_fma_f32 v119, v119, s32, -v2\n"
      "v_mfma_f32_16x16x32_bf16 v[72:75], v[44:47], a[216:219], v[72:75]\n"
      "v_fma_f32 v92, v92, s32, -v2\n"
      "v_fma_f32 v102, v102, s32, -v3\n"
      "v_mfma_f32_16x16x32_bf16 v[72:75], v[68:71], a[220:223], v[72:75]\n"
      "s_waitcnt lgkmcnt(1)\n"
      "v_exp_f32 v100, v100\n"
      "v_mfma_f32_16x16x32_bf16 v[32:35], v[60:63], a[224:227], 0\n"
      "v_exp_f32 v101, v101\n"
      "v_mfma_f32_16x16x32_bf16 v[32:35], v[64:67], a[228:231], v[32:35]\n"
      "v_fma_f32 v103, v103, s32, -v3\n"
      "v_fma_f32 v84, v84, s32, -v3\n"
      "v_mfma_f32_16x16x32_bf16 v[32:35], v[44:47], a[232:235], v[32:35]\n"
      "v_exp_f32 v118, v118\n"
      "v_mfma_f32_16x16x32_bf16 v[32:35], v[68:71], a[236:239], v[32:35]\n"
      "v_exp_f32 v119, v119\n"
      "v_mfma_f32_16x16x32_bf16 v[36:39], v[60:63], a[240:243], 0\n"
      "s_waitcnt lgkmcnt(0)\n"
      "s_barrier\n"
      "v_fma_f32 v93, v93, s32, -v2\n"
      "v_mfma_f32_16x16x32_bf16 v[36:39], v[64:67], a[244:247], v[36:39]\n"
      "v_exp_f32 v92, v92\n"
      "v_mfma_f32_16x16x32_bf16 v[36:39], v[44:47], a[248:251], v[36:39]\n"
      "v_fma_f32 v94, v94, s32, -v2\n"
      "v_add_f32 v4, v116, v117\n"
      "v_mfma_f32_16x16x32_bf16 v[36:39], v[68:71], a[252:255], v[36:39]\n"
      "v_exp_f32 v102, v102\n"
      "v_mfma_f32_16x16x32_bf16 v[76:79], v[24:27], a[192:195], 0\n"
      "v_exp_f32 v103, v103\n"
      "v_mfma_f32_16x16x32_bf16 v[76:79], v[80:83], a[196:199], v[76:79]\n"
      ".Lpad1%=:\n"
      "s_cbranch_execz .Lret0%=\n"
      "v_exp_f32 v84, v84\n"
      "v_mfma_f32_16x16x32_bf16 v[76:79], v[88:91], a[200:203], v[76:79]\n"
      "v_fma_f32 v85, v85, s32, -v3\n"
      "v_fma_f32 v86, v86, s32, -v3\n"
      "v_mfma_f32_16x16x32_bf16 v[76:79], v[104:107], a[204:207], v[76:79]\n"
      "v_add_f32 v2, v100, v101\n"
      "v_add_f32 v5, v118, v119\n"
      "v_mfma_f32_16x16x32_bf16 v[44:47], v[24:27], a[208:211], 0\n"
      "s_waitcnt vmcnt(0)\n"
      "ds_read_b128 v[60:63], v6 offset:20480\n"
      "v_add_f32 v4, v4, v92\n"
      "v_mfma_f32_16x16x32_bf16 v[44:47], v[80:83], a[212:215], v[44:47]\n"
      "ds_read_b128 v[64:67], v1 offset:20480\n"
      "v_add_f32 v7, v102, v103\n"
      "v_mfma_f32_16x16x32_bf16 v[44:47], v[88:91], a[216:219], v[44:47]\n"
      "ds_read_b128 v[68:71], v42 offset:20480\n"
      "v_add_f32 v2, v2, v84\n"
      "v_mfma_f32_16x16x32_bf16 v[44:47], v[104:107], a[220:223], v[44:47]\n"
      "ds_read_b128 v[96:99], v43 offset:20480\n"
      "v_mfma_f32_16x16x32_bf16 v[108:111], v[24:27], a[224:227], 0\n"
      "v_exp_f32 v93, v93\n"
      "v_mfma_f32_16x16x32_bf16 v[108:111], v[80:83], a[228:231], v[108:111]\n"
      "global_load_lds_dwordx4 v18, s[24:25] sc0 sc1 nt\n"
      "v_mfma_f32_16x16x32_bf16 v[108:111], v[88:91], a[232:235], v[108:111]\n"
      "global_load_lds_dwordx4 v19, s[24:25] offset:1024 sc0 sc1 nt\n"
      "v_mfma_f32_16x16x32_bf16 v[108:111], v[104:107], a[236:239], v[108:111]\n"
      "global_load_lds_dwordx4 v20, s[24:25] offset:2048 sc0 sc1 nt\n"
      "v_mfma_f32_16x16x32_bf16 v[120:123], v[24:27], a[240:243], 0\n"
      "global_load_lds_dwordx4 v17, s[24:25] offset:3072 sc0 sc1 nt\n"
      "v_mfma_f32_16x16x32_bf16 v[120:123], v[80:83], a[244:247], v[120:123]\n"
      "s_add_u32 s24, s22, 16384\n"
      "s_addc_u32 s25, s23, 0\n"
      "s_add_u32 m0, s19, 28672\n"
      "v_mfma_f32_16x16x32_bf16 v[120:123], v[88:91], a[248:251], v[120:123]\n"
      "v_exp_f32 v94, v94\n"
      "v_mfma_f32_16x16x32_bf16 v[120:123], v[104:107], a[252:255], v[120:123]\n"
      "s_waitcnt lgkmcnt(0)\n"
      "v_exp_f32 v85, v85\n"
      "v_mfma_f32_16x16x32_bf16 v[124:127], a[0:3], v[60:63], 0\n"
      "v_exp_f32 v86, v86\n"
      "v_mfma_f32_16x16x32_bf16 v[124:127], a[4:7], v[64:67], v[124:127]\n"
      "v_add_f32 v5, v5, v93\n"
      "v_add_f32 v4, v4, v94\n"
      "v_mfma_f32_16x16x32_bf16 v[124:127], a[8:11], v[68:71], v[124:127]\n"
      "v_add_f32 v7, v7, v85\n"
      "v_add_f32 v2, v2, v86\n"
      "v_mfma_f32_16x16x32_bf16 v[124:127], a[12:15], v[96:99], v[124:127]\n"
      "v_add_f32 v4, v4, v5\n"
      "v_add_f32 v2, v2, v7\n"
      "v_mfma_f32_16x16x32_bf16 v[24:27], a[16:19], v[60:63], 0\n"
      "s_nop 0\n"
      "v_permlane32_swap_b32 v4, v2\n"
      "v_mfma_f32_16x16x32_bf16 v[24:27], a[20:23], v[64:67], v[24:27]\n"
      "v_cvt_pk_bf16_f32 v80, v124, v125\n"
      "v_cvt_pk_bf16_f32 v81, v126, v127\n"
      "v_mfma_f32_16x16x32_bf16 v[24:27], a[24:27], v[68:71], v[24:27]\n"
      "v_add_f32 v5, v4, v2\n"
      "v_add_f32 v7, v4, v2\n"
      "v_mfma_f32_16x16x32_bf16 v[24:27], a[28:31], v[96:99], v[24:27]\n"
      "s_nop 0\n"
      "v_permlane16_swap_b32 v5, v7\n"
      "v_mfma_f32_16x16x32_bf16 v[88:91], a[32:35], v[60:63], 0\n"
      "v_add_f32 v5, v5, v7\n"
      "v_mfma_f32_16x16x32_bf16 v[88:91], a[36:39], v[64:67], v[88:91]\n"
      "v_cvt_pk_bf16_f32 v82, v24, v25\n"
      "v_cvt_pk_bf16_f32 v83, v26, v27\n"
      "v_mfma_f32_16x16x32_bf16 v[88:91], a[40:43], v[68:71], v[88:91]\n"
      "v_rcp_f32 v5, v5\n"
      "v_mfma_f32_16x16x32_bf16 v[88:91], a[44:47], v[96:99], v[88:91]\n"
      "v_cndmask_b32_e64 v4, 0, v5, s[38:39]\n"
      "v_cndmask_b32_e64 v2, 0, v5, s[38:39]\n"
      "v_mfma_f32_16x16x32_bf16 v[104:107], a[48:51], v[60:63], 0\n"
      "s_nop 0\n"
      "v_permlane32_swap_b32 v4, v2\n"
      "v_mfma_f32_16x16x32_bf16 v[104:107], a[52:55], v[64:67], v[104:107]\n"
      "v_cvt_pk_bf16_f32 v124, v88, v89\n"
      "v_cvt_pk_bf16_f32 v125, v90, v91\n"
      "v_mfma_f32_16x16x32_bf16 v[104:107], a[56:59], v[68:71], v[104:107]\n"
      "v_mul_f32 v116, v116, v4\n"
      "v_mul_f32 v117, v117, v4\n"
      "v_mfma_f32_16x16x32_bf16 v[104:107], a[60:63], v[96:99], v[104:107]\n"
      "v_mul_f32 v118, v118, v4\n"
      "v_mul_f32 v119, v119, v4\n"
      "v_mfma_f32_16x16x32_bf16 v[24:27], a[64:67], v[60:63], 0\n"
      "v_mul_f32 v92, v92, v4\n"
      "v_mul_f32 v93, v93, v4\n"
      "v_mfma_f32_16x16x32_bf16 v[24:27], a[68:71], v[64:67], v[24:27]\n"
      "v_cvt_pk_bf16_f32 v126, v104, v105\n"
      "v_cvt_pk_bf16_f32 v127, v106, v107\n"
      "v_mfma_f32_16x16x32_bf16 v[24:27], a[72:75], v[68:71], v[24:27]\n"
      "v_mul_f32 v94, v94, v4\n"
      "v_cvt_pk_bf16_f32 v88, v116, v117\n"
      "v_mfma_f32_16x16x32_bf16 v[24:27], a[76:79], v[96:99], v[24:27]\n"
      "v_cvt_pk_bf16_f32 v89, v118, v119\n"
      "v_cvt_pk_bf16_f32 v90, v92, v93\n"
      "v_mfma_f32_16x16x32_bf16 v[132:135], a[80:83], v[60:63], 0\n"
      "v_cvt_pk_bf16_f32 v91, v94, 0\n"
      "v_mul_f32 v100, v100, v2\n"
      "v_mfma_f32_16x16x32_bf16 v[132:135], a[84:87], v[64:67], v[132:135]\n"
      "v_cvt_pk_bf16_f32 v136, v24, v25\n"
      "v_cvt_pk_bf16_f32 v137, v26, v27\n"
      "v_mfma_f32_16x16x32_bf16 v[132:135], a[88:91], v[68:71], v[132:135]\n"
      "v_mul_f32 v101, v101, v2\n"
      "v_mul_f32 v102, v102, v2\n"
      "v_mfma_f32_16x16x32_bf16 v[132:135], a[92:95], v[96:99], v[132:135]\n"
      "v_mul_f32 v103, v103, v2\n"
      "v_mul_f32 v84, v84, v2\n"
      "v_mfma_f32_16x16x32_bf16 v[104:107], a[96:99], v[60:63], 0\n"
      "v_mul_f32 v85, v85, v2\n"
      "v_mul_f32 v86, v86, v2\n"
      "v_mfma_f32_16x16x32_bf16 v[104:107], a[100:103], v[64:67], v[104:107]\n"
      "v_cvt_pk_bf16_f32 v138, v132, v133\n"
      "v_cvt_pk_bf16_f32 v139, v134, v135\n"
      "v_mfma_f32_16x16x32_bf16 v[104:107], a[104:107], v[68:71], v[104:107]\n"
      "v_cvt_pk_bf16_f32 v24, v100, v101\n"
      "v_cvt_pk_bf16_f32 v25, v102, v103\n"
      "v_mfma_f32_16x16x32_bf16 v[104:107], a[108:111], v[96:99], v[104:107]\n"
      "v_cvt_pk_bf16_f32 v26, v84, v85\n"
      "v_cvt_pk_bf16_f32 v27, v86, 0\n"
      "v_mfma_f32_16x16x32_bf16 v[92:95], a[112:115], v[60:63], 0\n"
      "v_mfma_f32_16x16x32_bf16 v[92:95], a[116:119], v[64:67], v[92:95]\n"
      "v_cvt_pk_bf16_f32 v116, v104, v105\n"
      "v_cvt_pk_bf16_f32 v117, v106, v107\n"
      "v_mfma_f32_16x16x32_bf16 v[92:95], a[120:123], v[68:71], v[92:95]\n"
      "v_mfma_f32_16x16x32_bf16 v[92:95], a[124:127], v[96:99], v[92:95]\n"
      "v_mfma_f32_16x16x32_bf16 v[132:135], v[60:63], a[128:131], 0\n"
      "v_mfma_f32_16x16x32_bf16 v[132:135], v[64:67], a[132:135], v[132:135]\n"
      "v_cvt_pk_bf16_f32 v118, v92, v93\n"
      "v_cvt_pk_bf16_f32 v119, v94, v95\n"
      "v_mfma_f32_16x16x32_bf16 v[132:135], v[68:71], a[136:139], v[132:135]\n"
      "v_mfma_f32_16x16x32_bf16 v[132:135], v[96:99], a[140:143], v[132:135]\n"
      "global_load_lds_dwordx4 v18, s[24:25] sc0 sc1 nt\n"
      "v_mfma_f32_16x16x32_bf16 v[28:31], v[88:91], v[250:253], v[28:31]\n"
      "global_load_lds_dwordx4 v19, s[24:25] offset:1024 sc0 sc1 nt\n"
      "v_mfma_f32_16x16x32_bf16 v[72:75], v[88:91], v[244:247], v[72:75]\n"
      "global_load_lds_dwordx4 v20, s[24:25] offset:2048 sc0 sc1 nt\n"
      "v_mfma_f32_16x16x32_bf16 v[32:35], v[88:91], v[238:241], v[32:35]\n"
      "global_load_lds_dwordx4 v17, s[24:25] offset:3072 sc0 sc1 nt\n"
      "v_mfma_f32_16x16x32_bf16 v[36:39], v[88:91], v[232:235], v[36:39]\n"
      "s_add_u32 s24, s22, 20480\n"
      "s_addc_u32 s25, s23, 0\n"
      "s_add_u32 m0, s19, 32768\n"
      "v_mfma_f32_16x16x32_bf16 v[76:79], v[24:27], v[250:253], v[76:79]\n"
      "v_cvt_pk_bf16_f32 v254, v132, v133\n"
      "v_cvt_pk_bf16_f32 v255, v134, v135\n"
      "v_mfma_f32_16x16x32_bf16 v[44:47], v[24:27], v[244:247], v[44:47]\n"
      "v_cvt_pk_bf16_f32 v2, v28, v72\n"
      "v_cvt_pk_bf16_f32 v4, v29, v73\n"
      "v_mfma_f32_16x16x32_bf16 v[108:111], v[24:27], v[238:241], v[108:111]\n"
      "v_cvt_pk_bf16_f32 v22, v30, v74\n"
      "v_cvt_pk_bf16_f32 v40, v31, v75\n"
      "v_mfma_f32_16x16x32_bf16 v[120:123], v[24:27], v[232:235], v[120:123]\n"
      "v_cvt_pk_bf16_f32 v3, v32, v36\n"
      "v_cvt_pk_bf16_f32 v5, v33, v37\n"
      "v_mfma_f32_16x16x32_bf16 v[84:87], v[60:63], a[144:147], 0\n"
      "v_cvt_pk_bf16_f32 v23, v34, v38\n"
      "v_cvt_pk_bf16_f32 v41, v35, v39\n"
      "v_mfma_f32_16x16x32_bf16 v[84:87], v[64:67], a[148:151], v[84:87]\n"
      "v_pk_max_i16 v2, v2, 0\n"
      "v_pk_max_i16 v4, v4, 0\n"
      "v_mfma_f32_16x16x32_bf16 v[84:87], v[68:71], a[152:155], v[84:87]\n"
      "v_pk_max_i16 v22, v22, 0\n"
      "v_pk_max_i16 v40, v40, 0\n"
      "v_mfma_f32_16x16x32_bf16 v[84:87], v[96:99], a[156:159], v[84:87]\n"
      "v_pk_max_i16 v3, v3, 0\n"
      "v_pk_max_i16 v5, v5, 0\n"
      "v_mfma_f32_16x16x32_bf16 v[24:27], v[60:63], a[160:163], 0\n"
      "v_pk_max_i16 v23, v23, 0\n"
      "v_pk_max_i16 v41, v41, 0\n"
      "v_mfma_f32_16x16x32_bf16 v[24:27], v[64:67], a[164:167], v[24:27]\n"
      "v_cvt_pk_bf16_f32 v248, v84, v85\n"
      "v_cvt_pk_bf16_f32 v249, v86, v87\n"
      "v_mfma_f32_16x16x32_bf16 v[24:27], v[68:71], a[168:171], v[24:27]\n"
      "v_mfma_f32_16x16x32_bf16 v[24:27], v[96:99], a[172:175], v[24:27]\n"
      "v_mfma_f32_16x16x32_bf16 v[28:31], v[60:63], a[176:179], 0\n"
      "s_waitcnt vmcnt(4)\n"
      "ds_read_b128 v[32:35], v6 offset:24576\n"
      "v_mfma_f32_16x16x32_bf16 v[28:31], v[64:67], a[180:183], v[28:31]\n"
      "v_cvt_pk_bf16_f32 v242, v24, v25\n"
      "v_cvt_pk_bf16_f32 v243, v26, v27\n"
      "v_mfma_f32_16x16x32_bf16 v[28:31], v[68:71], a[184:187], v[28:31]\n"
      "ds_read_b128 v[36:39], v1 offset:24576\n"
      "v_mfma_f32_16x16x32_bf16 v[28:31], v[96:99], a[188:191], v[28:31]\n"
      "ds_read_b128 v[72:75], v42 offset:24576\n"
      "v_mfma_f32_16x16x32_bf16 v[84:87], v[60:63], a[192:195], 0\n"
      "ds_read_b128 v[88:91], v43 offset:24576\n"
      "v_mfma_f32_16x16x32_bf16 v[84:87], v[64:67], a[196:199], v[84:87]\n"
      "v_cvt_pk_bf16_f32 v236, v28, v29\n"
      "v_cvt_pk_bf16_f32 v237, v30, v31\n"
      "v_mfma_f32_16x16x32_bf16 v[84:87], v[68:71], a[200:203], v[84:87]\n"
      "v_mfma_f32_16x16x32_bf16 v[84:87], v[96:99], a[204:207], v[84:87]\n"
      "v_mfma_f32_16x16x32_bf16 v[24:27], v[60:63], a[208:211], 0\n"
      "v_mfma_f32_16x16x32_bf16 v[24:27], v[64:67], a[212:215], v[24:27]\n"
      "v_mfma_f32_16x16x32_bf16 v[24:27], v[68:71], a[216:219], v[24:27]\n"
      "v_mfma_f32_16x16x32_bf16 v[24:27], v[96:99], a[220:223], v[24:27]\n"
      "v_mfma_f32_16x16x32_bf16 v[92:95], v[60:63], a[224:227], 0\n"
      "v_mfma_f32_16x16x32_bf16 v[92:95], v[64:67], a[228:231], v[92:95]\n"
      "v_mfma_f32_16x16x32_bf16 v[92:95], v[68:71], a[232:235], v[92:95]\n"
      "v_mfma_f32_16x16x32_bf16 v[92:95], v[96:99], a[236:239], v[92:95]\n"
      "v_mfma_f32_16x16x32_bf16 v[100:103], v[60:63], a[240:243], 0\n"
      "v_mfma_f32_16x16x32_bf16 v[100:103], v[64:67], a[244:247], v[100:103]\n"
      "v_mfma_f32_16x16x32_bf16 v[100:103], v[68:71], a[248:251], v[100:103]\n"
      "v_mfma_f32_16x16x32_bf16 v[100:103], v[96:99], a[252:255], v[100:103]\n"
      "s_waitcnt lgkmcnt(0)\n"
      "v_mfma_f32_16x16x32_bf16 v[28:31], a[0:3], v[32:35], 0\n"
      "v_mfma_f32_16x16x32_bf16 v[28:31], a[4:7], v[36:39], v[28:31]\n"
      "v_mfma_f32_16x16x32_bf16 v[28:31], a[8:11], v[72:75], v[28:31]\n"
      "v_mfma_f32_16x16x32_bf16 v[28:31], a[12:15], v[88:91], v[28:31]\n"
      "v_mfma_f32_16x16x32_bf16 v[104:107], a[16:19], v[32:35], 0\n"
      "v_mfma_f32_16x16x32_bf16 v[104:107], a[20:23], v[36:39], v[104:107]\n"
      "v_cvt_pk_bf16_f32 v132, v28, v29\n"
      "v_cvt_pk_bf16_f32 v133, v30, v31\n"
      "v_mfma_f32_16x16x32_bf16 v[104:107], a[24:27], v[72:75], v[104:107]\n"
      "v_cndmask_b32_e32 v12, v12, v132, vcc\n"
      "v_cndmask_b32_e32 v13, v13, v133, vcc\n"
      "v_mfma_f32_16x16x32_bf16 v[104:107], a[28:31], v[88:91], v[104:107]\n"
      "v_mfma_f32_16x16x32_bf16 v[60:63], a[32:35], v[32:35], 0\n"
      "v_mfma_f32_16x16x32_bf16 v[60:63], a[36:39], v[36:39], v[60:63]\n"
      "global_load_lds_dwordx4 v18, s[24:25] sc0 sc1 nt\n"
      "v_mfma_f32_16x16x32_bf16 v[60:63], a[40:43], v[72:75], v[60:63]\n"
      "global_load_lds_dwordx4 v19, s[24:25] offset:1024 sc0 sc1 nt\n"
      "v_mfma_f32_16x16x32_bf16 v[60:63], a[44:47], v[88:91], v[60:63]\n"
      "global_load_lds_dwordx4 v20, s[24:25] offset:2048 sc0 sc1 nt\n"
      "v_mfma_f32_16x16x32_bf16 v[28:31], a[48:51], v[32:35], 0\n"
      "global_load_lds_dwordx4 v17, s[24:25] offset:3072 sc0 sc1 nt\n"
      "v_mfma_f32_16x16x32_bf16 v[28:31], a[52:55], v[36:39], v[28:31]\n"
      "s_add_u32 s24, s22, 24576\n"
      "s_addc_u32 s25, s23, 0\n"
      "s_add_u32 m0, s19, 0\n"
      "v_mfma_f32_16x16x32_bf16 v[28:31], a[56:59], v[72:75], v[28:31]\n"
      "v_cvt_pk_bf16_f32 v134, v104, v105\n"
      "v_cvt_pk_bf16_f32 v135, v106, v107\n"
      "v_mfma_f32_16x16x32_bf16 v[28:31], a[60:63], v[88:91], v[28:31]\n"
      "v_cvt_pk_bf16_f32 v64, v60, v61\n"
      "v_cvt_pk_bf16_f32 v65, v62, v63\n"
      "v_mfma_f32_16x16x32_bf16 v[68:71], a[64:67], v[32:35], 0\n"
      "v_cndmask_b32_e32 v14, v14, v134, vcc\n"
      "v_cndmask_b32_e32 v15, v15, v135, vcc\n"
      "v_mfma_f32_16x16x32_bf16 v[68:71], a[68:71], v[36:39], v[68:71]\n"
      "v_cvt_pk_bf16_f32 v66, v28, v29\n"
      "v_cvt_pk_bf16_f32 v67, v30, v31\n"
      "v_mfma_f32_16x16x32_bf16 v[68:71], a[72:75], v[72:75], v[68:71]\n"
      "v_cndmask_b32_e32 v112, v112, v64, vcc\n"
      "v_cndmask_b32_e32 v113, v113, v65, vcc\n"
      "v_mfma_f32_16x16x32_bf16 v[68:71], a[76:79], v[88:91], v[68:71]\n"
      "v_cndmask_b32_e32 v114, v114, v66, vcc\n"
      "v_cndmask_b32_e32 v115, v115, v67, vcc\n"
      "v_mfma_f32_16x16x32_bf16 v[60:63], a[80:83], v[32:35], 0\n"
      "s_waitcnt vmcnt(4)\n"
      "ds_read_b128 v[96:99], v6 offset:28672\n"
      "v_mfma_f32_16x16x32_bf16 v[60:63], a[84:87], v[36:39], v[60:63]\n"
      "ds_read_b128 v[104:107], v1 offset:28672\n"
      "v_cvt_pk_bf16_f32 v28, v68, v69\n"
      "v_mfma_f32_16x16x32_bf16 v[60:63], a[88:91], v[72:75], v[60:63]\n"
      "ds_read_b128 v[140:143], v42 offset:28672\n"
      "v_cvt_pk_bf16_f32 v29, v70, v71\n"
      "v_mfma_f32_16x16x32_bf16 v[60:63], a[92:95], v[88:91], v[60:63]\n"
      "ds_read_b128 v[144:147], v43 offset:28672\n"
      "v_cndmask_b32_e32 v128, v128, v28, vcc\n"
      "v_mfma_f32_16x16x32_bf16 v[148:151], a[96:99], v[32:35], 0\n"
      "v_cndmask_b32_e32 v129, v129, v29, vcc\n"
      "v_mfma_f32_16x16x32_bf16 v[148:151], a[100:103], v[36:39], v[148:151]\n"
      "v_cvt_pk_bf16_f32 v30, v60, v61\n"
      "v_cvt_pk_bf16_f32 v31, v62, v63\n"
      "v_mfma_f32_16x16x32_bf16 v[148:151], a[104:107], v[72:75], v[148:151]\n"
      "v_cndmask_b32_e32 v130, v130, v30, vcc\n"
      "v_cndmask_b32_e32 v131, v131, v31, vcc\n"
      "v_mfma_f32_16x16x32_bf16 v[148:151], a[108:111], v[88:91], v[148:151]\n"
      "v_mfma_f32_16x16x32_bf16 v[68:71], a[112:115], v[32:35], 0\n"
      "v_mfma_f32_16x16x32_bf16 v[68:71], a[116:119], v[36:39], v[68:71]\n"
      "v_cvt_pk_bf16_f32 v152, v148, v149\n"
      "v_cvt_pk_bf16_f32 v153, v150, v151\n"
      "v_mfma_f32_16x16x32_bf16 v[68:71], a[120:123], v[72:75], v[68:71]\n"
      "v_cndmask_b32_e32 v8, v8, v152, vcc\n"
      "v_cndmask_b32_e32 v9, v9, v153, vcc\n"
      "v_mfma_f32_16x16x32_bf16 v[68:71], a[124:127], v[88:91], v[68:71]\n"
      "s_waitcnt lgkmcnt(0)\n"
      "v_mfma_f32_16x16x32_bf16 v[60:63], a[0:3], v[96:99], 0\n"
      "v_mfma_f32_16x16x32_bf16 v[60:63], a[4:7], v[104:107], v[60:63]\n"
      "v_cvt_pk_bf16_f32 v154, v68, v69\n"
      "v_cvt_pk_bf16_f32 v155, v70, v71\n"
      "v_mfma_f32_16x16x32_bf16 v[60:63], a[8:11], v[140:143], v[60:63]\n"
      "v_cndmask_b32_e32 v10, v10, v154, vcc\n"
      "v_cndmask_b32_e32 v11, v11, v155, vcc\n"
      "v_mfma_f32_16x16x32_bf16 v[60:63], a[12:15], v[144:147], v[60:63]\n"
      "v_mfma_f32_16x16x32_bf16 v[148:151], a[16:19], v[96:99], 0\n"
      "v_mfma_f32_16x16x32_bf16 v[148:151], a[20:23], v[104:107], v[148:151]\n"
      "v_cvt_pk_bf16_f32 v156, v60, v61\n"
      "v_cvt_pk_bf16_f32 v157, v62, v63\n"
      "v_mfma_f32_16x16x32_bf16 v[148:151], a[24:27], v[140:143], v[148:151]\n"
      "v_mfma_f32_16x16x32_bf16 v[148:151], a[28:31], v[144:147], v[148:151]\n"
      "v_mfma_f32_16x16x32_bf16 v[160:163], v[128:131], v[12:15], v[50:53]\n"
      "v_mfma_f32_16x16x32_bf16 v[160:163], v[8:11], v[112:115], v[160:163]\n"
      "v_cvt_pk_bf16_f32 v158, v148, v149\n"
      "v_cvt_pk_bf16_f32 v159, v150, v151\n"
      "v_mfma_f32_16x16x32_bf16 v[68:71], v[136:139], v[12:15], 0\n"
      "v_mfma_f32_16x16x32_bf16 v[68:71], v[116:119], v[112:115], v[68:71]\n"
      "v_max3_f32 v7, v160, v162, v163\n"
      "v_mfma_f32_16x16x32_bf16 v[164:167], v[128:131], v[80:83], v[50:53]\n"
      "v_mfma_f32_16x16x32_bf16 v[164:167], v[8:11], v[124:127], v[164:167]\n"
      "v_max3_f32 v7, v7, v68, v69\n"
      "v_mfma_f32_16x16x32_bf16 v[60:63], v[136:139], v[80:83], 0\n"
      "v_max3_f32 v7, v7, v70, v71\n"
      "v_mfma_f32_16x16x32_bf16 v[60:63], v[116:119], v[124:127], v[60:63]\n"
      "v_max3_f32 v16, v164, v166, v167\n"
      "v_mfma_f32_16x16x32_bf16 v[168:171], a[32:35], v[96:99], 0\n"
      "v_mfma_f32_16x16x32_bf16 v[168:171], a[36:39], v[104:107], v[168:171]\n"
      "v_max3_f32 v16, v16, v60, v61\n"
      "v_mfma_f32_16x16x32_bf16 v[168:171], a[40:43], v[140:143], v[168:171]\n"
      "v_max3_f32 v16, v16, v62, v63\n"
      "v_mfma_f32_16x16x32_bf16 v[168:171], a[44:47], v[144:147], v[168:171]\n"
      "s_nop 0\n"
      "v_permlane32_swap_b32 v7, v16\n"
      "v_mfma_f32_16x16x32_bf16 v[8:11], a[48:51], v[96:99], 0\n"
      "v_max_f32 v21, v7, v16\n"
      "v_max_f32 v12, v7, v16\n"
      "v_mfma_f32_16x16x32_bf16 v[8:11], a[52:55], v[104:107], v[8:11]\n"
      "v_cvt_pk_bf16_f32 v80, v168, v169\n"
      "v_cvt_pk_bf16_f32 v81, v170, v171\n"
      "v_mfma_f32_16x16x32_bf16 v[8:11], a[56:59], v[140:143], v[8:11]\n"
      "v_permlane16_swap_b32 v21, v12\n"
      "v_mfma_f32_16x16x32_bf16 v[8:11], a[60:63], v[144:147], v[8:11]\n"
      "global_load_lds_dwordx4 v18, s[24:25] sc0 sc1 nt\n"
      "v_mfma_f32_16x16x32_bf16 v[112:115], a[64:67], v[96:99], 0\n"
      "global_load_lds_dwordx4 v19, s[24:25] offset:1024 sc0 sc1 nt\n"
      "v_mfma_f32_16x16x32_bf16 v[112:115], a[68:71], v[104:107], v[112:115]\n"
      "global_load_lds_dwordx4 v20, s[24:25] offset:2048 sc0 sc1 nt\n"
      "v_mfma_f32_16x16x32_bf16 v[112:115], a[72:75], v[140:143], v[112:115]\n"
      "global_load_lds_dwordx4 v17, s[24:25] offset:3072 sc0 sc1 nt\n"
      "v_mfma_f32_16x16x32_bf16 v[112:115], a[76:79], v[144:147], v[112:115]\n"
      "s_add_u32 s24, s22, 28672\n"
      "s_addc_u32 s25, s23, 0\n"
      "s_add_u32 m0, s19, 4096\n"
      "v_mfma_f32_16x16x32_bf16 v[116:119], a[80:83], v[96:99], 0\n"
      "v_cvt_pk_bf16_f32 v82, v8, v9\n"
      "v_cvt_pk_bf16_f32 v83, v10, v11\n"
      "v_mfma_f32_16x16x32_bf16 v[116:119], a[84:87], v[104:107], v[116:119]\n"
      "v_cvt_pk_bf16_f32 v124, v112, v113\n"
      "v_cvt_pk_bf16_f32 v125, v114, v115\n"
      "v_mfma_f32_16x16x32_bf16 v[116:119], a[88:91], v[140:143], v[116:119]\n"
      "v_max_f32 v21, v21, v12\n"
      "v_mfma_f32_16x16x32_bf16 v[116:119], a[92:95], v[144:147], v[116:119]\n"
      "v_mul_f32 v7, s32, v21\n"
      "v_mul_f32 v16, s32, v21\n"
      "v_mfma_f32_16x16x32_bf16 v[128:131], a[96:99], v[96:99], 0\n"
      "s_nop 0\n"
      "v_permlane32_swap_b32 v7, v16\n"
      "v_mfma_f32_16x16x32_bf16 v[128:131], a[100:103], v[104:107], v[128:131]\n"
      "v_cvt_pk_bf16_f32 v126, v116, v117\n"
      "v_cvt_pk_bf16_f32 v127, v118, v119\n"
      "v_mfma_f32_16x16x32_bf16 v[128:131], a[104:107], v[140:143], v[128:131]\n"
      "v_fma_f32 v160, v160, s32, -v7\n"
      "v_fma_f32 v162, v162, s32, -v7\n"
      "v_mfma_f32_16x16x32_bf16 v[128:131], a[108:111], v[144:147], v[128:131]\n"
      "v_fma_f32 v164, v164, s32, -v16\n"
      "v_fma_f32 v166, v166, s32, -v16\n"
      "v_mfma_f32_16x16x32_bf16 v[8:11], a[112:115], v[96:99], 0\n"
      "v_exp_f32 v160, v160\n"
      "v_mfma_f32_16x16x32_bf16 v[8:11], a[116:119], v[104:107], v[8:11]\n"
      "v_cvt_pk_bf16_f32 v12, v128, v129\n"
      "v_cvt_pk_bf16_f32 v13, v130, v131\n"
      "v_mfma_f32_16x16x32_bf16 v[8:11], a[120:123], v[140:143], v[8:11]\n"
      "v_exp_f32 v162, v162\n"
      "v_mfma_f32_16x16x32_bf16 v[8:11], a[124:127], v[144:147], v[8:11]\n"
      "v_fma_f32 v163, v163, s32, -v7\n"
      "v_fma_f32 v68, v68, s32, -v7\n"
      "v_mfma_f32_16x16x32_bf16 v[112:115], v[32:35], a[128:131], 0\n"
      "v_fma_f32 v69, v69, s32, -v7\n"
      "v_fma_f32 v167, v167, s32, -v16\n"
      "v_mfma_f32_16x16x32_bf16 v[112:115], v[36:39], a[132:135], v[112:115]\n"
      "v_cvt_pk_bf16_f32 v14, v8, v9\n"
      "v_cvt_pk_bf16_f32 v15, v10, v11\n"
      "v_mfma_f32_16x16x32_bf16 v[112:115], v[72:75], a[136:139], v[112:115]\n"
      "v_exp_f32 v164, v164\n"
      "v_mfma_f32_16x16x32_bf16 v[112:115], v[88:91], a[140:143], v[112:115]\n"
      "v_exp_f32 v166, v166\n"
      "v_mfma_f32_16x16x32_bf16 v[116:119], v[32:35], a[144:147], 0\n"
      "v_fma_f32 v60, v60, s32, -v16\n"
      "v_fma_f32 v61, v61, s32, -v16\n"
      "v_mfma_f32_16x16x32_bf16 v[116:119], v[36:39], a[148:151], v[116:119]\n"
      "v_cvt_pk_bf16_f32 v226, v112, v113\n"
      "v_cvt_pk_bf16_f32 v227, v114, v115\n"
      "v_mfma_f32_16x16x32_bf16 v[116:119], v[72:75], a[152:155], v[116:119]\n"
      "v_exp_f32 v163, v163\n"
      "v_mfma_f32_16x16x32_bf16 v[116:119], v[88:91], a[156:159], v[116:119]\n"
      "v_exp_f32 v68, v68\n"
      "v_mfma_f32_16x16x32_bf16 v[8:11], v[28:31], v[132:135], 0\n"
      "v_exp_f32 v69, v69\n"
      "v_mfma_f32_16x16x32_bf16 v[8:11], v[152:155], v[64:67], v[8:11]\n"
      "v_cvt_pk_bf16_f32 v220, v116, v117\n"
      "v_cvt_pk_bf16_f32 v221, v118, v119\n"
      "v_mfma_f32_16x16x32_bf16 v[128:131], v[124:127], v[132:135], v[48:51]\n"
      "v_fma_f32 v70, v70, s32, -v7\n"
      "v_fma_f32 v71, v71, s32, -v7\n"
      "v_mfma_f32_16x16x32_bf16 v[128:131], v[12:15], v[64:67], v[128:131]\n"
      "v_add_f32 v21, v160, v162\n"
      "v_fma_f32 v62, v62, s32, -v16\n"
      "v_mfma_f32_16x16x32_bf16 v[112:115], v[28:31], v[156:159], 0\n"
      "v_exp_f32 v167, v167\n"
      "v_mfma_f32_16x16x32_bf16 v[112:115], v[152:155], v[80:83], v[112:115]\n"
      "v_exp_f32 v60, v60\n"
      "v_mfma_f32_16x16x32_bf16 v[136:139], v[124:127], v[156:159], v[48:51]\n"
      "v_exp_f32 v61, v61\n"
      "v_mfma_f32_16x16x32_bf16 v[136:139], v[12:15], v[80:83], v[136:139]\n"
      "v_fma_f32 v63, v63, s32, -v16\n"
      "v_add_f32 v7, v164, v166\n"
      "v_mfma_f32_16x16x32_bf16 v[64:67], v[32:35], a[160:163], 0\n"
      "v_exp_f32 v70, v70\n"
      "v_mfma_f32_16x16x32_bf16 v[64:67], v[36:39], a[164:167], v[64:67]\n"
      "v_exp_f32 v71, v71\n"
      "v_mfma_f32_16x16x32_bf16 v[64:67], v[72:75], a[168:171], v[64:67]\n"
      "v_add_f32 v28, v163, v68\n"
      "v_add_f32 v21, v21, v69\n"
      "v_mfma_f32_16x16x32_bf16 v[64:67], v[88:91], a[172:175], v[64:67]\n"
      "v_exp_f32 v62, v62\n"
      "v_mfma_f32_16x16x32_bf16 v[116:119], v[32:35], a[176:179], 0\n"
      "v_exp_f32 v63, v63\n"
      "v_mfma_f32_16x16x32_bf16 v[116:119], v[36:39], a[180:183], v[116:119]\n"
      "v_cvt_pk_bf16_f32 v214, v64, v65\n"
      "v_cvt_pk_bf16_f32 v215, v66, v67\n"
      "v_mfma_f32_16x16x32_bf16 v[116:119], v[72:75], a[184:187], v[116:119]\n"
      "v_add_f32 v16, v167, v60\n"
      "v_add_f32 v7, v7, v61\n"
      "v_mfma_f32_16x16x32_bf16 v[116:119], v[88:91], a[188:191], v[116:119]\n"
      "v_add_f32 v28, v28, v70\n"
      "v_add_f32 v21, v21, v71\n"
      "v_mfma_f32_16x16x32_bf16 v[132:135], v[32:35], a[192:195], 0\n"
      "v_add_f32 v16, v16, v62\n"
      "v_add_f32 v7, v7, v63\n"
      "v_mfma_f32_16x16x32_bf16 v[132:135], v[36:39], a[196:199], v[132:135]\n"
      "v_cvt_pk_bf16_f32 v208, v116, v117\n"
      "v_cvt_pk_bf16_f32 v209, v118, v119\n"
      "v_mfma_f32_16x16x32_bf16 v[132:135], v[72:75], a[200:203], v[132:135]\n"
      "v_add_f32 v21, v21, v28\n"
      "v_add_f32 v7, v7, v16\n"
      "v_mfma_f32_16x16x32_bf16 v[132:135], v[88:91], a[204:207], v[132:135]\n"
      "s_nop 0\n"
      "v_permlane32_swap_b32 v21, v7\n"
      "v_mfma_f32_16x16x32_bf16 v[64:67], v[32:35], a[208:211], 0\n"
      "s_waitcnt vmcnt(4)\n"
      "ds_read_b128 v[148:151], v6 offset:32768\n"
      "v_add_f32 v28, v21, v7\n"
      "v_mfma_f32_16x16x32_bf16 v[64:67], v[36:39], a[212:215], v[64:67]\n"
      "global_load_lds_dwordx4 v18, s[24:25] sc0 sc1 nt\n"
      "v_mfma_f32_16x16x32_bf16 v[64:67], v[72:75], a[216:219], v[64:67]\n"
      "global_load_lds_dwordx4 v19, s[24:25] offset:1024 sc0 sc1 nt\n"
      "v_mfma_f32_16x16x32_bf16 v[64:67], v[88:91], a[220:223], v[64:67]\n"
      "global_load_lds_dwordx4 v20, s[24:25] offset:2048 sc0 sc1 nt\n"
      "v_mfma_f32_16x16x32_bf16 v[116:119], v[32:35], a[224:227], 0\n"
      "global_load_lds_dwordx4 v17, s[24:25] offset:3072 sc0 sc1 nt\n"
      "v_mfma_f32_16x16x32_bf16 v[116:119], v[36:39], a[228:231], v[116:119]\n"
      "s_add_u32 s24, s22, 32768\n"
      "s_addc_u32 s25, s23, 0\n"
      "s_add_u32 m0, s19, 8192\n"
      "ds_read_b128 v[152:155], v1 offset:32768\n"
      "v_mfma_f32_16x16x32_bf16 v[116:119], v[72:75], a[232:235], v[116:119]\n"
      "ds_read_b128 v[168:171], v42 offset:32768\n"
      "v_add_f32 v16, v21, v7\n"
      "v_mfma_f32_16x16x32_bf16 v[116:119], v[88:91], a[236:239], v[116:119]\n"
      "ds_read_b128 v[172:175], v43 offset:32768\n"
      "v_mov_b32 v252, v226\n"
      "v_mfma_f32_16x16x32_bf16 v[176:179], v[32:35], a[240:243], 0\n"
      "v_permlane16_swap_b32 v28, v16\n"
      "v_mfma_f32_16x16x32_bf16 v[176:179], v[36:39], a[244:247], v[176:179]\n"
      "v_add_f32 v28, v28, v16\n"
      "v_mov_b32 v246, v220\n"
      "v_mfma_f32_16x16x32_bf16 v[176:179], v[72:75], a[248:251], v[176:179]\n"
      "v_rcp_f32 v28, v28\n"
      "v_mfma_f32_16x16x32_bf16 v[176:179], v[88:91], a[252:255], v[176:179]\n"
      "s_waitcnt lgkmcnt(2)\n"
      "v_cndmask_b32_e64 v21, 0, v28, s[40:41]\n"
      "v_cndmask_b32_e64 v7, 0, v28, s[40:41]\n"
      "v_mfma_f32_16x16x32_bf16 v[180:183], a[0:3], v[148:151], 0\n"
      "s_nop 0\n"
      "v_permlane32_swap_b32 v21, v7\n"
      "v_mfma_f32_16x16x32_bf16 v[180:183], a[4:7], v[152:155], v[180:183]\n"
      "s_waitcnt lgkmcnt(0)\n"
      "v_mul_f32 v160, v160, v21\n"
      "v_mul_f32 v162, v162, v21\n"
      "v_mfma_f32_16x16x32_bf16 v[180:183], a[8:11], v[168:171], v[180:183]\n"
      "v_mul_f32 v163, v163, v21\n"
      "v_mul_f32 v68, v68, v21\n"
      "v_mfma_f32_16x16x32_bf16 v[180:183], a[12:15], v[172:175], v[180:183]\n"
      "v_mul_f32 v69, v69, v21\n"
      "v_mul_f32 v70, v70, v21\n"
      "v_mfma_f32_16x16x32_bf16 v[28:31], a[16:19], v[148:151], 0\n"
      "v_mul_f32 v71, v71, v21\n"
      "v_cvt_pk_bf16_f32 v32, v160, 0\n"
      "v_mfma_f32_16x16x32_bf16 v[28:31], a[20:23], v[152:155], v[28:31]\n"
      "v_cvt_pk_bf16_f32 v36, v180, v181\n"
      "v_cvt_pk_bf16_f32 v37, v182, v183\n"
      "v_mfma_f32_16x16x32_bf16 v[28:31], a[24:27], v[168:171], v[28:31]\n"
      "v_cvt_pk_bf16_f32 v33, v162, v163\n"
      "v_cvt_pk_bf16_f32 v34, v68, v69\n"
      "v_mfma_f32_16x16x32_bf16 v[28:31], a[28:31], v[172:175], v[28:31]\n"
      "v_cvt_pk_bf16_f32 v35, v70, v71\n"
      "v_cndmask_b32_e32 v72, 0, v32, vcc\n"
      "v_mfma_f32_16x16x32_bf16 v[88:91], a[32:35], v[148:151], 0\n"
      "v_cndmask_b32_e32 v73, 0, v33, vcc\n"
      "v_cndmask_b32_e32 v74, 0, v34, vcc\n"
      "v_mfma_f32_16x16x32_bf16 v[88:91], a[36:39], v[152:155], v[88:91]\n"
      "v_cvt_pk_bf16_f32 v38, v28, v29\n"
      "v_cvt_pk_bf16_f32 v39, v30, v31\n"
      "v_mfma_f32_16x16x32_bf16 v[88:91], a[40:43], v[168:171], v[88:91]\n"
      "v_cndmask_b32_e32 v75, 0, v35, vcc\n"
      "v_cndmask_b32_e64 v32, v32, 0, vcc\n"
      "v_mfma_f32_16x16x32_bf16 v[88:91], a[44:47], v[172:175], v[88:91]\n"
      "v_cndmask_b32_e64 v33, v33, 0, vcc\n"
      "v_cndmask_b32_e64 v34, v34, 0, vcc\n"
      "v_mfma_f32_16x16x32_bf16 v[68:71], a[48:51], v[148:151], 0\n"
      "v_cndmask_b32_e64 v35, v35, 0, vcc\n"
      "v_mov_b32 v240, v214\n"
      "v_mfma_f32_16x16x32_bf16 v[68:71], a[52:55], v[152:155], v[68:71]\n"
      "v_cvt_pk_bf16_f32 v160, v88, v89\n"
      "v_cvt_pk_bf16_f32 v161, v90, v91\n"
      "v_mfma_f32_16x16x32_bf16 v[68:71], a[56:59], v[168:171], v[68:71]\n"
      "v_mul_f32 v164, v164, v7\n"
      "v_mul_f32 v166, v166, v7\n"
      "v_mfma_f32_16x16x32_bf16 v[68:71], a[60:63], v[172:175], v[68:71]\n"
      "v_mul_f32 v167, v167, v7\n"
      "v_mul_f32 v60, v60, v7\n"
      "v_mfma_f32_16x16x32_bf16 v[28:31], a[64:67], v[148:151], 0\n"
      "v_mul_f32 v61, v61, v7\n"
      "v_mul_f32 v62, v62, v7\n"
      "v_mfma_f32_16x16x32_bf16 v[28:31], a[68:71], v[152:155], v[28:31]\n"
      "v_cvt_pk_bf16_f32 v162, v68, v69\n"
      "v_cvt_pk_bf16_f32 v163, v70, v71\n"
      "v_mfma_f32_16x16x32_bf16 v[28:31], a[72:75], v[168:171], v[28:31]\n"
      "v_mul_f32 v63, v63, v7\n"
      "v_mov_b32 v234, v208\n"
      "v_mfma_f32_16x16x32_bf16 v[28:31], a[76:79], v[172:175], v[28:31]\n"
      "v_cvt_pk_bf16_f32 v88, v164, 0\n"
      "v_cvt_pk_bf16_f32 v89, v166, v167\n"
      "v_mfma_f32_16x16x32_bf16 v[180:183], a[80:83], v[148:151], 0\n"
      "s_waitcnt vmcnt(4)\n"
      "ds_read_b128 v[184:187], v6 offset:0\n"
      "v_cvt_pk_bf16_f32 v90, v60, v61\n"
      "v_mfma_f32_16x16x32_bf16 v[180:183], a[84:87], v[152:155], v[180:183]\n"
      "ds_read_b128 v[68:71], v1 offset:0\n"
      "v_cvt_pk_bf16_f32 v188, v28, v29\n"
      "v_mfma_f32_16x16x32_bf16 v[180:183], a[88:91], v[168:171], v[180:183]\n"
      "ds_read_b128 v[192:195], v42 offset:0\n"
      "v_cvt_pk_bf16_f32 v189, v30, v31\n"
      "v_mfma_f32_16x16x32_bf16 v[180:183], a[92:95], v[172:175], v[180:183]\n"
      "ds_read_b128 v[164:167], v43 offset:0\n"
      "v_cvt_pk_bf16_f32 v91, v62, v63\n"
      "v_mfma_f32_16x16x32_bf16 v[196:199], a[96:99], v[148:151], 0\n"
      "v_max3_f32 v7, v9, v10, v11\n"
      "v_max3_f32 v16, v113, v114, v115\n"
      "v_mfma_f32_16x16x32_bf16 v[196:199], a[100:103], v[152:155], v[196:199]\n"
      "v_cvt_pk_bf16_f32 v190, v180, v181\n"
      "v_cvt_pk_bf16_f32 v191, v182, v183\n"
      "v_mfma_f32_16x16x32_bf16 v[196:199], a[104:107], v[168:171], v[196:199]\n"
      "v_max3_f32 v7, v7, v128, v129\n"
      "v_max3_f32 v16, v16, v136, v137\n"
      "v_mfma_f32_16x16x32_bf16 v[196:199], a[108:111], v[172:175], v[196:199]\n"
      "v_max3_f32 v7, v7, v130, v131\n"
      "v_max3_f32 v16, v16, v138, v139\n"
      "v_mfma_f32_16x16x32_bf16 v[28:31], a[112:115], v[148:151], 0\n"
      "s_nop 0\n"
      "v_permlane32_swap_b32 v7, v16\n"
      "v_mfma_f32_16x16x32_bf16 v[28:31], a[116:119], v[152:155], v[28:31]\n"
      "v_cvt_pk_bf16_f32 v60, v196, v197\n"
      "v_cvt_pk_bf16_f32 v61, v198, v199\n"
      "v_mfma_f32_16x16x32_bf16 v[28:31], a[120:123], v[168:171], v[28:31]\n"
      "v_max_f32 v21, v7, v16\n"
      "v_max_f32 v180, v7, v16\n"
      "v_mfma_f32_16x16x32_bf16 v[28:31], a[124:127], v[172:175], v[28:31]\n"
      "s_waitcnt lgkmcnt(0)\n"
      "v_permlane16_swap_b32 v21, v180\n"
      "v_mfma_f32_16x16x32_bf16 v[200:203], a[0:3], v[184:187], 0\n"
      "v_max_f32 v21, v21, v180\n"
      "v_mfma_f32_16x16x32_bf16 v[200:203], a[4:7], v[68:71], v[200:203]\n"
      "v_cvt_pk_bf16_f32 v62, v28, v29\n"
      "v_cvt_pk_bf16_f32 v63, v30, v31\n"
      "v_mfma_f32_16x16x32_bf16 v[200:203], a[8:11], v[192:195], v[200:203]\n"
      "v_mul_f32 v7, s32, v21\n"
      "v_mul_f32 v16, s32, v21\n"
      "v_mfma_f32_16x16x32_bf16 v[200:203], a[12:15], v[164:167], v[200:203]\n"
      "global_load_lds_dwordx4 v18, s[24:25] sc0 sc1 nt\n"
      "v_mfma_f32_16x16x32_bf16 v[196:199], a[16:19], v[184:187], 0\n"
      "global_load_lds_dwordx4 v19, s[24:25] offset:1024 sc0 sc1 nt\n"
      "v_mfma_f32_16x16x32_bf16 v[196:199], a[20:23], v[68:71], v[196:199]\n"
      "global_load_lds_dwordx4 v20, s[24:25] offset:2048 sc0 sc1 nt\n"
      "v_mfma_f32_16x16x32_bf16 v[196:199], a[24:27], v[192:195], v[196:199]\n"
      ".Lpad2%=:\n"
      "s_cbranch_execz .Lret0%=\n"
      "global_load_lds_dwordx4 v17, s[24:25] offset:3072 sc0 sc1 nt\n"
      "v_mfma_f32_16x16x32_bf16 v[196:199], a[28:31], v[164:167], v[196:199]\n"
      "s_add_u32 s24, s22, 36864\n"
      "s_addc_u32 s25, s23, 0\n"
      "s_add_u32 m0, s19, 12288\n"
      "v_mfma_f32_16x16x32_bf16 v[28:31], a[32:35], v[184:187], 0\n"
      "v_cvt_pk_bf16_f32 v180, v200, v201\n"
      "v_cvt_pk_bf16_f32 v181, v202, v203\n"
      "v_mfma_f32_16x16x32_bf16 v[28:31], a[36:39], v[68:71], v[28:31]\n"
      "v_cvt_pk_bf16_f32 v182, v196, v197\n"
      "v_cvt_pk_bf16_f32 v183, v198, v199\n"
      "v_mfma_f32_16x16x32_bf16 v[28:31], a[40:43], v[192:195], v[28:31]\n"
      "v_cndmask_b32_e32 v156, v156, v180, vcc\n"
      "v_cndmask_b32_e32 v157, v157, v181, vcc\n"
      "v_mfma_f32_16x16x32_bf16 v[28:31], a[44:47], v[164:167], v[28:31]\n"
      "v_cndmask_b32_e32 v158, v158, v182, vcc\n"
      "v_cndmask_b32_e32 v159, v159, v183, vcc\n"
      "v_mfma_f32_16x16x32_bf16 v[76:79], v[32:35], v[252:255], v[76:79]\n"
      "v_permlane32_swap_b32 v7, v16\n"
      "v_mfma_f32_16x16x32_bf16 v[44:47], v[32:35], v[246:249], v[44:47]\n"
      "v_cvt_pk_bf16_f32 v200, v28, v29\n"
      "v_cvt_pk_bf16_f32 v201, v30, v31\n"
      "v_mfma_f32_16x16x32_bf16 v[108:111], v[32:35], v[240:243], v[108:111]\n"
      "v_cndmask_b32_e32 v80, v80, v200, vcc\n"
      "v_cndmask_b32_e32 v81, v81, v201, vcc\n"
      "v_mfma_f32_16x16x32_bf16 v[120:123], v[32:35], v[234:237], v[120:123]\n"
      "v_cvt_pk_bf16_f32 v196, v76, v44\n"
      "v_cvt_pk_bf16_f32 v198, v77, v45\n"
      "v_mfma_f32_16x16x32_bf16 v[84:87], v[88:91], v[252:255], v[84:87]\n"
      "v_cvt_pk_bf16_f32 v204, v78, v46\n"
      "v_cvt_pk_bf16_f32 v206, v79, v47\n"
      "v_mfma_f32_16x16x32_bf16 v[24:27], v[88:91], v[246:249], v[24:27]\n"
      "v_cvt_pk_bf16_f32 v197, v108, v120\n"
      "v_cvt_pk_bf16_f32 v199, v109, v121\n"
      "v_mfma_f32_16x16x32_bf16 v[92:95], v[88:91], v[240:243], v[92:95]\n"
      "v_cvt_pk_bf16_f32 v205, v110, v122\n"
      "v_cvt_pk_bf16_f32 v207, v111, v123\n"
      "v_mfma_f32_16x16x32_bf16 v[100:103], v[88:91], v[234:237], v[100:103]\n"
      "v_cvt_pk_bf16_f32 v28, v84, v24\n"
      "v_cvt_pk_bf16_f32 v30, v85, v25\n"
      "v_mfma_f32_16x16x32_bf16 v[132:135], v[72:75], v[252:255], v[132:135]\n"
      "v_cvt_pk_bf16_f32 v32, v86, v26\n"
      "v_cvt_pk_bf16_f32 v34, v87, v27\n"
      "v_mfma_f32_16x16x32_bf16 v[64:67], v[72:75], v[246:249], v[64:67]\n"
      "v_cvt_pk_bf16_f32 v29, v92, v100\n"
      "v_cvt_pk_bf16_f32 v31, v93, v101\n"
      "v_mfma_f32_16x16x32_bf16 v[116:119], v[72:75], v[240:243], v[116:119]\n"
      "v_cvt_pk_bf16_f32 v33, v94, v102\n"
      "v_cvt_pk_bf16_f32 v35, v95, v103\n"
      "v_mfma_f32_16x16x32_bf16 v[176:179], v[72:75], v[234:237], v[176:179]\n"
      "v_fma_f32 v9, v9, s32, -v7\n"
      "v_fma_f32 v10, v10, s32, -v7\n"
      "v_mfma_f32_16x16x32_bf16 v[44:47], a[48:51], v[184:187], 0\n"
      "v_fma_f32 v113, v113, s32, -v16\n"
      "v_fma_f32 v114, v114, s32, -v16\n"
      "v_mfma_f32_16x16x32_bf16 v[44:47], a[52:55], v[68:71], v[44:47]\n"
      "v_exp_f32 v9, v9\n"
      "v_mfma_f32_16x16x32_bf16 v[44:47], a[56:59], v[192:195], v[44:47]\n"
      "v_exp_f32 v10, v10\n"
      "v_mfma_f32_16x16x32_bf16 v[44:47], a[60:63], v[164:167], v[44:47]\n"
      "v_fma_f32 v11, v11, s32, -v7\n"
      "v_fma_f32 v128, v128, s32, -v7\n"
      "v_mfma_f32_16x16x32_bf16 v[24:27], a[64:67], v[184:187], 0\n"
      "v_fma_f32 v129, v129, s32, -v7\n"
      "v_fma_f32 v115, v115, s32, -v16\n"
      "v_mfma_f32_16x16x32_bf16 v[24:27], a[68:71], v[68:71], v[24:27]\n"
      "v_cvt_pk_bf16_f32 v202, v44, v45\n"
      "v_cvt_pk_bf16_f32 v203, v46, v47\n"
      "v_mfma_f32_16x16x32_bf16 v[24:27], a[72:75], v[192:195], v[24:27]\n"
      "v_cndmask_b32_e32 v82, v82, v202, vcc\n"
      "v_cndmask_b32_e32 v83, v83, v203, vcc\n"
      "v_mfma_f32_16x16x32_bf16 v[24:27], a[76:79], v[164:167], v[24:27]\n"
      "v_exp_f32 v113, v113\n"
      "v_mfma_f32_16x16x32_bf16 v[72:75], a[80:83], v[184:187], 0\n"
      "v_exp_f32 v114, v114\n"
      "v_mfma_f32_16x16x32_bf16 v[72:75], a[84:87], v[68:71], v[72:75]\n"
      "v_cvt_pk_bf16_f32 v76, v24, v25\n"
      "v_cvt_pk_bf16_f32 v77, v26, v27\n"
      "v_mfma_f32_16x16x32_bf16 v[72:75], a[88:91], v[192:195], v[72:75]\n"
      "v_cndmask_b32_e32 v124, v124, v76, vcc\n"
      "v_cndmask_b32_e32 v125, v125, v77, vcc\n"
      "v_mfma_f32_16x16x32_bf16 v[72:75], a[92:95], v[164:167], v[72:75]\n"
      "v_fma_f32 v136, v136, s32, -v16\n"
      "v_fma_f32 v137, v137, s32, -v16\n"
      "v_mfma_f32_16x16x32_bf16 v[44:47], a[96:99], v[184:187], 0\n"
      "v_exp_f32 v11, v11\n"
      "v_mfma_f32_16x16x32_bf16 v[44:47], a[100:103], v[68:71], v[44:47]\n"
      "v_cvt_pk_bf16_f32 v78, v72, v73\n"
      "v_cvt_pk_bf16_f32 v79, v74, v75\n"
      "v_mfma_f32_16x16x32_bf16 v[44:47], a[104:107], v[192:195], v[44:47]\n"
      "v_cndmask_b32_e32 v126, v126, v78, vcc\n"
      "v_cndmask_b32_e32 v127, v127, v79, vcc\n"
      "v_mfma_f32_16x16x32_bf16 v[44:47], a[108:111], v[164:167], v[44:47]\n"
      "v_exp_f32 v128, v128\n"
      "v_mfma_f32_16x16x32_bf16 v[24:27], a[112:115], v[184:187], 0\n"
      "v_exp_f32 v129, v129\n"
      "v_mfma_f32_16x16x32_bf16 v[24:27], a[116:119], v[68:71], v[24:27]\n"
      "v_cvt_pk_bf16_f32 v84, v44, v45\n"
      "v_cvt_pk_bf16_f32 v85, v46, v47\n"
      "v_mfma_f32_16x16x32_bf16 v[24:27], a[120:123], v[192:195], v[24:27]\n"
      "v_cndmask_b32_e32 v12, v12, v84, vcc\n"
      "v_cndmask_b32_e32 v13, v13, v85, vcc\n"
      "v_mfma_f32_16x16x32_bf16 v[24:27], a[124:127], v[164:167], v[24:27]\n"
      "v_fma_f32 v130, v130, s32, -v7\n"
      "v_fma_f32 v131, v131, s32, -v7\n"
      "v_mfma_f32_16x16x32_bf16 v[72:75], v[96:99], a[128:131], 0\n"
      "v_add_f32 v21, v9, v10\n"
      "v_fma_f32 v138, v138, s32, -v16\n"
      "v_mfma_f32_16x16x32_bf16 v[72:75], v[104:107], a[132:135], v[72:75]\n"
      "v_cvt_pk_bf16_f32 v86, v24, v25\n"
      "v_cvt_pk_bf16_f32 v87, v26, v27\n"
      "v_mfma_f32_16x16x32_bf16 v[72:75], v[140:143], a[136:139], v[72:75]\n"
      "v_cndmask_b32_e32 v14, v14, v86, vcc\n"
      "v_cndmask_b32_e32 v15, v15, v87, vcc\n"
      "v_mfma_f32_16x16x32_bf16 v[72:75], v[144:147], a[140:143], v[72:75]\n"
      "v_exp_f32 v115, v115\n"
      "v_mfma_f32_16x16x32_bf16 v[44:47], v[96:99], a[144:147], 0\n"
      "v_exp_f32 v136, v136\n"
      "v_mfma_f32_16x16x32_bf16 v[44:47], v[104:107], a[148:151], v[44:47]\n"
      "global_load_lds_dwordx4 v18, s[24:25] sc0 sc1 nt\n"
      "v_mfma_f32_16x16x32_bf16 v[44:47], v[140:143], a[152:155], v[44:47]\n"
      "global_load_lds_dwordx4 v19, s[24:25] offset:1024 sc0 sc1 nt\n"
      "v_mfma_f32_16x16x32_bf16 v[44:47], v[144:147], a[156:159], v[44:47]\n"
      "global_load_lds_dwordx4 v20, s[24:25] offset:2048 sc0 sc1 nt\n"
      "v_mfma_f32_16x16x32_bf16 v[24:27], v[124:127], v[156:159], v[54:57]\n"
      "global_load_lds_dwordx4 v17, s[24:25] offset:3072 sc0 sc1 nt\n"
      "v_mfma_f32_16x16x32_bf16 v[24:27], v[12:15], v[80:83], v[24:27]\n"
      "s_add_u32 s24, s22, 40960\n"
      "s_addc_u32 s25, s23, 0\n"
      "s_add_u32 m0, s19, 16384\n"
      "v_mfma_f32_16x16x32_bf16 v[88:91], v[188:191], v[156:159], 0\n"
      "v_cvt_pk_bf16_f32 v228, v72, v73\n"
      "v_cvt_pk_bf16_f32 v229, v74, v75\n"
      "v_mfma_f32_16x16x32_bf16 v[88:91], v[60:63], v[80:83], v[88:91]\n"
      "v_cvt_pk_bf16_f32 v222, v44, v45\n"
      "v_cvt_pk_bf16_f32 v223, v46, v47\n"
      "v_mfma_f32_16x16x32_bf16 v[92:95], v[124:127], v[36:39], v[54:57]\n"
      "v_exp_f32 v137, v137\n"
      "v_mfma_f32_16x16x32_bf16 v[92:95], v[12:15], v[160:163], v[92:95]\n"
      "v_fma_f32 v139, v139, s32, -v16\n"
      "v_add_f32 v7, v113, v114\n"
      "v_mfma_f32_16x16x32_bf16 v[100:103], v[188:191], v[36:39], 0\n"
      "v_exp_f32 v130, v130\n"
      "v_mfma_f32_16x16x32_bf16 v[100:103], v[60:63], v[160:163], v[100:103]\n"
      "v_exp_f32 v131, v131\n"
      "v_mfma_f32_16x16x32_bf16 v[72:75], v[96:99], a[160:163], 0\n"
      "v_add_f32 v80, v11, v128\n"
      "v_add_f32 v21, v21, v129\n"
      "v_mfma_f32_16x16x32_bf16 v[72:75], v[104:107], a[164:167], v[72:75]\n"
      "v_exp_f32 v138, v138\n"
      "v_mfma_f32_16x16x32_bf16 v[72:75], v[140:143], a[168:171], v[72:75]\n"
      "v_exp_f32 v139, v139\n"
      "v_mfma_f32_16x16x32_bf16 v[72:75], v[144:147], a[172:175], v[72:75]\n"
      "v_add_f32 v16, v115, v136\n"
      "v_add_f32 v7, v7, v137\n"
      "v_mfma_f32_16x16x32_bf16 v[12:15], v[96:99], a[176:179], 0\n"
      "v_add_f32 v80, v80, v130\n"
      "v_add_f32 v21, v21, v131\n"
      "v_add_f32 v16, v16, v138\n"
      "v_add_f32 v7, v7, v139\n"
      "v_mfma_f32_16x16x32_bf16 v[12:15], v[104:107], a[180:183], v[12:15]\n"
      "v_add_f32 v21, v21, v80\n"
      "v_add_f32 v7, v7, v16\n"
      "v_mfma_f32_16x16x32_bf16 v[12:15], v[140:143], a[184:187], v[12:15]\n"
      "s_nop 0\n"
      "v_permlane32_swap_b32 v21, v7\n"
      "v_mfma_f32_16x16x32_bf16 v[12:15], v[144:147], a[188:191], v[12:15]\n"
      "v_add_f32 v80, v21, v7\n"
      "v_add_f32 v16, v21, v7\n"
      "v_mfma_f32_16x16x32_bf16 v[36:39], v[96:99], a[192:195], 0\n"
      "s_nop 0\n"
      "v_permlane16_swap_b32 v80, v16\n"
      "v_mfma_f32_16x16x32_bf16 v[36:39], v[104:107], a[196:199], v[36:39]\n"
      "v_add_f32 v80, v80, v16\n"
      "v_cvt_pk_bf16_f32 v216, v72, v73\n"
      "v_mfma_f32_16x16x32_bf16 v[36:39], v[140:143], a[200:203], v[36:39]\n"
      "v_rcp_f32 v80, v80\n"
      "v_mfma_f32_16x16x32_bf16 v[36:39], v[144:147], a[204:207], v[36:39]\n"
      "v_cndmask_b32_e64 v21, 0, v80, s[42:43]\n"
      "v_cndmask_b32_e64 v7, 0, v80, s[42:43]\n"
      "v_cvt_pk_bf16_f32 v217, v74, v75\n"
      "v_mfma_f32_16x16x32_bf16 v[44:47], v[96:99], a[208:211], 0\n"
      "v_permlane32_swap_b32 v21, v7\n"
      "v_mfma_f32_16x16x32_bf16 v[44:47], v[104:107], a[212:215], v[44:47]\n"
      "v_mul_f32 v9, v9, v21\n"
      "v_mul_f32 v10, v10, v21\n"
      "v_mul_f32 v11, v11, v21\n"
      "v_mul_f32 v128, v128, v21\n"
      "v_mul_f32 v129, v129, v21\n"
      "v_mul_f32 v130, v130, v21\n"
      "v_mul_f32 v131, v131, v21\n"
      "v_mfma_f32_16x16x32_bf16 v[44:47], v[140:143], a[216:219], v[44:47]\n"
      "v_cvt_pk_bf16_f32 v60, 0, v9\n"
      "v_cvt_pk_bf16_f32 v61, v10, v11\n"
      "v_cvt_pk_bf16_f32 v62, v128, v129\n"
      "v_cvt_pk_bf16_f32 v63, v130, v131\n"
      "v_mfma_f32_16x16x32_bf16 v[44:47], v[144:147], a[220:223], v[44:47]\n"
      "v_cvt_pk_bf16_f32 v210, v12, v13\n"
      "v_cvt_pk_bf16_f32 v211, v14, v15\n"
      "v_mfma_f32_16x16x32_bf16 v[132:135], v[60:63], v[226:229], v[132:135]\n"
      "v_mul_f32 v113, v113, v7\n"
      "v_mul_f32 v114, v114, v7\n"
      "v_mfma_f32_16x16x32_bf16 v[64:67], v[60:63], v[220:223], v[64:67]\n"
      "v_mul_f32 v115, v115, v7\n"
      "v_mul_f32 v136, v136, v7\n"
      "v_mul_f32 v137, v137, v7\n"
      "v_mul_f32 v138, v138, v7\n"
      "v_mul_f32 v139, v139, v7\n"
      "v_mfma_f32_16x16x32_bf16 v[116:119], v[60:63], v[214:217], v[116:119]\n"
      "v_cvt_pk_bf16_f32 v8, 0, v113\n"
      "v_cvt_pk_bf16_f32 v9, v114, v115\n"
      "v_cvt_pk_bf16_f32 v10, v136, v137\n"
      "v_cvt_pk_bf16_f32 v11, v138, v139\n"
      "v_mfma_f32_16x16x32_bf16 v[176:179], v[60:63], v[208:211], v[176:179]\n"
      "v_cvt_pk_bf16_f32 v12, v132, v64\n"
      "v_cvt_pk_bf16_f32 v14, v133, v65\n"
      "v_mfma_f32_16x16x32_bf16 v[36:39], v[8:11], v[226:229], v[36:39]\n"
      "v_cvt_pk_bf16_f32 v72, v134, v66\n"
      "v_cvt_pk_bf16_f32 v74, v135, v67\n"
      "v_mfma_f32_16x16x32_bf16 v[44:47], v[8:11], v[220:223], v[44:47]\n"
      "v_cvt_pk_bf16_f32 v13, v116, v176\n"
      "v_cvt_pk_bf16_f32 v15, v117, v177\n"
      "v_mfma_f32_16x16x32_bf16 v[80:83], v[8:11], v[214:217], 0\n"
      "v_cvt_pk_bf16_f32 v73, v118, v178\n"
      "v_cvt_pk_bf16_f32 v75, v119, v179\n"
      "v_mfma_f32_16x16x32_bf16 v[60:63], v[8:11], v[208:211], 0\n"
      "v_max3_f32 v7, v24, v25, v27\n"
      "v_max3_f32 v16, v92, v93, v95\n"
      "v_mfma_f32_16x16x32_bf16 v[80:83], v[96:99], a[224:227], v[80:83]\n"
      "v_max3_f32 v7, v7, v88, v89\n"
      "v_max3_f32 v16, v16, v100, v101\n"
      "v_mfma_f32_16x16x32_bf16 v[80:83], v[104:107], a[228:231], v[80:83]\n"
      "v_max3_f32 v7, v7, v90, v91\n"
      "v_max3_f32 v16, v16, v102, v103\n"
      "v_mfma_f32_16x16x32_bf16 v[80:83], v[140:143], a[232:235], v[80:83]\n"
      "s_nop 0\n"
      "v_permlane32_swap_b32 v7, v16\n"
      "v_mfma_f32_16x16x32_bf16 v[80:83], v[144:147], a[236:239], v[80:83]\n"
      "v_max_f32 v21, v7, v16\n"
      "v_max_f32 v8, v7, v16\n"
      "v_mfma_f32_16x16x32_bf16 v[60:63], v[96:99], a[240:243], v[60:63]\n"
      "s_nop 0\n"
      "v_permlane16_swap_b32 v21, v8\n"
      "v_mfma_f32_16x16x32_bf16 v[60:63], v[104:107], a[244:247], v[60:63]\n"
      "v_max_f32 v21, v21, v8\n"
      "v_pk_max_i16 v196, v196, 0\n"
      "v_mfma_f32_16x16x32_bf16 v[60:63], v[140:143], a[248:251], v[60:63]\n"
      "v_mul_f32 v7, s32, v21\n"
      "v_mul_f32 v16, s32, v21\n"
      "v_mfma_f32_16x16x32_bf16 v[60:63], v[144:147], a[252:255], v[60:63]\n"
      "s_nop 0\n"
      "v_permlane32_swap_b32 v7, v16\n"
      "v_mfma_f32_16x16x32_bf16 v[64:67], v[148:151], a[128:131], 0\n"
      "v_fma_f32 v24, v24, s32, -v7\n"
      "v_fma_f32 v25, v25, s32, -v7\n"
      "v_mfma_f32_16x16x32_bf16 v[64:67], v[152:155], a[132:135], v[64:67]\n"
      "v_fma_f32 v92, v92, s32, -v16\n"
      "v_fma_f32 v93, v93, s32, -v16\n"
      "v_mfma_f32_16x16x32_bf16 v[64:67], v[168:171], a[136:139], v[64:67]\n"
      "v_exp_f32 v24, v24\n"
      "v_mfma_f32_16x16x32_bf16 v[64:67], v[172:175], a[140:143], v[64:67]\n"
      "global_load_lds_dwordx4 v18, s[24:25] sc0 sc1 nt\n"
      "v_mfma_f32_16x16x32_bf16 v[8:11], v[148:151], a[144:147], 0\n"
      "global_load_lds_dwordx4 v19, s[24:25] offset:1024 sc0 sc1 nt\n"
      "v_mfma_f32_16x16x32_bf16 v[8:11], v[152:155], a[148:151], v[8:11]\n"
      "global_load_lds_dwordx4 v20, s[24:25] offset:2048 sc0 sc1 nt\n"
      "v_mfma_f32_16x16x32_bf16 v[8:11], v[168:171], a[152:155], v[8:11]\n"
      "global_load_lds_dwordx4 v17, s[24:25] offset:3072 sc0 sc1 nt\n"
      "v_mfma_f32_16x16x32_bf16 v[8:11], v[172:175], a[156:159], v[8:11]\n"
      "s_add_u32 s24, s22, 45056\n"
      "s_addc_u32 s25, s23, 0\n"
      "s_add_u32 m0, s19, 20480\n"
      "v_mfma_f32_16x16x32_bf16 v[96:99], v[148:151], a[160:163], 0\n"
      "v_cvt_pk_bf16_f32 v230, v64, v65\n"
      "v_cvt_pk_bf16_f32 v231, v66, v67\n"
      "v_mfma_f32_16x16x32_bf16 v[96:99], v[152:155], a[164:167], v[96:99]\n"
      "v_cvt_pk_bf16_f32 v224, v8, v9\n"
      "v_cvt_pk_bf16_f32 v225, v10, v11\n"
      "v_mfma_f32_16x16x32_bf16 v[96:99], v[168:171], a[168:171], v[96:99]\n"
      "v_exp_f32 v25, v25\n"
      "v_mfma_f32_16x16x32_bf16 v[96:99], v[172:175], a[172:175], v[96:99]\n"
      "v_fma_f32 v27, v27, s32, -v7\n"
      "v_fma_f32 v88, v88, s32, -v7\n"
      "v_mfma_f32_16x16x32_bf16 v[104:107], v[148:151], a[176:179], 0\n"
      "v_fma_f32 v89, v89, s32, -v7\n"
      "v_fma_f32 v95, v95, s32, -v16\n"
      "v_mfma_f32_16x16x32_bf16 v[104:107], v[152:155], a[180:183], v[104:107]\n"
      "v_cvt_pk_bf16_f32 v218, v96, v97\n"
      "v_cvt_pk_bf16_f32 v219, v98, v99\n"
      "v_mfma_f32_16x16x32_bf16 v[104:107], v[168:171], a[184:187], v[104:107]\n"
      "v_exp_f32 v92, v92\n"
      "v_mfma_f32_16x16x32_bf16 v[104:107], v[172:175], a[188:191], v[104:107]\n"
      "v_exp_f32 v93, v93\n"
      "v_mfma_f32_16x16x32_bf16 v[8:11], v[148:151], a[192:195], 0\n"
      "v_fma_f32 v100, v100, s32, -v16\n"
      "v_fma_f32 v101, v101, s32, -v16\n"
      "v_mfma_f32_16x16x32_bf16 v[8:11], v[152:155], a[196:199], v[8:11]\n"
      "v_cvt_pk_bf16_f32 v212, v104, v105\n"
      "v_cvt_pk_bf16_f32 v213, v106, v107\n"
      "v_mfma_f32_16x16x32_bf16 v[8:11], v[168:171], a[200:203], v[8:11]\n"
      "v_exp_f32 v27, v27\n"
      "v_mfma_f32_16x16x32_bf16 v[8:11], v[172:175], a[204:207], v[8:11]\n"
      "v_exp_f32 v88, v88\n"
      "v_mfma_f32_16x16x32_bf16 v[64:67], v[148:151], a[208:211], 0\n"
      "s_waitcnt vmcnt(12)\n"
      "ds_read_b128 v[96:99], v6 offset:4096\n"
      "v_fma_f32 v90, v90, s32, -v7\n"
      "v_mfma_f32_16x16x32_bf16 v[64:67], v[152:155], a[212:215], v[64:67]\n"
      "ds_read_b128 v[108:111], v1 offset:4096\n"
      "v_fma_f32 v91, v91, s32, -v7\n"
      "v_mfma_f32_16x16x32_bf16 v[64:67], v[168:171], a[216:219], v[64:67]\n"
      "ds_read_b128 v[104:107], v42 offset:4096\n"
      "v_add_f32 v21, v24, v25\n"
      "v_mfma_f32_16x16x32_bf16 v[64:67], v[172:175], a[220:223], v[64:67]\n"
      "ds_read_b128 v[112:115], v43 offset:4096\n"
      "v_fma_f32 v102, v102, s32, -v16\n"
      "v_mfma_f32_16x16x32_bf16 v[116:119], v[148:151], a[224:227], 0\n"
      "v_exp_f32 v89, v89\n"
      "v_mfma_f32_16x16x32_bf16 v[116:119], v[152:155], a[228:231], v[116:119]\n"
      "v_exp_f32 v95, v95\n"
      "v_mfma_f32_16x16x32_bf16 v[116:119], v[168:171], a[232:235], v[116:119]\n"
      "v_exp_f32 v100, v100\n"
      "v_mfma_f32_16x16x32_bf16 v[116:119], v[172:175], a[236:239], v[116:119]\n"
      "v_exp_f32 v101, v101\n"
      "v_mfma_f32_16x16x32_bf16 v[120:123], v[148:151], a[240:243], 0\n"
      "v_fma_f32 v103, v103, s32, -v16\n"
      "v_add_f32 v7, v92, v93\n"
      "v_mfma_f32_16x16x32_bf16 v[120:123], v[152:155], a[244:247], v[120:123]\n"
      "v_exp_f32 v90, v90\n"
      "v_mfma_f32_16x16x32_bf16 v[120:123], v[168:171], a[248:251], v[120:123]\n"
      "v_exp_f32 v91, v91\n"
      "v_mfma_f32_16x16x32_bf16 v[120:123], v[172:175], a[252:255], v[120:123]\n"
      "s_waitcnt lgkmcnt(0)\n"
      "v_add_f32 v124, v27, v88\n"
      "v_add_f32 v21, v21, v89\n"
      "v_mfma_f32_16x16x32_bf16 v[128:131], a[0:3], v[96:99], 0\n"
      "v_exp_f32 v102, v102\n"
      "v_mfma_f32_16x16x32_bf16 v[128:131], a[4:7], v[108:111], v[128:131]\n"
      "v_exp_f32 v103, v103\n"
      "v_mfma_f32_16x16x32_bf16 v[128:131], a[8:11], v[104:107], v[128:131]\n"
      "v_add_f32 v16, v95, v100\n"
      "v_add_f32 v7, v7, v101\n"
      "v_mfma_f32_16x16x32_bf16 v[128:131], a[12:15], v[112:115], v[128:131]\n"
      "v_add_f32 v124, v124, v90\n"
      "v_add_f32 v21, v21, v91\n"
      "v_mfma_f32_16x16x32_bf16 v[132:135], a[16:19], v[96:99], 0\n"
      "v_add_f32 v16, v16, v102\n"
      "v_add_f32 v7, v7, v103\n"
      "v_mfma_f32_16x16x32_bf16 v[132:135], a[20:23], v[108:111], v[132:135]\n"
      "v_cvt_pk_bf16_f32 v136, v128, v129\n"
      "v_cvt_pk_bf16_f32 v137, v130, v131\n"
      "v_mfma_f32_16x16x32_bf16 v[132:135], a[24:27], v[104:107], v[132:135]\n"
      "v_add_f32 v21, v21, v124\n"
      "v_add_f32 v7, v7, v16\n"
      "v_mfma_f32_16x16x32_bf16 v[132:135], a[28:31], v[112:115], v[132:135]\n"
      "s_nop 0\n"
      "v_permlane32_swap_b32 v21, v7\n"
      "v_mfma_f32_16x16x32_bf16 v[140:143], a[32:35], v[96:99], 0\n"
      "v_add_f32 v124, v21, v7\n"
      "v_add_f32 v16, v21, v7\n"
      "v_mfma_f32_16x16x32_bf16 v[140:143], a[36:39], v[108:111], v[140:143]\n"
      "v_cvt_pk_bf16_f32 v138, v132, v133\n"
      "v_cvt_pk_bf16_f32 v139, v134, v135\n"
      "v_mfma_f32_16x16x32_bf16 v[140:143], a[40:43], v[104:107], v[140:143]\n"
      "v_permlane16_swap_b32 v124, v16\n"
      "v_mfma_f32_16x16x32_bf16 v[140:143], a[44:47], v[112:115], v[140:143]\n"
      "v_add_f32 v124, v124, v16\n"
      "v_pk_max_i16 v198, v198, 0\n"
      "v_mfma_f32_16x16x32_bf16 v[128:131], a[48:51], v[96:99], 0\n"
      "v_rcp_f32 v124, v124\n"
      "v_mfma_f32_16x16x32_bf16 v[128:131], a[52:55], v[108:111], v[128:131]\n"
      "v_cvt_pk_bf16_f32 v144, v140, v141\n"
      "v_cvt_pk_bf16_f32 v145, v142, v143\n"
      "v_mfma_f32_16x16x32_bf16 v[128:131], a[56:59], v[104:107], v[128:131]\n"
      "v_cndmask_b32_e64 v21, 0, v124, s[44:45]\n"
      "v_cndmask_b32_e64 v7, 0, v124, s[44:45]\n"
      "v_mfma_f32_16x16x32_bf16 v[128:131], a[60:63], v[112:115], v[128:131]\n"
      "s_nop 0\n"
      "v_permlane32_swap_b32 v21, v7\n"
      "v_mfma_f32_16x16x32_bf16 v[132:135], a[64:67], v[96:99], 0\n"
      "v_mul_f32 v24, v24, v21\n"
      "v_mul_f32 v25, v25, v21\n"
      "v_mfma_f32_16x16x32_bf16 v[132:135], a[68:71], v[108:111], v[132:135]\n"
      "global_load_lds_dwordx4 v18, s[24:25] sc0 sc1 nt\n"
      "v_mfma_f32_16x16x32_bf16 v[132:135], a[72:75], v[104:107], v[132:135]\n"
      "global_load_lds_dwordx4 v19, s[24:25] offset:1024 sc0 sc1 nt\n"
      "v_mfma_f32_16x16x32_bf16 v[132:135], a[76:79], v[112:115], v[132:135]\n"
      "global_load_lds_dwordx4 v20, s[24:25] offset:2048 sc0 sc1 nt\n"
      "v_mfma_f32_16x16x32_bf16 v[124:127], a[80:83], v[96:99], 0\n"
      "global_load_lds_dwordx4 v17, s[24:25] offset:3072 sc0 sc1 nt\n"
      "v_mfma_f32_16x16x32_bf16 v[124:127], a[84:87], v[108:111], v[124:127]\n"
      "s_add_u32 s24, s22, 49152\n"
      "s_addc_u32 s25, s23, 0\n"
      "s_add_u32 m0, s19, 24576\n"
      "v_mfma_f32_16x16x32_bf16 v[124:127], a[88:91], v[104:107], v[124:127]\n"
      "v_cvt_pk_bf16_f32 v146, v128, v129\n"
      "v_cvt_pk_bf16_f32 v147, v130, v131\n"
      "v_mfma_f32_16x16x32_bf16 v[124:127], a[92:95], v[112:115], v[124:127]\n"
      "s_waitcnt vmcnt(12)\n"
      "ds_read_b128 v[140:143], v6 offset:8192\n"
      "v_cvt_pk_bf16_f32 v148, v132, v133\n"
      "v_mfma_f32_16x16x32_bf16 v[152:155], a[96:99], v[96:99], 0\n"
      "ds_read_b128 v[156:159], v1 offset:8192\n"
      "v_cvt_pk_bf16_f32 v149, v134, v135\n"
      "v_mfma_f32_16x16x32_bf16 v[152:155], a[100:103], v[108:111], v[152:155]\n"
      "ds_read_b128 v[160:163], v42 offset:8192\n"
      "v_cvt_pk_bf16_f32 v150, v124, v125\n"
      "v_mfma_f32_16x16x32_bf16 v[152:155], a[104:107], v[104:107], v[152:155]\n"
      "ds_read_b128 v[128:131], v43 offset:8192\n"
      "v_cvt_pk_bf16_f32 v151, v126, v127\n"
      "v_mfma_f32_16x16x32_bf16 v[152:155], a[108:111], v[112:115], v[152:155]\n"
      "v_mul_f32 v27, v27, v21\n"
      "v_mul_f32 v88, v88, v21\n"
      "v_mfma_f32_16x16x32_bf16 v[168:171], a[112:115], v[96:99], 0\n"
      "v_mul_f32 v89, v89, v21\n"
      "v_mul_f32 v90, v90, v21\n"
      "v_mfma_f32_16x16x32_bf16 v[168:171], a[116:119], v[108:111], v[168:171]\n"
      "v_cvt_pk_bf16_f32 v132, v152, v153\n"
      "v_cvt_pk_bf16_f32 v133, v154, v155\n"
      "v_mfma_f32_16x16x32_bf16 v[168:171], a[120:123], v[104:107], v[168:171]\n"
      "v_mul_f32 v91, v91, v21\n"
      "v_cvt_pk_bf16_f32 v124, v24, v25\n"
      "v_mfma_f32_16x16x32_bf16 v[168:171], a[124:127], v[112:115], v[168:171]\n"
      "s_waitcnt lgkmcnt(1)\n"
      "v_cvt_pk_bf16_f32 v125, 0, v27\n"
      "v_cvt_pk_bf16_f32 v126, v88, v89\n"
      "v_mfma_f32_16x16x32_bf16 v[172:175], a[0:3], v[140:143], 0\n"
      "v_cvt_pk_bf16_f32 v127, v90, v91\n"
      "v_cndmask_b32_e32 v176, 0, v124, vcc\n"
      "v_mfma_f32_16x16x32_bf16 v[172:175], a[4:7], v[156:159], v[172:175]\n"
      "v_cvt_pk_bf16_f32 v134, v168, v169\n"
      "v_cvt_pk_bf16_f32 v135, v170, v171\n"
      "v_mfma_f32_16x16x32_bf16 v[172:175], a[8:11], v[160:163], v[172:175]\n"
      "s_waitcnt lgkmcnt(0)\n"
      "v_cndmask_b32_e32 v177, 0, v125, vcc\n"
      "v_cndmask_b32_e32 v178, 0, v126, vcc\n"
      "v_mfma_f32_16x16x32_bf16 v[172:175], a[12:15], v[128:131], v[172:175]\n"
      "v_cndmask_b32_e32 v179, 0, v127, vcc\n"
      "v_cndmask_b32_e64 v124, v124, 0, vcc\n"
      "v_mfma_f32_16x16x32_bf16 v[24:27], a[16:19], v[140:143], 0\n"
      "v_cndmask_b32_e64 v125, v125, 0, vcc\n"
      "v_cndmask_b32_e64 v126, v126, 0, vcc\n"
      "v_mfma_f32_16x16x32_bf16 v[24:27], a[20:23], v[156:159], v[24:27]\n"
      "v_cvt_pk_bf16_f32 v88, v172, v173\n"
      "v_cvt_pk_bf16_f32 v89, v174, v175\n"
      "v_mfma_f32_16x16x32_bf16 v[24:27], a[24:27], v[160:163], v[24:27]\n"
      "v_cndmask_b32_e32 v180, v180, v88, vcc\n"
      "v_cndmask_b32_e32 v181, v181, v89, vcc\n"
      "v_mfma_f32_16x16x32_bf16 v[24:27], a[28:31], v[128:131], v[24:27]\n"
      "v_cndmask_b32_e64 v127, v127, 0, vcc\n"
      "v_mul_f32 v92, v92, v7\n"
      "v_mfma_f32_16x16x32_bf16 v[152:155], a[32:35], v[140:143], 0\n"
      "v_mul_f32 v93, v93, v7\n"
      "v_mul_f32 v95, v95, v7\n"
      "v_mfma_f32_16x16x32_bf16 v[152:155], a[36:39], v[156:159], v[152:155]\n"
      "v_cvt_pk_bf16_f32 v90, v24, v25\n"
      "v_cvt_pk_bf16_f32 v91, v26, v27\n"
      "v_mfma_f32_16x16x32_bf16 v[152:155], a[40:43], v[160:163], v[152:155]\n"
      "v_cndmask_b32_e32 v182, v182, v90, vcc\n"
      "v_cndmask_b32_e32 v183, v183, v91, vcc\n"
      "v_mfma_f32_16x16x32_bf16 v[152:155], a[44:47], v[128:131], v[152:155]\n"
      "v_mul_f32 v100, v100, v7\n"
      "v_mul_f32 v101, v101, v7\n"
      "v_mfma_f32_16x16x32_bf16 v[168:171], a[48:51], v[140:143], 0\n"
      "v_mul_f32 v102, v102, v7\n"
      "v_mul_f32 v103, v103, v7\n"
      "v_mfma_f32_16x16x32_bf16 v[168:171], a[52:55], v[156:159], v[168:171]\n"
      "v_cvt_pk_bf16_f32 v172, v152, v153\n"
      "v_cvt_pk_bf16_f32 v173, v154, v155\n"
      "v_mfma_f32_16x16x32_bf16 v[168:171], a[56:59], v[160:163], v[168:171]\n"
      "v_cndmask_b32_e32 v200, v200, v172, vcc\n"
      "v_cndmask_b32_e32 v201, v201, v173, vcc\n"
      "v_mfma_f32_16x16x32_bf16 v[168:171], a[60:63], v[128:131], v[168:171]\n"
      "v_cvt_pk_bf16_f32 v24, v92, v93\n"
      "v_cvt_pk_bf16_f32 v25, 0, v95\n"
      "v_mfma_f32_16x16x32_bf16 v[188:191], a[64:67], v[140:143], 0\n"
      "v_cvt_pk_bf16_f32 v26, v100, v101\n"
      "v_cvt_pk_bf16_f32 v27, v102, v103\n"
      "v_mfma_f32_16x16x32_bf16 v[188:191], a[68:71], v[156:159], v[188:191]\n"
      "v_cvt_pk_bf16_f32 v174, v168, v169\n"
      "v_cvt_pk_bf16_f32 v175, v170, v171\n"
      "v_mfma_f32_16x16x32_bf16 v[188:191], a[72:75], v[160:163], v[188:191]\n"
      "v_cndmask_b32_e32 v202, v202, v174, vcc\n"
      "v_cndmask_b32_e32 v203, v203, v175, vcc\n"
      "v_mfma_f32_16x16x32_bf16 v[188:191], a[76:79], v[128:131], v[188:191]\n"
      "v_pk_max_i16 v204, v204, 0\n"
      "v_pk_max_i16 v206, v206, 0\n"
      "v_mfma_f32_16x16x32_bf16 v[92:95], a[80:83], v[140:143], 0\n"
      "v_pk_max_i16 v197, v197, 0\n"
      "v_pk_max_i16 v199, v199, 0\n"
      "v_mfma_f32_16x16x32_bf16 v[92:95], a[84:87], v[156:159], v[92:95]\n"
      "v_cvt_pk_bf16_f32 v100, v188, v189\n"
      "v_cvt_pk_bf16_f32 v101, v190, v191\n"
      "v_mfma_f32_16x16x32_bf16 v[92:95], a[88:91], v[160:163], v[92:95]\n"
      "v_cndmask_b32_e32 v76, v76, v100, vcc\n"
      "v_cndmask_b32_e32 v77, v77, v101, vcc\n"
      "v_mfma_f32_16x16x32_bf16 v[92:95], a[92:95], v[128:131], v[92:95]\n"
      "v_pk_max_i16 v205, v205, 0\n"
      "v_pk_max_i16 v207, v207, 0\n"
      "v_mfma_f32_16x16x32_bf16 v[152:155], a[96:99], v[140:143], 0\n"
      "v_pk_max_i16 v28, v28, 0\n"
      "v_pk_max_i16 v30, v30, 0\n"
      "v_mfma_f32_16x16x32_bf16 v[152:155], a[100:103], v[156:159], v[152:155]\n"
      "v_cvt_pk_bf16_f32 v102, v92, v93\n"
      "v_cvt_pk_bf16_f32 v103, v94, v95\n"
      "v_mfma_f32_16x16x32_bf16 v[152:155], a[104:107], v[160:163], v[152:155]\n"
      "v_cndmask_b32_e32 v78, v78, v102, vcc\n"
      "v_cndmask_b32_e32 v79, v79, v103, vcc\n"
      "v_mfma_f32_16x16x32_bf16 v[152:155], a[108:111], v[128:131], v[152:155]\n"
      "v_pk_max_i16 v32, v32, 0\n"
      "v_pk_max_i16 v34, v34, 0\n"
      "v_mfma_f32_16x16x32_bf16 v[168:171], a[112:115], v[140:143], 0\n"
      "v_pk_max_i16 v29, v29, 0\n"
      "v_pk_max_i16 v31, v31, 0\n"
      "v_mfma_f32_16x16x32_bf16 v[168:171], a[116:119], v[156:159], v[168:171]\n"
      "v_cvt_pk_bf16_f32 v188, v152, v153\n"
      "v_cvt_pk_bf16_f32 v189, v154, v155\n"
      "v_mfma_f32_16x16x32_bf16 v[168:171], a[120:123], v[160:163], v[168:171]\n"
      "v_cndmask_b32_e32 v84, v84, v188, vcc\n"
      "v_cndmask_b32_e32 v85, v85, v189, vcc\n"
      "v_mfma_f32_16x16x32_bf16 v[168:171], a[124:127], v[128:131], v[168:171]\n"
      "global_load_lds_dwordx4 v18, s[24:25] sc0 sc1 nt\n"
      "v_mfma_f32_16x16x32_bf16 v[92:95], v[184:187], a[128:131], 0\n"
      "global_load_lds_dwordx4 v19, s[24:25] offset:1024 sc0 sc1 nt\n"
      "v_mfma_f32_16x16x32_bf16 v[92:95], v[68:71], a[132:135], v[92:95]\n"
      "global_load_lds_dwordx4 v20, s[24:25] offset:2048 sc0 sc1 nt\n"
      "v_mfma_f32_16x16x32_bf16 v[92:95], v[192:195], a[136:139], v[92:95]\n"
      "global_load_lds_dwordx4 v17, s[24:25] offset:3072 sc0 sc1 nt\n"
      "v_mfma_f32_16x16x32_bf16 v[92:95], v[164:167], a[140:143], v[92:95]\n"
      "v_cvt_pk_bf16_f32 v190, v168, v169\n"
      "v_cvt_pk_bf16_f32 v191, v170, v171\n"
      "v_mfma_f32_16x16x32_bf16 v[152:155], v[184:187], a[144:147], 0\n"
      "v_cndmask_b32_e32 v86, v86, v190, vcc\n"
      "v_cndmask_b32_e32 v87, v87, v191, vcc\n"
      "v_mfma_f32_16x16x32_bf16 v[152:155], v[68:71], a[148:151], v[152:155]\n"
      "v_cvt_pk_bf16_f32 v16, v92, v93\n"
      "v_cvt_pk_bf16_f32 v17, v94, v95\n"
      "v_mfma_f32_16x16x32_bf16 v[152:155], v[192:195], a[152:155], v[152:155]\n"
      "v_mov_b32 v228, v16\n"
      "v_pk_max_i16 v33, v33, 0\n"
      "v_mfma_f32_16x16x32_bf16 v[152:155], v[164:167], a[156:159], v[152:155]\n"
      "v_pk_max_i16 v35, v35, 0\n"
      "v_pk_max_i16 v12, v12, 0\n"
      "v_mfma_f32_16x16x32_bf16 v[168:171], v[76:79], v[180:183], v[56:59]\n"
      "v_pk_max_i16 v14, v14, 0\n"
      "v_pk_max_i16 v72, v72, 0\n"
      "v_mfma_f32_16x16x32_bf16 v[168:171], v[84:87], v[200:203], v[168:171]\n"
      "v_cvt_pk_bf16_f32 v92, v152, v153\n"
      "v_cvt_pk_bf16_f32 v93, v154, v155\n"
      "v_mfma_f32_16x16x32_bf16 v[152:155], v[148:151], v[180:183], 0\n"
      "v_mov_b32 v222, v92\n"
      "v_pk_max_i16 v74, v74, 0\n"
      "v_mfma_f32_16x16x32_bf16 v[152:155], v[132:135], v[200:203], v[152:155]\n"
      "v_pk_max_i16 v13, v13, 0\n"
      "v_pk_max_i16 v15, v15, 0\n"
      "v_mfma_f32_16x16x32_bf16 v[180:183], v[76:79], v[136:139], v[56:59]\n"
      "v_pk_max_i16 v73, v73, 0\n"
      "v_pk_max_i16 v75, v75, 0\n"
      "v_mfma_f32_16x16x32_bf16 v[180:183], v[84:87], v[144:147], v[180:183]\n"
      "v_max3_f32 v7, v168, v170, v171\n"
      "v_mfma_f32_16x16x32_bf16 v[200:203], v[148:151], v[136:139], 0\n"
      "v_max3_f32 v7, v7, v152, v153\n"
      "v_mfma_f32_16x16x32_bf16 v[200:203], v[132:135], v[144:147], v[200:203]\n"
      "v_max3_f32 v20, v180, v182, v183\n"
      "v_max3_f32 v7, v7, v154, v155\n"
      "v_mfma_f32_16x16x32_bf16 v[76:79], v[184:187], a[160:163], 0\n"
      "v_mfma_f32_16x16x32_bf16 v[76:79], v[68:71], a[164:167], v[76:79]\n"
      "v_max3_f32 v20, v20, v200, v201\n"
      "v_mfma_f32_16x16x32_bf16 v[76:79], v[192:195], a[168:171], v[76:79]\n"
      "v_max3_f32 v20, v20, v202, v203\n"
      "v_mfma_f32_16x16x32_bf16 v[76:79], v[164:167], a[172:175], v[76:79]\n"
      "s_nop 0\n"
      "v_permlane32_swap_b32 v7, v20\n"
      "v_mfma_f32_16x16x32_bf16 v[84:87], v[184:187], a[176:179], 0\n"
      "v_max_f32 v21, v7, v20\n"
      "v_max_f32 v132, v7, v20\n"
      "v_mfma_f32_16x16x32_bf16 v[84:87], v[68:71], a[180:183], v[84:87]\n"
      "v_cvt_pk_bf16_f32 v136, v76, v77\n"
      "v_cvt_pk_bf16_f32 v137, v78, v79\n"
      "v_mfma_f32_16x16x32_bf16 v[84:87], v[192:195], a[184:187], v[84:87]\n"
      "v_mov_b32 v216, v136\n"
      "v_mfma_f32_16x16x32_bf16 v[84:87], v[164:167], a[188:191], v[84:87]\n"
      "v_permlane16_swap_b32 v21, v132\n"
      "v_mfma_f32_16x16x32_bf16 v[144:147], v[184:187], a[192:195], 0\n"
      "v_max_f32 v21, v21, v132\n"
      "v_mfma_f32_16x16x32_bf16 v[144:147], v[68:71], a[196:199], v[144:147]\n"
      "v_cvt_pk_bf16_f32 v148, v84, v85\n"
      "v_cvt_pk_bf16_f32 v149, v86, v87\n"
      "v_mfma_f32_16x16x32_bf16 v[144:147], v[192:195], a[200:203], v[144:147]\n"
      "v_mov_b32 v210, v148\n"
      "v_mul_f32 v7, s32, v21\n"
      "v_mfma_f32_16x16x32_bf16 v[144:147], v[164:167], a[204:207], v[144:147]\n"
      "v_mul_f32 v20, s32, v21\n"
      "v_mfma_f32_16x16x32_bf16 v[76:79], v[184:187], a[208:211], 0\n"
      "s_nop 0\n"
      "v_permlane32_swap_b32 v7, v20\n"
      "v_mfma_f32_16x16x32_bf16 v[76:79], v[68:71], a[212:215], v[76:79]\n"
      "v_fma_f32 v168, v168, s32, -v7\n"
      "v_fma_f32 v170, v170, s32, -v7\n"
      "v_mfma_f32_16x16x32_bf16 v[76:79], v[192:195], a[216:219], v[76:79]\n"
      "v_fma_f32 v180, v180, s32, -v20\n"
      "v_fma_f32 v182, v182, s32, -v20\n"
      "v_mfma_f32_16x16x32_bf16 v[76:79], v[164:167], a[220:223], v[76:79]\n"
      ".Lpad3%=:\n"
      "s_cbranch_execz .Lret0%=\n"
      "v_exp_f32 v168, v168\n"
      "v_mfma_f32_16x16x32_bf16 v[36:39], v[124:127], v[228:231], v[36:39]\n"
      "v_exp_f32 v170, v170\n"
      "v_mfma_f32_16x16x32_bf16 v[44:47], v[124:127], v[222:225], v[44:47]\n"
      "v_fma_f32 v171, v171, s32, -v7\n"
      "v_fma_f32 v152, v152, s32, -v7\n"
      "v_mfma_f32_16x16x32_bf16 v[80:83], v[124:127], v[216:219], v[80:83]\n"
      "v_fma_f32 v153, v153, s32, -v7\n"
      "v_fma_f32 v183, v183, s32, -v20\n"
      "v_mfma_f32_16x16x32_bf16 v[60:63], v[124:127], v[210:213], v[60:63]\n"
      "v_cvt_pk_bf16_f32 v84, v36, v44\n"
      "v_cvt_pk_bf16_f32 v86, v37, v45\n"
      "v_mfma_f32_16x16x32_bf16 v[8:11], v[24:27], v[228:231], v[8:11]\n"
      "v_cvt_pk_bf16_f32 v132, v38, v46\n"
      "v_cvt_pk_bf16_f32 v134, v39, v47\n"
      "v_mfma_f32_16x16x32_bf16 v[64:67], v[24:27], v[222:225], v[64:67]\n"
      "v_cvt_pk_bf16_f32 v85, v80, v60\n"
      "v_cvt_pk_bf16_f32 v87, v81, v61\n"
      "v_mfma_f32_16x16x32_bf16 v[116:119], v[24:27], v[216:219], v[116:119]\n"
      "v_cvt_pk_bf16_f32 v133, v82, v62\n"
      "v_cvt_pk_bf16_f32 v135, v83, v63\n"
      "v_mfma_f32_16x16x32_bf16 v[120:123], v[24:27], v[210:213], v[120:123]\n"
      "v_cvt_pk_bf16_f32 v124, v8, v64\n"
      "v_cvt_pk_bf16_f32 v126, v9, v65\n"
      "v_mfma_f32_16x16x32_bf16 v[144:147], v[176:179], v[228:231], v[144:147]\n"
      "s_waitcnt vmcnt(12)\n"
      "ds_read_b128 v[36:39], v6 offset:12288\n"
      "v_cvt_pk_bf16_f32 v44, v10, v66\n"
      "v_mfma_f32_16x16x32_bf16 v[76:79], v[176:179], v[222:225], v[76:79]\n"
      "ds_read_b128 v[60:63], v1 offset:12288\n"
      "v_cvt_pk_bf16_f32 v46, v11, v67\n"
      "v_mfma_f32_16x16x32_bf16 v[80:83], v[176:179], v[216:219], 0\n"
      "ds_read_b128 v[24:27], v42 offset:12288\n"
      "v_cvt_pk_bf16_f32 v125, v116, v120\n"
      "v_mfma_f32_16x16x32_bf16 v[228:231], v[176:179], v[210:213], 0\n"
      "ds_read_b128 v[220:223], v43 offset:12288\n"
      "v_cvt_pk_bf16_f32 v127, v117, v121\n"
      "v_mfma_f32_16x16x32_bf16 v[80:83], v[184:187], a[224:227], v[80:83]\n"
      "v_cvt_pk_bf16_f32 v45, v118, v122\n"
      "v_cvt_pk_bf16_f32 v47, v119, v123\n"
      "v_mfma_f32_16x16x32_bf16 v[80:83], v[68:71], a[228:231], v[80:83]\n"
      "v_pk_max_i16 v84, v84, 0\n"
      "v_pk_max_i16 v86, v86, 0\n"
      "v_mfma_f32_16x16x32_bf16 v[80:83], v[192:195], a[232:235], v[80:83]\n"
      "v_pk_max_i16 v132, v132, 0\n"
      "v_pk_max_i16 v134, v134, 0\n"
      "v_mfma_f32_16x16x32_bf16 v[80:83], v[164:167], a[236:239], v[80:83]\n"
      "v_pk_max_i16 v85, v85, 0\n"
      "v_pk_max_i16 v87, v87, 0\n"
      "v_mfma_f32_16x16x32_bf16 v[228:231], v[184:187], a[240:243], v[228:231]\n"
      "v_pk_max_i16 v133, v133, 0\n"
      "v_pk_max_i16 v135, v135, 0\n"
      "v_mfma_f32_16x16x32_bf16 v[228:231], v[68:71], a[244:247], v[228:231]\n"
      "v_pk_max_i16 v124, v124, 0\n"
      "v_pk_max_i16 v126, v126, 0\n"
      "v_mfma_f32_16x16x32_bf16 v[228:231], v[192:195], a[248:251], v[228:231]\n"
      "v_pk_max_i16 v44, v44, 0\n"
      "v_pk_max_i16 v46, v46, 0\n"
      "v_mfma_f32_16x16x32_bf16 v[228:231], v[164:167], a[252:255], v[228:231]\n"
      "s_waitcnt lgkmcnt(0)\n"
      "v_pk_max_i16 v125, v125, 0\n"
      "v_pk_max_i16 v127, v127, 0\n"
      "v_mfma_f32_16x16x32_bf16 v[8:11], a[0:3], v[36:39], 0\n"
      "v_pk_max_i16 v45, v45, 0\n"
      "v_pk_max_i16 v47, v47, 0\n"
      "v_mfma_f32_16x16x32_bf16 v[8:11], a[4:7], v[60:63], v[8:11]\n"
      "v_exp_f32 v180, v180\n"
      "v_mfma_f32_16x16x32_bf16 v[8:11], a[8:11], v[24:27], v[8:11]\n"
      "v_exp_f32 v182, v182\n"
      "v_mfma_f32_16x16x32_bf16 v[8:11], a[12:15], v[220:223], v[8:11]\n"
      "v_fma_f32 v200, v200, s32, -v20\n"
      "v_fma_f32 v201, v201, s32, -v20\n"
      "v_mfma_f32_16x16x32_bf16 v[64:67], a[16:19], v[36:39], 0\n"
      "v_exp_f32 v171, v171\n"
      "v_mfma_f32_16x16x32_bf16 v[64:67], a[20:23], v[60:63], v[64:67]\n"
      "v_cvt_pk_bf16_f32 v68, v8, v9\n"
      "v_cvt_pk_bf16_f32 v69, v10, v11\n"
      "v_mfma_f32_16x16x32_bf16 v[64:67], a[24:27], v[24:27], v[64:67]\n"
      "v_exp_f32 v152, v152\n"
      "v_mfma_f32_16x16x32_bf16 v[64:67], a[28:31], v[220:223], v[64:67]\n"
      "v_exp_f32 v153, v153\n"
      "v_mfma_f32_16x16x32_bf16 v[116:119], a[32:35], v[36:39], 0\n"
      "v_fma_f32 v154, v154, s32, -v7\n"
      "v_fma_f32 v155, v155, s32, -v7\n"
      "v_mfma_f32_16x16x32_bf16 v[116:119], a[36:39], v[60:63], v[116:119]\n"
      "v_cvt_pk_bf16_f32 v70, v64, v65\n"
      "v_cvt_pk_bf16_f32 v71, v66, v67\n"
      "v_mfma_f32_16x16x32_bf16 v[116:119], a[40:43], v[24:27], v[116:119]\n"
      "v_add_f32 v21, v168, v170\n"
      "v_fma_f32 v202, v202, s32, -v20\n"
      "v_mfma_f32_16x16x32_bf16 v[116:119], a[44:47], v[220:223], v[116:119]\n"
      "v_exp_f32 v183, v183\n"
      "v_mfma_f32_16x16x32_bf16 v[8:11], a[48:51], v[36:39], 0\n"
      "v_exp_f32 v200, v200\n"
      "v_mfma_f32_16x16x32_bf16 v[8:11], a[52:55], v[60:63], v[8:11]\n"
      "v_cvt_pk_bf16_f32 v120, v116, v117\n"
      "v_cvt_pk_bf16_f32 v121, v118, v119\n"
      "v_mfma_f32_16x16x32_bf16 v[8:11], a[56:59], v[24:27], v[8:11]\n"
      "v_exp_f32 v201, v201\n"
      "v_mfma_f32_16x16x32_bf16 v[8:11], a[60:63], v[220:223], v[8:11]\n"
      "v_fma_f32 v203, v203, s32, -v20\n"
      "v_add_f32 v7, v180, v182\n"
      "v_mfma_f32_16x16x32_bf16 v[64:67], a[64:67], v[36:39], 0\n"
      "v_exp_f32 v154, v154\n"
      "v_mfma_f32_16x16x32_bf16 v[64:67], a[68:71], v[60:63], v[64:67]\n"
      "v_cvt_pk_bf16_f32 v122, v8, v9\n"
      "v_cvt_pk_bf16_f32 v123, v10, v11\n"
      "v_mfma_f32_16x16x32_bf16 v[64:67], a[72:75], v[24:27], v[64:67]\n"
      "v_exp_f32 v155, v155\n"
      "v_mfma_f32_16x16x32_bf16 v[64:67], a[76:79], v[220:223], v[64:67]\n"
      "v_add_f32 v116, v171, v152\n"
      "v_add_f32 v21, v21, v153\n"
      "v_mfma_f32_16x16x32_bf16 v[164:167], a[80:83], v[36:39], 0\n"
      "v_exp_f32 v202, v202\n"
      "v_mfma_f32_16x16x32_bf16 v[164:167], a[84:87], v[60:63], v[164:167]\n"
      "v_cvt_pk_bf16_f32 v176, v64, v65\n"
      "v_cvt_pk_bf16_f32 v177, v66, v67\n"
      "v_mfma_f32_16x16x32_bf16 v[164:167], a[88:91], v[24:27], v[164:167]\n"
      "v_exp_f32 v203, v203\n"
      "v_mfma_f32_16x16x32_bf16 v[164:167], a[92:95], v[220:223], v[164:167]\n"
      "v_add_f32 v20, v183, v200\n"
      "v_add_f32 v7, v7, v201\n"
      "v_mfma_f32_16x16x32_bf16 v[8:11], a[96:99], v[36:39], 0\n"
      "v_add_f32 v116, v116, v154\n"
      "v_add_f32 v21, v21, v155\n"
      "v_mfma_f32_16x16x32_bf16 v[8:11], a[100:103], v[60:63], v[8:11]\n"
      "v_cvt_pk_bf16_f32 v178, v164, v165\n"
      "v_cvt_pk_bf16_f32 v179, v166, v167\n"
      "v_mfma_f32_16x16x32_bf16 v[8:11], a[104:107], v[24:27], v[8:11]\n"
      "v_add_f32 v20, v20, v202\n"
      "v_add_f32 v7, v7, v203\n"
      "v_mfma_f32_16x16x32_bf16 v[8:11], a[108:111], v[220:223], v[8:11]\n"
      "v_add_f32 v21, v21, v116\n"
      "v_add_f32 v7, v7, v20\n"
      "v_mfma_f32_16x16x32_bf16 v[64:67], a[112:115], v[36:39], 0\n"
      "s_nop 0\n"
      "v_permlane32_swap_b32 v21, v7\n"
      "v_mfma_f32_16x16x32_bf16 v[64:67], a[116:119], v[60:63], v[64:67]\n"
      "v_cvt_pk_bf16_f32 v184, v8, v9\n"
      "v_cvt_pk_bf16_f32 v185, v10, v11\n"
      "v_mfma_f32_16x16x32_bf16 v[64:67], a[120:123], v[24:27], v[64:67]\n"
      "v_add_f32 v116, v21, v7\n"
      "v_add_f32 v20, v21, v7\n"
      "v_mfma_f32_16x16x32_bf16 v[64:67], a[124:127], v[220:223], v[64:67]\n"
      "s_nop 0\n"
      "v_permlane16_swap_b32 v116, v20\n"
      "v_mfma_f32_16x16x32_bf16 v[164:167], v[96:99], a[128:131], 0\n"
      "v_add_f32 v116, v116, v20\n"
      "v_mfma_f32_16x16x32_bf16 v[164:167], v[108:111], a[132:135], v[164:167]\n"
      "v_cvt_pk_bf16_f32 v186, v64, v65\n"
      "v_cvt_pk_bf16_f32 v187, v66, v67\n"
      "v_mfma_f32_16x16x32_bf16 v[164:167], v[104:107], a[136:139], v[164:167]\n"
      "v_rcp_f32 v116, v116\n"
      "v_mfma_f32_16x16x32_bf16 v[164:167], v[112:115], a[140:143], v[164:167]\n"
      "v_cndmask_b32_e64 v21, 0, v116, s[46:47]\n"
      "v_cndmask_b32_e64 v7, 0, v116, s[46:47]\n"
      "v_mfma_f32_16x16x32_bf16 v[8:11], v[96:99], a[144:147], 0\n"
      "s_nop 0\n"
      "v_permlane32_swap_b32 v21, v7\n"
      "v_mfma_f32_16x16x32_bf16 v[8:11], v[108:111], a[148:151], v[8:11]\n"
      "v_cvt_pk_bf16_f32 v18, v164, v165\n"
      "v_cvt_pk_bf16_f32 v19, v166, v167\n"
      "v_mfma_f32_16x16x32_bf16 v[8:11], v[104:107], a[152:155], v[8:11]\n"
      "v_mul_f32 v168, v168, v21\n"
      "v_mul_f32 v170, v170, v21\n"
      "v_mfma_f32_16x16x32_bf16 v[8:11], v[112:115], a[156:159], v[8:11]\n"
      "v_mul_f32 v171, v171, v21\n"
      "v_mul_f32 v152, v152, v21\n"
      "v_mfma_f32_16x16x32_bf16 v[64:67], v[100:103], v[88:91], v[52:55]\n"
      "v_mul_f32 v153, v153, v21\n"
      "v_mul_f32 v154, v154, v21\n"
      "v_mfma_f32_16x16x32_bf16 v[64:67], v[188:191], v[172:175], v[64:67]\n"
      "v_cvt_pk_bf16_f32 v94, v8, v9\n"
      "v_cvt_pk_bf16_f32 v95, v10, v11\n"
      "v_mfma_f32_16x16x32_bf16 v[116:119], v[176:179], v[88:91], 0\n"
      "v_mul_f32 v155, v155, v21\n"
      "v_cvt_pk_bf16_f32 v164, v168, 0\n"
      "v_mfma_f32_16x16x32_bf16 v[116:119], v[184:187], v[172:175], v[116:119]\n"
      "v_cvt_pk_bf16_f32 v165, v170, v171\n"
      "v_cvt_pk_bf16_f32 v166, v152, v153\n"
      "v_mfma_f32_16x16x32_bf16 v[192:195], v[100:103], v[68:71], v[52:55]\n"
      "s_add_u32 s33, s20, 0\n"
      "v_cvt_pk_bf16_f32 v167, v154, v155\n"
      "v_cndmask_b32_e32 v208, 0, v164, vcc\n"
      "v_mfma_f32_16x16x32_bf16 v[192:195], v[188:191], v[120:123], v[192:195]\n"
      "s_add_u32 s34, s20, 4096\n"
      "v_cndmask_b32_e32 v209, 0, v165, vcc\n"
      "v_cndmask_b32_e32 v210, 0, v166, vcc\n"
      "v_mfma_f32_16x16x32_bf16 v[8:11], v[176:179], v[68:71], 0\n"
      "s_add_u32 s35, s20, 8192\n"
      "v_cndmask_b32_e32 v211, 0, v167, vcc\n"
      "v_cndmask_b32_e64 v164, v164, 0, vcc\n"
      "v_mfma_f32_16x16x32_bf16 v[8:11], v[184:187], v[120:123], v[8:11]\n"
      "v_cndmask_b32_e64 v165, v165, 0, vcc\n"
      "v_cndmask_b32_e64 v166, v166, 0, vcc\n"
      "v_mfma_f32_16x16x32_bf16 v[88:91], v[96:99], a[160:163], 0\n"
      "buffer_store_dwordx2 v[2:3], v0, s[28:31], s33 offen sc1\n"
      "v_mfma_f32_16x16x32_bf16 v[88:91], v[108:111], a[164:167], v[88:91]\n"
      "v_cndmask_b32_e64 v167, v167, 0, vcc\n"
      "v_mul_f32 v180, v180, v7\n"
      "v_mfma_f32_16x16x32_bf16 v[88:91], v[104:107], a[168:171], v[88:91]\n"
      "v_mul_f32 v182, v182, v7\n"
      "v_mul_f32 v183, v183, v7\n"
      "v_mfma_f32_16x16x32_bf16 v[88:91], v[112:115], a[172:175], v[88:91]\n"
      "v_mul_f32 v200, v200, v7\n"
      "v_mul_f32 v201, v201, v7\n"
      "v_mfma_f32_16x16x32_bf16 v[100:103], v[96:99], a[176:179], 0\n"
      "v_mul_f32 v202, v202, v7\n"
      "v_mul_f32 v203, v203, v7\n"
      "v_mfma_f32_16x16x32_bf16 v[100:103], v[108:111], a[180:183], v[100:103]\n"
      "buffer_store_dwordx2 v[4:5], v0, s[28:31], s33 offen offset:1024 sc1\n"
      "v_mfma_f32_16x16x32_bf16 v[100:103], v[104:107], a[184:187], v[100:103]\n"
      "v_cvt_pk_bf16_f32 v138, v88, v89\n"
      "v_cvt_pk_bf16_f32 v139, v90, v91\n"
      "v_mfma_f32_16x16x32_bf16 v[100:103], v[112:115], a[188:191], v[100:103]\n"
      "v_cvt_pk_bf16_f32 v152, v180, 0\n"
      "v_cvt_pk_bf16_f32 v153, v182, v183\n"
      "v_mfma_f32_16x16x32_bf16 v[168:171], v[96:99], a[192:195], 0\n"
      "v_cvt_pk_bf16_f32 v154, v200, v201\n"
      "v_cvt_pk_bf16_f32 v155, v202, v203\n"
      "v_mfma_f32_16x16x32_bf16 v[168:171], v[108:111], a[196:199], v[168:171]\n"
      "v_cvt_pk_bf16_f32 v150, v100, v101\n"
      "v_cvt_pk_bf16_f32 v151, v102, v103\n"
      "v_mfma_f32_16x16x32_bf16 v[168:171], v[104:107], a[200:203], v[168:171]\n"
      "buffer_store_dwordx2 v[22:23], v0, s[28:31], s33 offen offset:2048 sc1\n"
      "v_mfma_f32_16x16x32_bf16 v[168:171], v[112:115], a[204:207], v[168:171]\n"
      "v_max3_f32 v2, v64, v65, v66\n"
      "v_max3_f32 v3, v192, v193, v194\n"
      "v_mfma_f32_16x16x32_bf16 v[88:91], v[96:99], a[208:211], 0\n"
      "s_waitcnt vmcnt(11)\n"
      "ds_read_b128 v[172:175], v6 offset:16384\n"
      "v_max3_f32 v2, v2, v67, v116\n"
      "v_mfma_f32_16x16x32_bf16 v[88:91], v[108:111], a[212:215], v[88:91]\n"
      "ds_read_b128 v[180:183], v1 offset:16384\n"
      "v_max3_f32 v3, v3, v195, v8\n"
      "v_mfma_f32_16x16x32_bf16 v[88:91], v[104:107], a[216:219], v[88:91]\n"
      "ds_read_b128 v[20:23], v42 offset:16384\n"
      "v_max3_f32 v2, v2, v117, v118\n"
      "v_mfma_f32_16x16x32_bf16 v[88:91], v[112:115], a[220:223], v[88:91]\n"
      "buffer_store_dwordx2 v[40:41], v0, s[28:31], s33 offen offset:3072 sc1\n"
      "v_mfma_f32_16x16x32_bf16 v[100:103], v[96:99], a[224:227], 0\n"
      "s_add_u32 s33, s20, 12288\n"
      "ds_read_b128 v[188:191], v43 offset:16384\n"
      "v_max3_f32 v3, v3, v9, v10\n"
      "v_mfma_f32_16x16x32_bf16 v[100:103], v[108:111], a[228:231], v[100:103]\n"
      "s_nop 0\n"
      "v_permlane32_swap_b32 v2, v3\n"
      "v_mfma_f32_16x16x32_bf16 v[100:103], v[104:107], a[232:235], v[100:103]\n"
      "v_max_f32 v4, v2, v3\n"
      "v_max_f32 v5, v2, v3\n"
      "v_mfma_f32_16x16x32_bf16 v[100:103], v[112:115], a[236:239], v[100:103]\n"
      "s_nop 0\n"
      "v_permlane16_swap_b32 v4, v5\n"
      "v_mfma_f32_16x16x32_bf16 v[200:203], v[96:99], a[240:243], 0\n"
      "buffer_store_dwordx2 v[196:197], v0, s[28:31], s34 offen sc1\n"
      "v_mfma_f32_16x16x32_bf16 v[200:203], v[108:111], a[244:247], v[200:203]\n"
      "v_max_f32 v4, v4, v5\n"
      "v_mfma_f32_16x16x32_bf16 v[200:203], v[104:107], a[248:251], v[200:203]\n"
      "v_mul_f32 v2, s32, v4\n"
      "v_mul_f32 v3, s32, v4\n"
      "v_mfma_f32_16x16x32_bf16 v[200:203], v[112:115], a[252:255], v[200:203]\n"
      "s_waitcnt lgkmcnt(0)\n"
      "v_permlane32_swap_b32 v2, v3\n"
      "v_mfma_f32_16x16x32_bf16 v[212:215], a[0:3], v[172:175], 0\n"
      "v_fma_f32 v64, v64, s32, -v2\n"
      "v_fma_f32 v65, v65, s32, -v2\n"
      "v_mfma_f32_16x16x32_bf16 v[212:215], a[4:7], v[180:183], v[212:215]\n"
      "buffer_store_dwordx2 v[198:199], v0, s[28:31], s34 offen offset:1024 sc1\n"
      "v_mfma_f32_16x16x32_bf16 v[212:215], a[8:11], v[20:23], v[212:215]\n"
      "v_fma_f32 v192, v192, s32, -v3\n"
      "v_fma_f32 v193, v193, s32, -v3\n"
      "v_mfma_f32_16x16x32_bf16 v[212:215], a[12:15], v[188:191], v[212:215]\n"
      "v_exp_f32 v64, v64\n"
      "v_mfma_f32_16x16x32_bf16 v[96:99], a[16:19], v[172:175], 0\n"
      "v_exp_f32 v65, v65\n"
      "v_mfma_f32_16x16x32_bf16 v[96:99], a[20:23], v[180:183], v[96:99]\n"
      "v_cvt_pk_bf16_f32 v104, v212, v213\n"
      "v_cvt_pk_bf16_f32 v105, v214, v215\n"
      "v_mfma_f32_16x16x32_bf16 v[96:99], a[24:27], v[20:23], v[96:99]\n"
      "buffer_store_dwordx2 v[204:205], v0, s[28:31], s34 offen offset:2048 sc1\n"
      "v_mfma_f32_16x16x32_bf16 v[96:99], a[28:31], v[188:191], v[96:99]\n"
      "v_fma_f32 v66, v66, s32, -v2\n"
      "v_fma_f32 v67, v67, s32, -v2\n"
      "v_mfma_f32_16x16x32_bf16 v[108:111], a[32:35], v[172:175], 0\n"
      "v_fma_f32 v116, v116, s32, -v2\n"
      "v_fma_f32 v194, v194, s32, -v3\n"
      "v_mfma_f32_16x16x32_bf16 v[108:111], a[36:39], v[180:183], v[108:111]\n"
      "v_cvt_pk_bf16_f32 v106, v96, v97\n"
      "v_cvt_pk_bf16_f32 v107, v98, v99\n"
      "v_mfma_f32_16x16x32_bf16 v[108:111], a[40:43], v[20:23], v[108:111]\n"
      "v_exp_f32 v192, v192\n"
      "v_mfma_f32_16x16x32_bf16 v[108:111], a[44:47], v[188:191], v[108:111]\n"
      "buffer_store_dwordx2 v[206:207], v0, s[28:31], s34 offen offset:3072 sc1\n"
      "s_add_u32 s34, s20, 16384\n"
      "v_mfma_f32_16x16x32_bf16 v[112:115], a[48:51], v[172:175], 0\n"
      "v_exp_f32 v193, v193\n"
      "v_mfma_f32_16x16x32_bf16 v[112:115], a[52:55], v[180:183], v[112:115]\n"
      "v_cvt_pk_bf16_f32 v196, v108, v109\n"
      "v_cvt_pk_bf16_f32 v197, v110, v111\n"
      "v_mfma_f32_16x16x32_bf16 v[112:115], a[56:59], v[20:23], v[112:115]\n"
      "v_fma_f32 v195, v195, s32, -v3\n"
      "v_fma_f32 v8, v8, s32, -v3\n"
      "v_mfma_f32_16x16x32_bf16 v[112:115], a[60:63], v[188:191], v[112:115]\n"
      "v_exp_f32 v66, v66\n"
      "v_mfma_f32_16x16x32_bf16 v[96:99], a[64:67], v[172:175], 0\n"
      "buffer_store_dwordx2 v[28:29], v0, s[28:31], s35 offen sc1\n"
      "v_mfma_f32_16x16x32_bf16 v[96:99], a[68:71], v[180:183], v[96:99]\n"
      "v_cvt_pk_bf16_f32 v198, v112, v113\n"
      "v_cvt_pk_bf16_f32 v199, v114, v115\n"
      "v_mfma_f32_16x16x32_bf16 v[96:99], a[72:75], v[20:23], v[96:99]\n"
      "v_exp_f32 v67, v67\n"
      "v_mfma_f32_16x16x32_bf16 v[96:99], a[76:79], v[188:191], v[96:99]\n"
      "v_exp_f32 v116, v116\n"
      "v_mfma_f32_16x16x32_bf16 v[108:111], a[80:83], v[172:175], 0\n"
      "s_waitcnt vmcnt(13)\n"
      "ds_read_b128 v[204:207], v6 offset:20480\n"
      "v_fma_f32 v117, v117, s32, -v2\n"
      "v_mfma_f32_16x16x32_bf16 v[108:111], a[84:87], v[180:183], v[108:111]\n"
      "buffer_store_dwordx2 v[30:31], v0, s[28:31], s35 offen offset:1024 sc1\n"
      "v_mfma_f32_16x16x32_bf16 v[108:111], a[88:91], v[20:23], v[108:111]\n"
      "ds_read_b128 v[112:115], v1 offset:20480\n"
      "v_cvt_pk_bf16_f32 v212, v96, v97\n"
      "v_mfma_f32_16x16x32_bf16 v[108:111], a[92:95], v[188:191], v[108:111]\n"
      "ds_read_b128 v[216:219], v42 offset:20480\n"
      "v_cvt_pk_bf16_f32 v213, v98, v99\n"
      "v_mfma_f32_16x16x32_bf16 v[224:227], a[96:99], v[172:175], 0\n"
      "ds_read_b128 v[28:31], v43 offset:20480\n"
      "v_fma_f32 v118, v118, s32, -v2\n"
      "v_mfma_f32_16x16x32_bf16 v[224:227], a[100:103], v[180:183], v[224:227]\n"
      "v_cvt_pk_bf16_f32 v214, v108, v109\n"
      "v_cvt_pk_bf16_f32 v215, v110, v111\n"
      "v_mfma_f32_16x16x32_bf16 v[224:227], a[104:107], v[20:23], v[224:227]\n"
      "buffer_store_dwordx2 v[32:33], v0, s[28:31], s35 offen offset:2048 sc1\n"
      "v_mfma_f32_16x16x32_bf16 v[224:227], a[108:111], v[188:191], v[224:227]\n"
      "v_add_f32 v4, v64, v65\n"
      "v_fma_f32 v9, v9, s32, -v3\n"
      "v_mfma_f32_16x16x32_bf16 v[96:99], a[112:115], v[172:175], 0\n"
      "v_exp_f32 v194, v194\n"
      "v_mfma_f32_16x16x32_bf16 v[96:99], a[116:119], v[180:183], v[96:99]\n"
      "v_cvt_pk_bf16_f32 v108, v224, v225\n"
      "v_cvt_pk_bf16_f32 v109, v226, v227\n"
      "v_mfma_f32_16x16x32_bf16 v[96:99], a[120:123], v[20:23], v[96:99]\n"
      "v_exp_f32 v195, v195\n"
      "v_mfma_f32_16x16x32_bf16 v[96:99], a[124:127], v[188:191], v[96:99]\n"
      "buffer_store_dwordx2 v[34:35], v0, s[28:31], s35 offen offset:3072 sc1\n"
      "s_waitcnt lgkmcnt(0)\n"
      "s_add_u32 s35, s20, 20480\n"
      "v_mfma_f32_16x16x32_bf16 v[224:227], a[0:3], v[204:207], 0\n"
      "v_exp_f32 v8, v8\n"
      "v_mfma_f32_16x16x32_bf16 v[224:227], a[4:7], v[112:115], v[224:227]\n"
      "v_cvt_pk_bf16_f32 v110, v96, v97\n"
      "v_cvt_pk_bf16_f32 v111, v98, v99\n"
      "v_mfma_f32_16x16x32_bf16 v[224:227], a[8:11], v[216:219], v[224:227]\n"
      "v_fma_f32 v10, v10, s32, -v3\n"
      "v_add_f32 v2, v192, v193\n"
      "v_mfma_f32_16x16x32_bf16 v[224:227], a[12:15], v[28:31], v[224:227]\n"
      "v_exp_f32 v117, v117\n"
      "v_mfma_f32_16x16x32_bf16 v[32:35], a[16:19], v[204:207], 0\n"
      "buffer_store_dwordx2 v[12:13], v0, s[28:31], s33 offen sc1\n"
      "v_mfma_f32_16x16x32_bf16 v[32:35], a[20:23], v[112:115], v[32:35]\n"
      "v_cvt_pk_bf16_f32 v96, v224, v225\n"
      "v_cvt_pk_bf16_f32 v97, v226, v227\n"
      "v_mfma_f32_16x16x32_bf16 v[32:35], a[24:27], v[216:219], v[32:35]\n"
      "v_cndmask_b32_e32 v68, v68, v96, vcc\n"
      "v_cndmask_b32_e32 v69, v69, v97, vcc\n"
      "v_mfma_f32_16x16x32_bf16 v[32:35], a[28:31], v[28:31], v[32:35]\n"
      "v_exp_f32 v118, v118\n"
      "v_mfma_f32_16x16x32_bf16 v[224:227], a[32:35], v[204:207], 0\n"
      "v_add_f32 v3, v66, v67\n"
      "v_add_f32 v4, v4, v116\n"
      "v_mfma_f32_16x16x32_bf16 v[224:227], a[36:39], v[112:115], v[224:227]\n"
      "buffer_store_dwordx2 v[14:15], v0, s[28:31], s33 offen offset:1024 sc1\n"
      "v_mfma_f32_16x16x32_bf16 v[224:227], a[40:43], v[216:219], v[224:227]\n"
      "v_cvt_pk_bf16_f32 v98, v32, v33\n"
      "v_cvt_pk_bf16_f32 v99, v34, v35\n"
      "v_mfma_f32_16x16x32_bf16 v[224:227], a[44:47], v[28:31], v[224:227]\n"
      "v_cndmask_b32_e32 v70, v70, v98, vcc\n"
      "v_cndmask_b32_e32 v71, v71, v99, vcc\n"
      "v_mfma_f32_16x16x32_bf16 v[12:15], a[48:51], v[204:207], 0\n"
      "v_exp_f32 v9, v9\n"
      "v_mfma_f32_16x16x32_bf16 v[12:15], a[52:55], v[112:115], v[12:15]\n"
      "v_cvt_pk_bf16_f32 v32, v224, v225\n"
      "v_cvt_pk_bf16_f32 v33, v226, v227\n"
      "v_mfma_f32_16x16x32_bf16 v[12:15], a[56:59], v[216:219], v[12:15]\n"
      "buffer_store_dwordx2 v[72:73], v0, s[28:31], s33 offen offset:2048 sc1\n"
      "v_mfma_f32_16x16x32_bf16 v[12:15], a[60:63], v[28:31], v[12:15]\n"
      "v_cndmask_b32_e32 v120, v120, v32, vcc\n"
      "v_cndmask_b32_e32 v121, v121, v33, vcc\n"
      "v_mfma_f32_16x16x32_bf16 v[224:227], a[64:67], v[204:207], 0\n"
      "v_exp_f32 v10, v10\n"
      "v_mfma_f32_16x16x32_bf16 v[224:227], a[68:71], v[112:115], v[224:227]\n"
      "v_cvt_pk_bf16_f32 v34, v12, v13\n"
      "v_cvt_pk_bf16_f32 v35, v14, v15\n"
      "v_mfma_f32_16x16x32_bf16 v[224:227], a[72:75], v[216:219], v[224:227]\n"
      "v_cndmask_b32_e32 v122, v122, v34, vcc\n"
      "v_cndmask_b32_e32 v123, v123, v35, vcc\n"
      "v_mfma_f32_16x16x32_bf16 v[224:227], a[76:79], v[28:31], v[224:227]\n"
      "buffer_store_dwordx2 v[74:75], v0, s[28:31], s33 offen offset:3072 sc1\n"
      "v_mfma_f32_16x16x32_bf16 v[12:15], a[80:83], v[204:207], 0\n"
      "v_add_f32 v5, v194, v195\n"
      "v_add_f32 v2, v2, v8\n"
      "v_mfma_f32_16x16x32_bf16 v[12:15], a[84:87], v[112:115], v[12:15]\n"
      "v_cvt_pk_bf16_f32 v72, v224, v225\n"
      "v_cvt_pk_bf16_f32 v73, v226, v227\n"
      "v_mfma_f32_16x16x32_bf16 v[12:15], a[88:91], v[216:219], v[12:15]\n"
      "v_cndmask_b32_e32 v176, v176, v72, vcc\n"
      "v_cndmask_b32_e32 v177, v177, v73, vcc\n"
      "v_mfma_f32_16x16x32_bf16 v[12:15], a[92:95], v[28:31], v[12:15]\n"
      "v_add_f32 v3, v3, v117\n"
      "v_add_f32 v4, v4, v118\n"
      "v_mfma_f32_16x16x32_bf16 v[224:227], a[96:99], v[204:207], 0\n"
      "buffer_store_dwordx2 v[84:85], v0, s[28:31], s34 offen sc1\n"
      "v_mfma_f32_16x16x32_bf16 v[224:227], a[100:103], v[112:115], v[224:227]\n"
      "v_cvt_pk_bf16_f32 v74, v12, v13\n"
      "v_cvt_pk_bf16_f32 v75, v14, v15\n"
      "v_mfma_f32_16x16x32_bf16 v[224:227], a[104:107], v[216:219], v[224:227]\n"
      "v_cndmask_b32_e32 v178, v178, v74, vcc\n"
      "v_cndmask_b32_e32 v179, v179, v75, vcc\n"
      "v_mfma_f32_16x16x32_bf16 v[224:227], a[108:111], v[28:31], v[224:227]\n"
      "v_add_f32 v5, v5, v9\n"
      "v_add_f32 v2, v2, v10\n"
      "v_mfma_f32_16x16x32_bf16 v[12:15], a[112:115], v[204:207], 0\n"
      "v_add_f32 v4, v4, v3\n"
      "v_add_f32 v2, v2, v5\n"
      "v_mfma_f32_16x16x32_bf16 v[12:15], a[116:119], v[112:115], v[12:15]\n"
      "buffer_store_dwordx2 v[86:87], v0, s[28:31], s34 offen offset:1024 sc1\n"
      "v_mfma_f32_16x16x32_bf16 v[12:15], a[120:123], v[216:219], v[12:15]\n"
      "v_cvt_pk_bf16_f32 v84, v224, v225\n"
      "v_cvt_pk_bf16_f32 v85, v226, v227\n"
      "v_mfma_f32_16x16x32_bf16 v[12:15], a[124:127], v[28:31], v[12:15]\n"
      "v_cndmask_b32_e32 v184, v184, v84, vcc\n"
      "v_cndmask_b32_e32 v185, v185, v85, vcc\n"
      "v_mfma_f32_16x16x32_bf16 v[224:227], v[140:143], a[128:131], 0\n"
      "v_permlane32_swap_b32 v4, v2\n"
      "v_mfma_f32_16x16x32_bf16 v[224:227], v[156:159], a[132:135], v[224:227]\n"
      "v_cvt_pk_bf16_f32 v86, v12, v13\n"
      "v_cvt_pk_bf16_f32 v87, v14, v15\n"
      "v_mfma_f32_16x16x32_bf16 v[224:227], v[160:163], a[136:139], v[224:227]\n"
      "buffer_store_dwordx2 v[132:133], v0, s[28:31], s34 offen offset:2048 sc1\n"
      "v_mfma_f32_16x16x32_bf16 v[224:227], v[128:131], a[140:143], v[224:227]\n"
      "v_cndmask_b32_e32 v186, v186, v86, vcc\n"
      "v_cndmask_b32_e32 v187, v187, v87, vcc\n"
      "v_mfma_f32_16x16x32_bf16 v[12:15], v[140:143], a[144:147], 0\n"
      "v_add_f32 v3, v4, v2\n"
      "v_add_f32 v5, v4, v2\n"
      "v_mfma_f32_16x16x32_bf16 v[12:15], v[156:159], a[148:151], v[12:15]\n"
      "v_cvt_pk_bf16_f32 v250, v224, v225\n"
      "v_cvt_pk_bf16_f32 v251, v226, v227\n"
      "v_mfma_f32_16x16x32_bf16 v[12:15], v[160:163], a[152:155], v[12:15]\n"
      "v_mov_b32 v16, v250\n"
      "v_mfma_f32_16x16x32_bf16 v[12:15], v[128:131], a[156:159], v[12:15]\n"
      "buffer_store_dwordx2 v[134:135], v0, s[28:31], s34 offen offset:3072 sc1\n"
      "v_mfma_f32_16x16x32_bf16 v[224:227], v[176:179], v[68:71], v[50:53]\n"
      "v_permlane16_swap_b32 v3, v5\n"
      "v_mfma_f32_16x16x32_bf16 v[224:227], v[184:187], v[120:123], v[224:227]\n"
      "v_cvt_pk_bf16_f32 v244, v12, v13\n"
      "v_cvt_pk_bf16_f32 v245, v14, v15\n"
      "v_mfma_f32_16x16x32_bf16 v[132:135], v[212:215], v[68:71], 0\n"
      "v_mov_b32 v92, v244\n"
      "v_add_f32 v3, v3, v5\n"
      "v_mfma_f32_16x16x32_bf16 v[132:135], v[108:111], v[120:123], v[132:135]\n"
      "v_rcp_f32 v3, v3\n"
      "v_mfma_f32_16x16x32_bf16 v[12:15], v[176:179], v[104:107], v[50:53]\n"
      "buffer_store_dwordx2 v[124:125], v0, s[28:31], s35 offen sc1\n"
      "v_mfma_f32_16x16x32_bf16 v[12:15], v[184:187], v[196:199], v[12:15]\n"
      "v_cndmask_b32_e64 v4, 0, v3, s[48:49]\n"
      "v_cndmask_b32_e64 v2, 0, v3, s[48:49]\n"
      "v_mfma_f32_16x16x32_bf16 v[68:71], v[212:215], v[104:107], 0\n"
      "s_nop 0\n"
      "v_permlane32_swap_b32 v4, v2\n"
      "v_mfma_f32_16x16x32_bf16 v[68:71], v[108:111], v[196:199], v[68:71]\n"
      "v_mul_f32 v64, v64, v4\n"
      "v_mul_f32 v65, v65, v4\n"
      "v_mfma_f32_16x16x32_bf16 v[120:123], v[140:143], a[160:163], 0\n"
      "v_mul_f32 v66, v66, v4\n"
      "v_mul_f32 v67, v67, v4\n"
      "v_mfma_f32_16x16x32_bf16 v[120:123], v[156:159], a[164:167], v[120:123]\n"
      "buffer_store_dwordx2 v[126:127], v0, s[28:31], s35 offen offset:1024 sc1\n"
      "v_mfma_f32_16x16x32_bf16 v[120:123], v[160:163], a[168:171], v[120:123]\n"
      "v_mul_f32 v116, v116, v4\n"
      "v_mul_f32 v117, v117, v4\n"
      "v_mfma_f32_16x16x32_bf16 v[120:123], v[128:131], a[172:175], v[120:123]\n"
      "v_mul_f32 v118, v118, v4\n"
      "v_cvt_pk_bf16_f32 v104, v64, v65\n"
      "v_mfma_f32_16x16x32_bf16 v[108:111], v[140:143], a[176:179], 0\n"
      "v_cvt_pk_bf16_f32 v105, v66, v67\n"
      "v_cvt_pk_bf16_f32 v106, v116, v117\n"
      "v_mfma_f32_16x16x32_bf16 v[108:111], v[156:159], a[180:183], v[108:111]\n"
      "v_cvt_pk_bf16_f32 v238, v120, v121\n"
      "v_cvt_pk_bf16_f32 v239, v122, v123\n"
      "v_mfma_f32_16x16x32_bf16 v[108:111], v[160:163], a[184:187], v[108:111]\n"
      "buffer_store_dwordx2 v[44:45], v0, s[28:31], s35 offen offset:2048 sc1\n"
      "v_mfma_f32_16x16x32_bf16 v[108:111], v[128:131], a[188:191], v[108:111]\n"
      "v_mov_b32 v136, v238\n"
      "v_cvt_pk_bf16_f32 v107, v118, 0\n"
      "v_mfma_f32_16x16x32_bf16 v[124:127], v[140:143], a[192:195], 0\n"
      "v_mul_f32 v192, v192, v2\n"
      "v_mul_f32 v193, v193, v2\n"
      "v_mfma_f32_16x16x32_bf16 v[124:127], v[156:159], a[196:199], v[124:127]\n"
      "v_cvt_pk_bf16_f32 v232, v108, v109\n"
      "v_cvt_pk_bf16_f32 v233, v110, v111\n"
      "v_mfma_f32_16x16x32_bf16 v[124:127], v[160:163], a[200:203], v[124:127]\n"
      "v_mov_b32 v148, v232\n"
      "v_mul_f32 v194, v194, v2\n"
      "v_mfma_f32_16x16x32_bf16 v[124:127], v[128:131], a[204:207], v[124:127]\n"
      "buffer_store_dwordx2 v[46:47], v0, s[28:31], s35 offen offset:3072 sc1\n"
      "v_mfma_f32_16x16x32_bf16 v[64:67], v[140:143], a[208:211], 0\n"
      "v_mul_f32 v195, v195, v2\n"
      "v_mul_f32 v8, v8, v2\n"
      "v_mfma_f32_16x16x32_bf16 v[64:67], v[156:159], a[212:215], v[64:67]\n"
      "v_mul_f32 v9, v9, v2\n"
      "v_mul_f32 v10, v10, v2\n"
      "v_mfma_f32_16x16x32_bf16 v[64:67], v[160:163], a[216:219], v[64:67]\n"
      "v_cvt_pk_bf16_f32 v108, v192, v193\n"
      "v_cvt_pk_bf16_f32 v109, v194, v195\n"
      "v_mfma_f32_16x16x32_bf16 v[64:67], v[128:131], a[220:223], v[64:67]\n"
      "v_cvt_pk_bf16_f32 v110, v8, v9\n"
      "v_cvt_pk_bf16_f32 v111, v10, 0\n"
      "v_mfma_f32_16x16x32_bf16 v[144:147], v[164:167], v[16:19], v[144:147]\n"
      "v_max3_f32 v3, v224, v225, v227\n"
      "v_max3_f32 v4, v12, v13, v15\n"
      "v_mfma_f32_16x16x32_bf16 v[76:79], v[164:167], v[92:95], v[76:79]\n"
      "v_max3_f32 v3, v3, v132, v133\n"
      "v_max3_f32 v4, v4, v68, v69\n"
      "v_mfma_f32_16x16x32_bf16 v[80:83], v[164:167], v[136:139], v[80:83]\n"
      "v_max3_f32 v3, v3, v134, v135\n"
      "v_max3_f32 v4, v4, v70, v71\n"
      "v_mfma_f32_16x16x32_bf16 v[228:231], v[164:167], v[148:151], v[228:231]\n"
      "v_cvt_pk_bf16_f32 v40, v144, v76\n"
      "v_cvt_pk_bf16_f32 v8, v145, v77\n"
      "v_mfma_f32_16x16x32_bf16 v[168:171], v[152:155], v[16:19], v[168:171]\n"
      "s_add_u32 s33, s20, 24576\n"
      "v_cvt_pk_bf16_f32 v10, v146, v78\n"
      "v_cvt_pk_bf16_f32 v44, v147, v79\n"
      "v_mfma_f32_16x16x32_bf16 v[88:91], v[152:155], v[92:95], v[88:91]\n"
      "v_cvt_pk_bf16_f32 v41, v80, v228\n"
      "v_cvt_pk_bf16_f32 v9, v81, v229\n"
      "v_mfma_f32_16x16x32_bf16 v[100:103], v[152:155], v[136:139], v[100:103]\n"
      "v_cvt_pk_bf16_f32 v11, v82, v230\n"
      "v_cvt_pk_bf16_f32 v45, v83, v231\n"
      "v_mfma_f32_16x16x32_bf16 v[200:203], v[152:155], v[148:151], v[200:203]\n"
      "v_pk_max_i16 v40, v40, 0\n"
      "v_pk_max_i16 v8, v8, 0\n"
      "v_mfma_f32_16x16x32_bf16 v[124:127], v[208:211], v[16:19], v[124:127]\n"
      "s_waitcnt vmcnt(24)\n"
      "ds_read_b128 v[76:79], v6 offset:24576\n"
      "v_pk_max_i16 v10, v10, 0\n"
      "v_mfma_f32_16x16x32_bf16 v[64:67], v[208:211], v[92:95], v[64:67]\n"
      "s_add_u32 s34, s20, 28672\n"
      "ds_read_b128 v[116:119], v1 offset:24576\n"
      "v_pk_max_i16 v44, v44, 0\n"
      "v_mfma_f32_16x16x32_bf16 v[80:83], v[208:211], v[136:139], 0\n"
      "ds_read_b128 v[120:123], v42 offset:24576\n"
      "v_pk_max_i16 v41, v41, 0\n"
      "v_mfma_f32_16x16x32_bf16 v[144:147], v[208:211], v[148:151], 0\n"
      "buffer_store_dwordx2 v[40:41], v0, s[28:31], s33 offen sc1\n"
      "v_mfma_f32_16x16x32_bf16 v[80:83], v[140:143], a[224:227], v[80:83]\n"
      "ds_read_b128 v[16:19], v43 offset:24576\n"
      "v_pk_max_i16 v9, v9, 0\n"
      "v_mfma_f32_16x16x32_bf16 v[80:83], v[156:159], a[228:231], v[80:83]\n"
      "v_pk_max_i16 v11, v11, 0\n"
      "v_pk_max_i16 v45, v45, 0\n"
      "v_mfma_f32_16x16x32_bf16 v[80:83], v[160:163], a[232:235], v[80:83]\n"
      "v_cvt_pk_bf16_f32 v6, v168, v88\n"
      "v_cvt_pk_bf16_f32 v46, v169, v89\n"
      "v_mfma_f32_16x16x32_bf16 v[80:83], v[128:131], a[236:239], v[80:83]\n"
      "v_cvt_pk_bf16_f32 v92, v170, v90\n"
      "v_cvt_pk_bf16_f32 v94, v171, v91\n"
      "v_mfma_f32_16x16x32_bf16 v[144:147], v[140:143], a[240:243], v[144:147]\n"
      "buffer_store_dwordx2 v[8:9], v0, s[28:31], s33 offen offset:1024 sc1\n"
      "v_mfma_f32_16x16x32_bf16 v[144:147], v[156:159], a[244:247], v[144:147]\n"
      "v_cvt_pk_bf16_f32 v7, v100, v200\n"
      "v_cvt_pk_bf16_f32 v47, v101, v201\n"
      "v_mfma_f32_16x16x32_bf16 v[144:147], v[160:163], a[248:251], v[144:147]\n"
      ".Lpad4%=:\n"
      "s_cbranch_execz .Lret0%=\n"
      "v_cvt_pk_bf16_f32 v93, v102, v202\n"
      "v_cvt_pk_bf16_f32 v95, v103, v203\n"
      "v_mfma_f32_16x16x32_bf16 v[144:147], v[128:131], a[252:255], v[144:147]\n"
      "s_waitcnt lgkmcnt(0)\n"
      "v_pk_max_i16 v6, v6, 0\n"
      "v_pk_max_i16 v46, v46, 0\n"
      "v_mfma_f32_16x16x32_bf16 v[40:43], a[0:3], v[76:79], 0\n"
      "v_pk_max_i16 v92, v92, 0\n"
      "v_pk_max_i16 v94, v94, 0\n"
      "v_mfma_f32_16x16x32_bf16 v[40:43], a[4:7], v[116:119], v[40:43]\n"
      "buffer_store_dwordx2 v[10:11], v0, s[28:31], s33 offen offset:2048 sc1\n"
      "v_mfma_f32_16x16x32_bf16 v[40:43], a[8:11], v[120:123], v[40:43]\n"
      "v_pk_max_i16 v7, v7, 0\n"
      "v_pk_max_i16 v47, v47, 0\n"
      "v_mfma_f32_16x16x32_bf16 v[40:43], a[12:15], v[16:19], v[40:43]\n"
      "v_pk_max_i16 v93, v93, 0\n"
      "v_pk_max_i16 v95, v95, 0\n"
      "v_mfma_f32_16x16x32_bf16 v[88:91], a[16:19], v[76:79], 0\n"
      "v_permlane32_swap_b32 v3, v4\n"
      "v_mfma_f32_16x16x32_bf16 v[88:91], a[20:23], v[116:119], v[88:91]\n"
      "v_cvt_pk_bf16_f32 v100, v40, v41\n"
      "v_cvt_pk_bf16_f32 v101, v42, v43\n"
      "v_mfma_f32_16x16x32_bf16 v[88:91], a[24:27], v[120:123], v[88:91]\n"
      "buffer_store_dwordx2 v[44:45], v0, s[28:31], s33 offen offset:3072 sc1\n"
      "v_mfma_f32_16x16x32_bf16 v[88:91], a[28:31], v[16:19], v[88:91]\n"
      "v_max_f32 v1, v3, v4\n"
      "v_max_f32 v2, v3, v4\n"
      "v_mfma_f32_16x16x32_bf16 v[8:11], a[32:35], v[76:79], 0\n"
      "s_nop 0\n"
      "v_permlane16_swap_b32 v1, v2\n"
      "v_mfma_f32_16x16x32_bf16 v[8:11], a[36:39], v[116:119], v[8:11]\n"
      "v_cvt_pk_bf16_f32 v102, v88, v89\n"
      "v_cvt_pk_bf16_f32 v103, v90, v91\n"
      "v_mfma_f32_16x16x32_bf16 v[8:11], a[40:43], v[120:123], v[8:11]\n"
      "v_max_f32 v1, v1, v2\n"
      "v_mfma_f32_16x16x32_bf16 v[8:11], a[44:47], v[16:19], v[8:11]\n"
      "buffer_store_dwordx2 v[6:7], v0, s[28:31], s34 offen sc1\n"
      "v_mfma_f32_16x16x32_bf16 v[40:43], a[48:51], v[76:79], 0\n"
      "v_mul_f32 v3, s32, v1\n"
      "v_mul_f32 v4, s32, v1\n"
      "v_mfma_f32_16x16x32_bf16 v[40:43], a[52:55], v[116:119], v[40:43]\n"
      "v_cvt_pk_bf16_f32 v128, v8, v9\n"
      "v_cvt_pk_bf16_f32 v129, v10, v11\n"
      "v_mfma_f32_16x16x32_bf16 v[40:43], a[56:59], v[120:123], v[40:43]\n"
      "v_permlane32_swap_b32 v3, v4\n"
      "v_mfma_f32_16x16x32_bf16 v[40:43], a[60:63], v[16:19], v[40:43]\n"
      "v_fma_f32 v224, v224, s32, -v3\n"
      "v_fma_f32 v225, v225, s32, -v3\n"
      "v_mfma_f32_16x16x32_bf16 v[88:91], a[64:67], v[76:79], 0\n"
      "buffer_store_dwordx2 v[46:47], v0, s[28:31], s34 offen offset:1024 sc1\n"
      "v_mfma_f32_16x16x32_bf16 v[88:91], a[68:71], v[116:119], v[88:91]\n"
      "v_cvt_pk_bf16_f32 v130, v40, v41\n"
      "v_cvt_pk_bf16_f32 v131, v42, v43\n"
      "v_mfma_f32_16x16x32_bf16 v[88:91], a[72:75], v[120:123], v[88:91]\n"
      "v_fma_f32 v12, v12, s32, -v4\n"
      "v_fma_f32 v13, v13, s32, -v4\n"
      "v_mfma_f32_16x16x32_bf16 v[88:91], a[76:79], v[16:19], v[88:91]\n"
      "v_exp_f32 v224, v224\n"
      "v_mfma_f32_16x16x32_bf16 v[8:11], a[80:83], v[76:79], 0\n"
      "v_exp_f32 v225, v225\n"
      "v_mfma_f32_16x16x32_bf16 v[8:11], a[84:87], v[116:119], v[8:11]\n"
      "buffer_store_dwordx2 v[92:93], v0, s[28:31], s34 offen offset:2048 sc1\n"
      "v_mfma_f32_16x16x32_bf16 v[8:11], a[88:91], v[120:123], v[8:11]\n"
      "v_cvt_pk_bf16_f32 v44, v88, v89\n"
      "v_cvt_pk_bf16_f32 v45, v90, v91\n"
      "v_mfma_f32_16x16x32_bf16 v[8:11], a[92:95], v[16:19], v[8:11]\n"
      "v_fma_f32 v227, v227, s32, -v3\n"
      "v_fma_f32 v132, v132, s32, -v3\n"
      "v_mfma_f32_16x16x32_bf16 v[40:43], a[96:99], v[76:79], 0\n"
      "v_fma_f32 v133, v133, s32, -v3\n"
      "v_fma_f32 v15, v15, s32, -v4\n"
      "v_mfma_f32_16x16x32_bf16 v[40:43], a[100:103], v[116:119], v[40:43]\n"
      "v_cvt_pk_bf16_f32 v46, v8, v9\n"
      "v_cvt_pk_bf16_f32 v47, v10, v11\n"
      "v_mfma_f32_16x16x32_bf16 v[40:43], a[104:107], v[120:123], v[40:43]\n"
      "buffer_store_dwordx2 v[94:95], v0, s[28:31], s34 offen offset:3072 sc1\n"
      "v_mfma_f32_16x16x32_bf16 v[40:43], a[108:111], v[16:19], v[40:43]\n"
      "v_exp_f32 v12, v12\n"
      "v_mfma_f32_16x16x32_bf16 v[88:91], a[112:115], v[76:79], 0\n"
      "v_exp_f32 v13, v13\n"
      "v_mfma_f32_16x16x32_bf16 v[88:91], a[116:119], v[116:119], v[88:91]\n"
      "v_cvt_pk_bf16_f32 v136, v40, v41\n"
      "v_cvt_pk_bf16_f32 v137, v42, v43\n"
      "v_mfma_f32_16x16x32_bf16 v[88:91], a[120:123], v[120:123], v[88:91]\n"
      "v_fma_f32 v68, v68, s32, -v4\n"
      "v_fma_f32 v69, v69, s32, -v4\n"
      "v_mfma_f32_16x16x32_bf16 v[88:91], a[124:127], v[16:19], v[88:91]\n"
      "v_exp_f32 v227, v227\n"
      "v_mfma_f32_16x16x32_bf16 v[8:11], v[36:39], a[128:131], 0\n"
      "v_exp_f32 v132, v132\n"
      "v_mfma_f32_16x16x32_bf16 v[8:11], v[60:63], a[132:135], v[8:11]\n"
      "v_cvt_pk_bf16_f32 v138, v88, v89\n"
      "v_cvt_pk_bf16_f32 v139, v90, v91\n"
      "v_mfma_f32_16x16x32_bf16 v[8:11], v[24:27], a[136:139], v[8:11]\n"
      "v_exp_f32 v133, v133\n"
      "v_mfma_f32_16x16x32_bf16 v[8:11], v[220:223], a[140:143], v[8:11]\n"
      "v_fma_f32 v134, v134, s32, -v3\n"
      "v_fma_f32 v135, v135, s32, -v3\n"
      "v_mfma_f32_16x16x32_bf16 v[40:43], v[36:39], a[144:147], 0\n"
      "v_add_f32 v1, v224, v225\n"
      "v_fma_f32 v70, v70, s32, -v4\n"
      "v_mfma_f32_16x16x32_bf16 v[40:43], v[60:63], a[148:151], v[40:43]\n"
      "v_cvt_pk_bf16_f32 v252, v8, v9\n"
      "v_cvt_pk_bf16_f32 v253, v10, v11\n"
      "v_mfma_f32_16x16x32_bf16 v[40:43], v[24:27], a[152:155], v[40:43]\n"
      "v_exp_f32 v15, v15\n"
      "v_mfma_f32_16x16x32_bf16 v[40:43], v[220:223], a[156:159], v[40:43]\n"
      "v_exp_f32 v68, v68\n"
      "v_mfma_f32_16x16x32_bf16 v[88:91], v[72:75], v[96:99], v[56:59]\n"
      "v_exp_f32 v69, v69\n"
      "v_mfma_f32_16x16x32_bf16 v[88:91], v[84:87], v[32:35], v[88:91]\n"
      "v_cvt_pk_bf16_f32 v246, v40, v41\n"
      "v_cvt_pk_bf16_f32 v247, v42, v43\n"
      "v_mfma_f32_16x16x32_bf16 v[92:95], v[44:47], v[96:99], 0\n"
      "v_fma_f32 v71, v71, s32, -v4\n"
      "v_add_f32 v2, v12, v13\n"
      "v_mfma_f32_16x16x32_bf16 v[92:95], v[136:139], v[32:35], v[92:95]\n"
      "v_exp_f32 v134, v134\n"
      "v_mfma_f32_16x16x32_bf16 v[8:11], v[72:75], v[100:103], v[56:59]\n"
      "v_exp_f32 v135, v135\n"
      "v_mfma_f32_16x16x32_bf16 v[8:11], v[84:87], v[128:131], v[8:11]\n"
      "v_add_f32 v3, v227, v132\n"
      "v_add_f32 v1, v1, v133\n"
      "v_mfma_f32_16x16x32_bf16 v[140:143], v[44:47], v[100:103], 0\n"
      "v_exp_f32 v70, v70\n"
      "v_mfma_f32_16x16x32_bf16 v[140:143], v[136:139], v[128:131], v[140:143]\n"
      "v_exp_f32 v71, v71\n"
      "v_mfma_f32_16x16x32_bf16 v[4:7], v[36:39], a[160:163], 0\n"
      "v_add_f32 v32, v15, v68\n"
      "v_add_f32 v2, v2, v69\n"
      "v_mfma_f32_16x16x32_bf16 v[4:7], v[60:63], a[164:167], v[4:7]\n"
      "v_add_f32 v3, v3, v134\n"
      "v_add_f32 v1, v1, v135\n"
      "v_mfma_f32_16x16x32_bf16 v[4:7], v[24:27], a[168:171], v[4:7]\n"
      "v_add_f32 v32, v32, v70\n"
      "v_add_f32 v2, v2, v71\n"
      "v_mfma_f32_16x16x32_bf16 v[4:7], v[220:223], a[172:175], v[4:7]\n"
      "v_add_f32 v1, v1, v3\n"
      "v_add_f32 v2, v2, v32\n"
      "v_mfma_f32_16x16x32_bf16 v[40:43], v[36:39], a[176:179], 0\n"
      "s_nop 0\n"
      "v_permlane32_swap_b32 v1, v2\n"
      "v_mfma_f32_16x16x32_bf16 v[40:43], v[60:63], a[180:183], v[40:43]\n"
      "v_cvt_pk_bf16_f32 v240, v4, v5\n"
      "v_cvt_pk_bf16_f32 v241, v6, v7\n"
      "v_mfma_f32_16x16x32_bf16 v[40:43], v[24:27], a[184:187], v[40:43]\n"
      "v_add_f32 v3, v1, v2\n"
      "v_add_f32 v32, v1, v2\n"
      "v_mfma_f32_16x16x32_bf16 v[40:43], v[220:223], a[188:191], v[40:43]\n"
      "s_nop 0\n"
      "v_permlane16_swap_b32 v3, v32\n"
      "v_mfma_f32_16x16x32_bf16 v[44:47], v[36:39], a[192:195], 0\n"
      "v_add_f32 v3, v3, v32\n"
      "v_max3_f32 v33, v89, v90, v91\n"
      "v_mfma_f32_16x16x32_bf16 v[44:47], v[60:63], a[196:199], v[44:47]\n"
      "v_cvt_pk_bf16_f32 v234, v40, v41\n"
      "v_cvt_pk_bf16_f32 v235, v42, v43\n"
      "v_mfma_f32_16x16x32_bf16 v[44:47], v[24:27], a[200:203], v[44:47]\n"
      "v_rcp_f32 v3, v3\n"
      "v_mfma_f32_16x16x32_bf16 v[44:47], v[220:223], a[204:207], v[44:47]\n"
      "v_cndmask_b32_e64 v1, 0, v3, s[50:51]\n"
      "v_cndmask_b32_e64 v2, 0, v3, s[50:51]\n"
      "v_mfma_f32_16x16x32_bf16 v[4:7], v[36:39], a[208:211], 0\n"
      "s_nop 0\n"
      "v_permlane32_swap_b32 v1, v2\n"
      "v_mfma_f32_16x16x32_bf16 v[4:7], v[60:63], a[212:215], v[4:7]\n"
      "v_mul_f32 v224, v224, v1\n"
      "v_mul_f32 v225, v225, v1\n"
      "v_mfma_f32_16x16x32_bf16 v[4:7], v[24:27], a[216:219], v[4:7]\n"
      "v_mul_f32 v227, v227, v1\n"
      "v_mul_f32 v132, v132, v1\n"
      "v_mfma_f32_16x16x32_bf16 v[4:7], v[220:223], a[220:223], v[4:7]\n"
      "v_mul_f32 v133, v133, v1\n"
      "v_mul_f32 v134, v134, v1\n"
      "v_mfma_f32_16x16x32_bf16 v[124:127], v[104:107], v[250:253], v[124:127]\n"
      "v_mul_f32 v135, v135, v1\n"
      "v_cvt_pk_bf16_f32 v40, v224, v225\n"
      "v_mfma_f32_16x16x32_bf16 v[64:67], v[104:107], v[244:247], v[64:67]\n"
      "v_cvt_pk_bf16_f32 v41, 0, v227\n"
      "v_cvt_pk_bf16_f32 v42, v132, v133\n"
      "v_mfma_f32_16x16x32_bf16 v[80:83], v[104:107], v[238:241], v[80:83]\n"
      "v_cvt_pk_bf16_f32 v43, v134, v135\n"
      "v_cndmask_b32_e32 v48, 0, v40, vcc\n"
      "v_mfma_f32_16x16x32_bf16 v[144:147], v[104:107], v[232:235], v[144:147]\n"
      "v_cvt_pk_bf16_f32 v34, v124, v64\n"
      "v_cvt_pk_bf16_f32 v52, v125, v65\n"
      "v_mfma_f32_16x16x32_bf16 v[44:47], v[108:111], v[250:253], v[44:47]\n"
      "s_add_u32 s35, s20, 32768\n"
      "v_cvt_pk_bf16_f32 v54, v126, v66\n"
      "v_cvt_pk_bf16_f32 v56, v127, v67\n"
      "v_mfma_f32_16x16x32_bf16 v[4:7], v[108:111], v[244:247], v[4:7]\n"
      "v_cvt_pk_bf16_f32 v35, v80, v144\n"
      "v_cvt_pk_bf16_f32 v53, v81, v145\n"
      "v_mfma_f32_16x16x32_bf16 v[72:75], v[108:111], v[238:241], 0\n"
      "v_cvt_pk_bf16_f32 v55, v82, v146\n"
      "v_cvt_pk_bf16_f32 v57, v83, v147\n"
      "v_mfma_f32_16x16x32_bf16 v[84:87], v[108:111], v[232:235], 0\n"
      "v_pk_max_i16 v34, v34, 0\n"
      "v_pk_max_i16 v52, v52, 0\n"
      "v_mfma_f32_16x16x32_bf16 v[72:75], v[36:39], a[224:227], v[72:75]\n"
      "v_pk_max_i16 v54, v54, 0\n"
      "v_pk_max_i16 v56, v56, 0\n"
      "v_mfma_f32_16x16x32_bf16 v[72:75], v[60:63], a[228:231], v[72:75]\n"
      "v_pk_max_i16 v35, v35, 0\n"
      "v_pk_max_i16 v53, v53, 0\n"
      "v_mfma_f32_16x16x32_bf16 v[72:75], v[24:27], a[232:235], v[72:75]\n"
      "buffer_store_dwordx2 v[34:35], v0, s[28:31], s35 offen sc1\n"
      "v_mfma_f32_16x16x32_bf16 v[72:75], v[220:223], a[236:239], v[72:75]\n"
      "v_pk_max_i16 v55, v55, 0\n"
      "v_pk_max_i16 v57, v57, 0\n"
      "v_mfma_f32_16x16x32_bf16 v[84:87], v[36:39], a[240:243], v[84:87]\n"
      "v_cndmask_b32_e32 v49, 0, v41, vcc\n"
      "v_cndmask_b32_e32 v50, 0, v42, vcc\n"
      "v_mfma_f32_16x16x32_bf16 v[84:87], v[60:63], a[244:247], v[84:87]\n"
      "v_cndmask_b32_e32 v51, 0, v43, vcc\n"
      "v_cndmask_b32_e64 v40, v40, 0, vcc\n"
      "v_mfma_f32_16x16x32_bf16 v[84:87], v[24:27], a[248:251], v[84:87]\n"
      "v_cndmask_b32_e64 v41, v41, 0, vcc\n"
      "v_cndmask_b32_e64 v42, v42, 0, vcc\n"
      "v_mfma_f32_16x16x32_bf16 v[84:87], v[220:223], a[252:255], v[84:87]\n"
      "buffer_store_dwordx2 v[52:53], v0, s[28:31], s35 offen offset:1024 sc1\n"
      "v_mfma_f32_16x16x32_bf16 v[64:67], v[172:175], a[128:131], 0\n"
      "v_cndmask_b32_e64 v43, v43, 0, vcc\n"
      "v_mul_f32 v12, v12, v2\n"
      "v_mfma_f32_16x16x32_bf16 v[64:67], v[180:183], a[132:135], v[64:67]\n"
      "v_mul_f32 v13, v13, v2\n"
      "v_mul_f32 v15, v15, v2\n"
      "v_mfma_f32_16x16x32_bf16 v[64:67], v[20:23], a[136:139], v[64:67]\n"
      "v_mul_f32 v68, v68, v2\n"
      "v_mul_f32 v69, v69, v2\n"
      "v_mfma_f32_16x16x32_bf16 v[64:67], v[188:191], a[140:143], v[64:67]\n"
      "v_mul_f32 v70, v70, v2\n"
      "v_mul_f32 v71, v71, v2\n"
      "v_mfma_f32_16x16x32_bf16 v[24:27], v[172:175], a[144:147], 0\n"
      "buffer_store_dwordx2 v[54:55], v0, s[28:31], s35 offen offset:2048 sc1\n"
      "v_mfma_f32_16x16x32_bf16 v[24:27], v[180:183], a[148:151], v[24:27]\n"
      "v_cvt_pk_bf16_f32 v254, v64, v65\n"
      "v_cvt_pk_bf16_f32 v255, v66, v67\n"
      "v_mfma_f32_16x16x32_bf16 v[24:27], v[20:23], a[152:155], v[24:27]\n"
      "v_cvt_pk_bf16_f32 v36, v12, v13\n"
      "v_cvt_pk_bf16_f32 v37, 0, v15\n"
      "v_mfma_f32_16x16x32_bf16 v[24:27], v[188:191], a[156:159], v[24:27]\n"
      "v_cvt_pk_bf16_f32 v38, v68, v69\n"
      "v_cvt_pk_bf16_f32 v39, v70, v71\n"
      "v_mfma_f32_16x16x32_bf16 v[60:63], v[172:175], a[160:163], 0\n"
      "v_max3_f32 v1, v9, v10, v11\n"
      "v_max3_f32 v33, v33, v92, v93\n"
      "v_mfma_f32_16x16x32_bf16 v[60:63], v[180:183], a[164:167], v[60:63]\n"
      "buffer_store_dwordx2 v[56:57], v0, s[28:31], s35 offen offset:3072 sc1\n"
      "v_mfma_f32_16x16x32_bf16 v[60:63], v[20:23], a[168:171], v[60:63]\n"
      "v_cvt_pk_bf16_f32 v248, v24, v25\n"
      "v_cvt_pk_bf16_f32 v249, v26, v27\n"
      "v_mfma_f32_16x16x32_bf16 v[60:63], v[188:191], a[172:175], v[60:63]\n"
      "v_max3_f32 v1, v1, v140, v141\n"
      "v_max3_f32 v33, v33, v94, v95\n"
      "v_mfma_f32_16x16x32_bf16 v[12:15], v[172:175], a[176:179], 0\n"
      "v_max3_f32 v1, v1, v142, v143\n"
      "v_mfma_f32_16x16x32_bf16 v[12:15], v[180:183], a[180:183], v[12:15]\n"
      "v_cvt_pk_bf16_f32 v242, v60, v61\n"
      "v_cvt_pk_bf16_f32 v243, v62, v63\n"
      "v_mfma_f32_16x16x32_bf16 v[12:15], v[20:23], a[184:187], v[12:15]\n"
      "v_permlane32_swap_b32 v33, v1\n"
      "v_mfma_f32_16x16x32_bf16 v[12:15], v[188:191], a[188:191], v[12:15]\n"
      "v_max_f32 v2, v33, v1\n"
      "v_max_f32 v3, v33, v1\n"
      "v_mfma_f32_16x16x32_bf16 v[24:27], v[204:207], a[128:131], 0\n"
      "s_nop 0\n"
      "v_permlane16_swap_b32 v2, v3\n"
      "v_mfma_f32_16x16x32_bf16 v[24:27], v[112:115], a[132:135], v[24:27]\n"
      "v_cvt_pk_bf16_f32 v236, v12, v13\n"
      "v_cvt_pk_bf16_f32 v237, v14, v15\n"
      "v_mfma_f32_16x16x32_bf16 v[24:27], v[216:219], a[136:139], v[24:27]\n"
      "v_max_f32 v2, v2, v3\n"
      "v_mfma_f32_16x16x32_bf16 v[24:27], v[28:31], a[140:143], v[24:27]\n"
      "v_mul_f32 v33, s32, v2\n"
      "v_mul_f32 v1, s32, v2\n"
      "v_mfma_f32_16x16x32_bf16 v[52:55], v[204:207], a[144:147], 0\n"
      "s_nop 0\n"
      "v_permlane32_swap_b32 v33, v1\n"
      "v_mfma_f32_16x16x32_bf16 v[52:55], v[112:115], a[148:151], v[52:55]\n"
      "v_cvt_pk_bf16_f32 v56, v24, v25\n"
      "v_cvt_pk_bf16_f32 v57, v26, v27\n"
      "v_mfma_f32_16x16x32_bf16 v[52:55], v[216:219], a[152:155], v[52:55]\n"
      "v_mov_b32 v252, v56\n"
      "v_fma_f32 v89, v89, s32, -v33\n"
      "v_mfma_f32_16x16x32_bf16 v[52:55], v[28:31], a[156:159], v[52:55]\n"
      "v_fma_f32 v90, v90, s32, -v33\n"
      "v_fma_f32 v9, v9, s32, -v1\n"
      "v_mfma_f32_16x16x32_bf16 v[12:15], v[204:207], a[160:163], 0\n"
      "v_fma_f32 v10, v10, s32, -v1\n"
      "v_fma_f32 v91, v91, s32, -v33\n"
      "v_mfma_f32_16x16x32_bf16 v[12:15], v[112:115], a[164:167], v[12:15]\n"
      "v_cvt_pk_bf16_f32 v60, v52, v53\n"
      "v_cvt_pk_bf16_f32 v61, v54, v55\n"
      "v_mfma_f32_16x16x32_bf16 v[12:15], v[216:219], a[168:171], v[12:15]\n"
      "v_mov_b32 v246, v60\n"
      "v_fma_f32 v92, v92, s32, -v33\n"
      "v_mfma_f32_16x16x32_bf16 v[12:15], v[28:31], a[172:175], v[12:15]\n"
      "v_exp_f32 v89, v89\n"
      "v_mfma_f32_16x16x32_bf16 v[24:27], v[204:207], a[176:179], 0\n"
      "v_exp_f32 v90, v90\n"
      "v_mfma_f32_16x16x32_bf16 v[24:27], v[112:115], a[180:183], v[24:27]\n"
      "v_cvt_pk_bf16_f32 v64, v12, v13\n"
      "v_cvt_pk_bf16_f32 v65, v14, v15\n"
      "v_mfma_f32_16x16x32_bf16 v[24:27], v[216:219], a[184:187], v[24:27]\n"
      "v_mov_b32 v240, v64\n"
      "v_fma_f32 v93, v93, s32, -v33\n"
      "v_mfma_f32_16x16x32_bf16 v[24:27], v[28:31], a[188:191], v[24:27]\n"
      "v_exp_f32 v9, v9\n"
      "v_mfma_f32_16x16x32_bf16 v[52:55], v[172:175], a[192:195], 0\n"
      "v_exp_f32 v10, v10\n"
      "v_mfma_f32_16x16x32_bf16 v[52:55], v[180:183], a[196:199], v[52:55]\n"
      "v_cvt_pk_bf16_f32 v68, v24, v25\n"
      "v_cvt_pk_bf16_f32 v69, v26, v27\n"
      "v_mfma_f32_16x16x32_bf16 v[52:55], v[20:23], a[200:203], v[52:55]\n"
      "v_mov_b32 v234, v68\n"
      "v_fma_f32 v11, v11, s32, -v1\n"
      "v_mfma_f32_16x16x32_bf16 v[52:55], v[188:191], a[204:207], v[52:55]\n"
      "v_fma_f32 v140, v140, s32, -v1\n"
      "v_fma_f32 v141, v141, s32, -v1\n"
      "v_mfma_f32_16x16x32_bf16 v[12:15], v[172:175], a[208:211], 0\n"
      "v_exp_f32 v91, v91\n"
      "v_mfma_f32_16x16x32_bf16 v[12:15], v[180:183], a[212:215], v[12:15]\n"
      "v_exp_f32 v92, v92\n"
      "v_mfma_f32_16x16x32_bf16 v[12:15], v[20:23], a[216:219], v[12:15]\n"
      "v_exp_f32 v93, v93\n"
      "v_mfma_f32_16x16x32_bf16 v[12:15], v[188:191], a[220:223], v[12:15]\n"
      "v_fma_f32 v94, v94, s32, -v33\n"
      "v_fma_f32 v95, v95, s32, -v33\n"
      "v_mfma_f32_16x16x32_bf16 v[44:47], v[40:43], v[252:255], v[44:47]\n"
      "v_add_f32 v2, v89, v90\n"
      "v_fma_f32 v142, v142, s32, -v1\n"
      "v_mfma_f32_16x16x32_bf16 v[4:7], v[40:43], v[246:249], v[4:7]\n"
      "v_exp_f32 v11, v11\n"
      "v_mfma_f32_16x16x32_bf16 v[72:75], v[40:43], v[240:243], v[72:75]\n"
      "v_exp_f32 v140, v140\n"
      "v_mfma_f32_16x16x32_bf16 v[84:87], v[40:43], v[234:237], v[84:87]\n"
      "v_cvt_pk_bf16_f32 v24, v44, v4\n"
      "v_cvt_pk_bf16_f32 v26, v45, v5\n"
      "v_mfma_f32_16x16x32_bf16 v[52:55], v[36:39], v[252:255], v[52:55]\n"
      "s_add_u32 s33, s20, 36864\n"
      "v_cvt_pk_bf16_f32 v32, v46, v6\n"
      "v_cvt_pk_bf16_f32 v34, v47, v7\n"
      "v_mfma_f32_16x16x32_bf16 v[12:15], v[36:39], v[246:249], v[12:15]\n"
      "v_cvt_pk_bf16_f32 v25, v72, v84\n"
      "v_cvt_pk_bf16_f32 v27, v73, v85\n"
      "v_mfma_f32_16x16x32_bf16 v[80:83], v[36:39], v[240:243], 0\n"
      "v_cvt_pk_bf16_f32 v33, v74, v86\n"
      "v_cvt_pk_bf16_f32 v35, v75, v87\n"
      "v_mfma_f32_16x16x32_bf16 v[40:43], v[36:39], v[234:237], 0\n"
      "v_exp_f32 v141, v141\n"
      "v_mfma_f32_16x16x32_bf16 v[96:99], v[48:51], v[252:255], 0\n"
      "v_fma_f32 v143, v143, s32, -v1\n"
      "v_add_f32 v3, v9, v10\n"
      "v_mfma_f32_16x16x32_bf16 v[4:7], v[48:51], v[246:249], 0\n"
      "v_pk_max_i16 v24, v24, 0\n"
      "v_pk_max_i16 v26, v26, 0\n"
      "v_mfma_f32_16x16x32_bf16 v[44:47], v[48:51], v[240:243], 0\n"
      "v_pk_max_i16 v32, v32, 0\n"
      "v_pk_max_i16 v34, v34, 0\n"
      "v_mfma_f32_16x16x32_bf16 v[72:75], v[48:51], v[234:237], 0\n"
      "v_pk_max_i16 v25, v25, 0\n"
      "v_pk_max_i16 v27, v27, 0\n"
      "v_mfma_f32_16x16x32_bf16 v[80:83], v[172:175], a[224:227], v[80:83]\n"
      "buffer_store_dwordx2 v[24:25], v0, s[28:31], s33 offen sc1\n"
      "v_mfma_f32_16x16x32_bf16 v[80:83], v[180:183], a[228:231], v[80:83]\n"
      "v_pk_max_i16 v33, v33, 0\n"
      "v_pk_max_i16 v35, v35, 0\n"
      "v_mfma_f32_16x16x32_bf16 v[80:83], v[20:23], a[232:235], v[80:83]\n"
      "v_exp_f32 v94, v94\n"
      "v_mfma_f32_16x16x32_bf16 v[80:83], v[188:191], a[236:239], v[80:83]\n"
      "v_exp_f32 v95, v95\n"
      "v_mfma_f32_16x16x32_bf16 v[40:43], v[172:175], a[240:243], v[40:43]\n"
      "v_add_f32 v1, v91, v92\n"
      "v_add_f32 v2, v2, v93\n"
      "v_mfma_f32_16x16x32_bf16 v[40:43], v[180:183], a[244:247], v[40:43]\n"
      "buffer_store_dwordx2 v[26:27], v0, s[28:31], s33 offen offset:1024 sc1\n"
      "v_mfma_f32_16x16x32_bf16 v[40:43], v[20:23], a[248:251], v[40:43]\n"
      "v_exp_f32 v142, v142\n"
      "v_mfma_f32_16x16x32_bf16 v[40:43], v[188:191], a[252:255], v[40:43]\n"
      "v_exp_f32 v143, v143\n"
      "v_mfma_f32_16x16x32_bf16 v[36:39], v[76:79], a[128:131], 0\n"
      "s_add_u32 s34, s20, 40960\n"
      "v_add_f32 v24, v11, v140\n"
      "v_add_f32 v3, v3, v141\n"
      "v_mfma_f32_16x16x32_bf16 v[36:39], v[116:119], a[132:135], v[36:39]\n"
      "v_cvt_pk_bf16_f32 v48, v52, v12\n"
      "v_cvt_pk_bf16_f32 v50, v53, v13\n"
      "v_mfma_f32_16x16x32_bf16 v[36:39], v[120:123], a[136:139], v[36:39]\n"
      "buffer_store_dwordx2 v[32:33], v0, s[28:31], s33 offen offset:2048 sc1\n"
      "v_mfma_f32_16x16x32_bf16 v[36:39], v[16:19], a[140:143], v[36:39]\n"
      "v_cvt_pk_bf16_f32 v26, v54, v14\n"
      "v_cvt_pk_bf16_f32 v20, v55, v15\n"
      "v_mfma_f32_16x16x32_bf16 v[84:87], v[76:79], a[144:147], 0\n"
      "v_add_f32 v1, v1, v94\n"
      "v_add_f32 v2, v2, v95\n"
      "v_mfma_f32_16x16x32_bf16 v[84:87], v[116:119], a[148:151], v[84:87]\n"
      "v_cvt_pk_bf16_f32 v58, v36, v37\n"
      "v_cvt_pk_bf16_f32 v59, v38, v39\n"
      "v_mfma_f32_16x16x32_bf16 v[84:87], v[120:123], a[152:155], v[84:87]\n"
      "v_add_f32 v24, v24, v142\n"
      "v_add_f32 v3, v3, v143\n"
      "v_mfma_f32_16x16x32_bf16 v[84:87], v[16:19], a[156:159], v[84:87]\n"
      "buffer_store_dwordx2 v[34:35], v0, s[28:31], s33 offen offset:3072 sc1\n"
      "v_mfma_f32_16x16x32_bf16 v[12:15], v[76:79], a[160:163], 0\n"
      "v_add_f32 v2, v2, v1\n"
      "v_add_f32 v3, v3, v24\n"
      "v_mfma_f32_16x16x32_bf16 v[12:15], v[116:119], a[164:167], v[12:15]\n"
      "s_nop 0\n"
      "v_permlane32_swap_b32 v2, v3\n"
      "v_mfma_f32_16x16x32_bf16 v[12:15], v[120:123], a[168:171], v[12:15]\n"
      "v_add_f32 v1, v2, v3\n"
      "v_add_f32 v24, v2, v3\n"
      "v_mfma_f32_16x16x32_bf16 v[12:15], v[16:19], a[172:175], v[12:15]\n"
      "v_cvt_pk_bf16_f32 v62, v84, v85\n"
      "v_cvt_pk_bf16_f32 v63, v86, v87\n"
      "v_mfma_f32_16x16x32_bf16 v[32:35], v[76:79], a[176:179], 0\n"
      "v_permlane16_swap_b32 v1, v24\n"
      "v_mfma_f32_16x16x32_bf16 v[32:35], v[116:119], a[180:183], v[32:35]\n"
      "v_add_f32 v1, v1, v24\n"
      "v_cvt_pk_bf16_f32 v66, v12, v13\n"
      "v_mfma_f32_16x16x32_bf16 v[32:35], v[120:123], a[184:187], v[32:35]\n"
      "v_rcp_f32 v1, v1\n"
      "v_mfma_f32_16x16x32_bf16 v[32:35], v[16:19], a[188:191], v[32:35]\n"
      "v_cndmask_b32_e64 v2, 0, v1, s[52:53]\n"
      "v_cndmask_b32_e64 v3, 0, v1, s[52:53]\n"
      "v_mfma_f32_16x16x32_bf16 v[96:99], v[204:207], a[192:195], v[96:99]\n"
      "v_cvt_pk_bf16_f32 v67, v14, v15\n"
      "v_permlane32_swap_b32 v2, v3\n"
      "v_mfma_f32_16x16x32_bf16 v[96:99], v[112:115], a[196:199], v[96:99]\n"
      "v_mul_f32 v89, v89, v2\n"
      "v_mul_f32 v90, v90, v2\n"
      "v_mul_f32 v91, v91, v2\n"
      "v_mfma_f32_16x16x32_bf16 v[96:99], v[216:219], a[200:203], v[96:99]\n"
      "v_mul_f32 v92, v92, v2\n"
      "v_mul_f32 v93, v93, v2\n"
      "v_mul_f32 v94, v94, v2\n"
      "v_mfma_f32_16x16x32_bf16 v[96:99], v[28:31], a[204:207], v[96:99]\n"
      "v_mul_f32 v95, v95, v2\n"
      "v_cvt_pk_bf16_f32 v12, 0, v89\n"
      "v_cvt_pk_bf16_f32 v13, v90, v91\n"
      "v_mfma_f32_16x16x32_bf16 v[4:7], v[204:207], a[208:211], v[4:7]\n"
      "v_cvt_pk_bf16_f32 v14, v92, v93\n"
      "v_cvt_pk_bf16_f32 v15, v94, v95\n"
      "v_cvt_pk_bf16_f32 v70, v32, v33\n"
      "v_mfma_f32_16x16x32_bf16 v[4:7], v[112:115], a[212:215], v[4:7]\n"
      "v_cvt_pk_bf16_f32 v71, v34, v35\n"
      "v_mul_f32 v9, v9, v3\n"
      "v_mul_f32 v10, v10, v3\n"
      "v_mfma_f32_16x16x32_bf16 v[4:7], v[216:219], a[216:219], v[4:7]\n"
      "v_mul_f32 v11, v11, v3\n"
      "v_mul_f32 v140, v140, v3\n"
      "v_mul_f32 v141, v141, v3\n"
      "v_mfma_f32_16x16x32_bf16 v[4:7], v[28:31], a[220:223], v[4:7]\n"
      "v_mul_f32 v142, v142, v3\n"
      "v_mul_f32 v143, v143, v3\n"
      "v_cvt_pk_bf16_f32 v49, v80, v40\n"
      "v_mfma_f32_16x16x32_bf16 v[96:99], v[12:15], v[56:59], v[96:99]\n"
      "v_cvt_pk_bf16_f32 v51, v81, v41\n"
      "v_cvt_pk_bf16_f32 v27, v82, v42\n"
      "v_cvt_pk_bf16_f32 v21, v83, v43\n"
      "v_mfma_f32_16x16x32_bf16 v[4:7], v[12:15], v[60:63], v[4:7]\n"
      "v_cvt_pk_bf16_f32 v32, 0, v9\n"
      "v_cvt_pk_bf16_f32 v33, v10, v11\n"
      "v_cvt_pk_bf16_f32 v34, v140, v141\n"
      "v_mfma_f32_16x16x32_bf16 v[44:47], v[12:15], v[64:67], v[44:47]\n"
      "v_cvt_pk_bf16_f32 v35, v142, v143\n"
      "v_pk_max_i16 v48, v48, 0\n"
      "v_pk_max_i16 v50, v50, 0\n"
      "v_pk_max_i16 v26, v26, 0\n"
      "v_pk_max_i16 v20, v20, 0\n"
      "v_pk_max_i16 v49, v49, 0\n"
      "v_pk_max_i16 v51, v51, 0\n"
      "v_pk_max_i16 v27, v27, 0\n"
      "v_pk_max_i16 v21, v21, 0\n"
      "v_mfma_f32_16x16x32_bf16 v[72:75], v[12:15], v[68:71], v[72:75]\n"
      "buffer_store_dwordx2 v[48:49], v0, s[28:31], s34 offen sc1\n"
      "v_cvt_pk_bf16_f32 v2, v96, v4\n"
      "v_mfma_f32_16x16x32_bf16 v[8:11], v[32:35], v[56:59], 0\n"
      "v_cvt_pk_bf16_f32 v22, v97, v5\n"
      "v_cvt_pk_bf16_f32 v24, v98, v6\n"
      "v_cvt_pk_bf16_f32 v36, v99, v7\n"
      "v_mfma_f32_16x16x32_bf16 v[40:43], v[32:35], v[60:63], 0\n"
      "v_pk_max_i16 v2, v2, 0\n"
      "v_pk_max_i16 v22, v22, 0\n"
      "v_pk_max_i16 v24, v24, 0\n"
      "v_mfma_f32_16x16x32_bf16 v[52:55], v[32:35], v[64:67], 0\n"
      "v_pk_max_i16 v36, v36, 0\n"
      "v_mfma_f32_16x16x32_bf16 v[12:15], v[32:35], v[68:71], 0\n"
      "buffer_store_dwordx2 v[50:51], v0, s[28:31], s34 offen offset:1024 sc1\n"
      "v_mfma_f32_16x16x32_bf16 v[44:47], v[204:207], a[224:227], v[44:47]\n"
      "v_mfma_f32_16x16x32_bf16 v[44:47], v[112:115], a[228:231], v[44:47]\n"
      "v_mfma_f32_16x16x32_bf16 v[44:47], v[216:219], a[232:235], v[44:47]\n"
      "v_mfma_f32_16x16x32_bf16 v[44:47], v[28:31], a[236:239], v[44:47]\n"
      "buffer_store_dwordx2 v[26:27], v0, s[28:31], s34 offen offset:2048 sc1\n"
      "v_mfma_f32_16x16x32_bf16 v[72:75], v[204:207], a[240:243], v[72:75]\n"
      "v_mfma_f32_16x16x32_bf16 v[72:75], v[112:115], a[244:247], v[72:75]\n"
      "v_mfma_f32_16x16x32_bf16 v[72:75], v[216:219], a[248:251], v[72:75]\n"
      "v_mfma_f32_16x16x32_bf16 v[72:75], v[28:31], a[252:255], v[72:75]\n"
      "buffer_store_dwordx2 v[20:21], v0, s[28:31], s34 offen offset:3072 sc1\n"
      "v_mfma_f32_16x16x32_bf16 v[8:11], v[76:79], a[192:195], v[8:11]\n"
      "s_add_u32 s35, s20, 45056\n"
      "v_mfma_f32_16x16x32_bf16 v[8:11], v[116:119], a[196:199], v[8:11]\n"
      "v_cvt_pk_bf16_f32 v3, v44, v72\n"
      "v_cvt_pk_bf16_f32 v23, v45, v73\n"
      "v_cvt_pk_bf16_f32 v25, v46, v74\n"
      "v_mfma_f32_16x16x32_bf16 v[8:11], v[120:123], a[200:203], v[8:11]\n"
      "v_cvt_pk_bf16_f32 v37, v47, v75\n"
      "v_pk_max_i16 v3, v3, 0\n"
      "v_pk_max_i16 v23, v23, 0\n"
      "v_mfma_f32_16x16x32_bf16 v[8:11], v[16:19], a[204:207], v[8:11]\n"
      "buffer_store_dwordx2 v[2:3], v0, s[28:31], s35 offen sc1\n"
      "v_pk_max_i16 v25, v25, 0\n"
      "v_mfma_f32_16x16x32_bf16 v[40:43], v[76:79], a[208:211], v[40:43]\n"
      "v_pk_max_i16 v37, v37, 0\n"
      "v_mfma_f32_16x16x32_bf16 v[40:43], v[116:119], a[212:215], v[40:43]\n"
      "v_mfma_f32_16x16x32_bf16 v[40:43], v[120:123], a[216:219], v[40:43]\n"
      "v_mfma_f32_16x16x32_bf16 v[40:43], v[16:19], a[220:223], v[40:43]\n"
      "buffer_store_dwordx2 v[22:23], v0, s[28:31], s35 offen offset:1024 sc1\n"
      "v_mfma_f32_16x16x32_bf16 v[52:55], v[76:79], a[224:227], v[52:55]\n"
      "v_mfma_f32_16x16x32_bf16 v[52:55], v[116:119], a[228:231], v[52:55]\n"
      "v_cvt_pk_bf16_f32 v26, v8, v40\n"
      "v_cvt_pk_bf16_f32 v38, v9, v41\n"
      "v_cvt_pk_bf16_f32 v2, v10, v42\n"
      "v_mfma_f32_16x16x32_bf16 v[52:55], v[120:123], a[232:235], v[52:55]\n"
      "v_cvt_pk_bf16_f32 v4, v11, v43\n"
      "v_pk_max_i16 v26, v26, 0\n"
      "v_pk_max_i16 v38, v38, 0\n"
      "v_mfma_f32_16x16x32_bf16 v[52:55], v[16:19], a[236:239], v[52:55]\n"
      "buffer_store_dwordx2 v[24:25], v0, s[28:31], s35 offen offset:2048 sc1\n"
      "v_pk_max_i16 v2, v2, 0\n"
      "v_mfma_f32_16x16x32_bf16 v[12:15], v[76:79], a[240:243], v[12:15]\n"
      "v_pk_max_i16 v4, v4, 0\n"
      "v_mfma_f32_16x16x32_bf16 v[12:15], v[116:119], a[244:247], v[12:15]\n"
      "v_mfma_f32_16x16x32_bf16 v[12:15], v[120:123], a[248:251], v[12:15]\n"
      "s_add_u32 s33, s20, 49152\n"
      "v_mfma_f32_16x16x32_bf16 v[12:15], v[16:19], a[252:255], v[12:15]\n"
      "buffer_store_dwordx2 v[36:37], v0, s[28:31], s35 offen offset:3072 sc1\n"
      "s_nop 6\n"
      "v_cvt_pk_bf16_f32 v27, v52, v12\n"
      "v_cvt_pk_bf16_f32 v39, v53, v13\n"
      "v_pk_max_i16 v27, v27, 0\n"
      "buffer_store_dwordx2 v[26:27], v0, s[28:31], s33 offen sc1\n"
      "v_cvt_pk_bf16_f32 v3, v54, v14\n"
      "v_pk_max_i16 v39, v39, 0\n"
      "buffer_store_dwordx2 v[38:39], v0, s[28:31], s33 offen offset:1024 sc1\n"
      "v_cvt_pk_bf16_f32 v5, v55, v15\n"
      "v_pk_max_i16 v3, v3, 0\n"
      "buffer_store_dwordx2 v[2:3], v0, s[28:31], s33 offen offset:2048 sc1\n"
      "v_pk_max_i16 v5, v5, 0\n"
      "buffer_store_dwordx2 v[4:5], v0, s[28:31], s33 offen offset:3072 sc1\n"
      ".Lend%=:\n"
      : "+{v0}"(tid)
      : "{s[0:1]}"(__builtin_amdgcn_kernarg_segment_ptr()), "{s2}"(blockIdx.x)
      : "memory", "m0", "scc", "vcc", "v1", "v2", "v3", "v4", "v5", "v6", "v7", "v8", "v9", "v10", "v11", "v12", "v13", "v14", "v15", "v16", "v17", "v18", "v19", "v20", "v21", "v22", "v23", "v24", "v25", "v26", "v27", "v28", "v29", "v30", "v31", "v32", "v33", "v34", "v35", "v36", "v37", "v38", "v39", "v40", "v41", "v42", "v43", "v44", "v45", "v46", "v47", "v48", "v49", "v50", "v51", "v52", "v53", "v54", "v55", "v56", "v57", "v58", "v59", "v60", "v61", "v62", "v63", "v64", "v65", "v66", "v67", "v68", "v69", "v70", "v71", "v72", "v73", "v74", "v75", "v76", "v77", "v78", "v79", "v80", "v81", "v82", "v83", "v84", "v85", "v86", "v87", "v88", "v89", "v90", "v91", "v92", "v93", "v94", "v95", "v96", "v97", "v98", "v99", "v100", "v101", "v102", "v103", "v104", "v105", "v106", "v107", "v108", "v109", "v110", "v111", "v112", "v113", "v114", "v115", "v116", "v117", "v118", "v119", "v120", "v121", "v122", "v123", "v124", "v125", "v126", "v127", "v128", "v129", "v130", "v131", "v132", "v133", "v134", "v135", "v136", "v137", "v138", "v139", "v140", "v141", "v142", "v143", "v144", "v145", "v146", "v147", "v148", "v149", "v150", "v151", "v152", "v153", "v154", "v155", "v156", "v157", "v158", "v159", "v160", "v161", "v162", "v163", "v164", "v165", "v166", "v167", "v168", "v169", "v170", "v171", "v172", "v173", "v174", "v175", "v176", "v177", "v178", "v179", "v180", "v181", "v182", "v183", "v184", "v185", "v186", "v187", "v188", "v189", "v190", "v191", "v192", "v193", "v194", "v195", "v196", "v197", "v198", "v199", "v200", "v201", "v202", "v203", "v204", "v205", "v206", "v207", "v208", "v209", "v210", "v211", "v212", "v213", "v214", "v215", "v216", "v217", "v218", "v219", "v220", "v221", "v222", "v223", "v224", "v225", "v226", "v227", "v228", "v229", "v230", "v231", "v232", "v233", "v234", "v235", "v236", "v237", "v238", "v239", "v240", "v241", "v242", "v243", "v244", "v245", "v246", "v247", "v248", "v249", "v250", "v251", "v252", "v253", "v254", "v255", "a0", "a1", "a2", "a3", "a4", "a5", "a6", "a7", "a8", "a9", "a10", "a11", "a12", "a13", "a14", "a15", "a16", "a17", "a18", "a19", "a20", "a21", "a22", "a23", "a24", "a25", "a26", "a27", "a28", "a29", "a30", "a31", "a32", "a33", "a34", "a35", "a36", "a37", "a38", "a39", "a40", "a41", "a42", "a43", "a44", "a45", "a46", "a47", "a48", "a49", "a50", "a51", "a52", "a53", "a54", "a55", "a56", "a57", "a58", "a59", "a60", "a61", "a62", "a63", "a64", "a65", "a66", "a67", "a68", "a69", "a70", "a71", "a72", "a73", "a74", "a75", "a76", "a77", "a78", "a79", "a80", "a81", "a82", "a83", "a84", "a85", "a86", "a87", "a88", "a89", "a90", "a91", "a92", "a93", "a94", "a95", "a96", "a97", "a98", "a99", "a100", "a101", "a102", "a103", "a104", "a105", "a106", "a107", "a108", "a109", "a110", "a111", "a112", "a113", "a114", "a115", "a116", "a117", "a118", "a119", "a120", "a121", "a122", "a123", "a124", "a125", "a126", "a127", "a128", "a129", "a130", "a131", "a132", "a133", "a134", "a135", "a136", "a137", "a138", "a139", "a140", "a141", "a142", "a143", "a144", "a145", "a146", "a147", "a148", "a149", "a150", "a151", "a152", "a153", "a154", "a155", "a156", "a157", "a158", "a159", "a160", "a161", "a162", "a163", "a164", "a165", "a166", "a167", "a168", "a169", "a170", "a171", "a172", "a173", "a174", "a175", "a176", "a177", "a178", "a179", "a180", "a181", "a182", "a183", "a184", "a185", "a186", "a187", "a188", "a189", "a190", "a191", "a192", "a193", "a194", "a195", "a196", "a197", "a198", "a199", "a200", "a201", "a202", "a203", "a204", "a205", "a206", "a207", "a208", "a209", "a210", "a211", "a212", "a213", "a214", "a215", "a216", "a217", "a218", "a219", "a220", "a221", "a222", "a223", "a224", "a225", "a226", "a227", "a228", "a229", "a230", "a231", "a232", "a233", "a234", "a235", "a236", "a237", "a238", "a239", "a240", "a241", "a242", "a243", "a244", "a245", "a246", "a247", "a248", "a249", "a250", "a251", "a252", "a253", "a254", "a255", "s3", "s4", "s5", "s6", "s7", "s8", "s9", "s10", "s11", "s12", "s13", "s14", "s15", "s16", "s17", "s18", "s19", "s20", "s21", "s22", "s23", "s24", "s25", "s26", "s27", "s28", "s29", "s30", "s31", "s32", "s33", "s34", "s35", "s36", "s37", "s38", "s39", "s40", "s41", "s42", "s43", "s44", "s45", "s46", "s47", "s48", "s49", "s50", "s51", "s52", "s53", "s54", "s55", "s56", "s57", "s58", "s59", "s60", "s61", "s62", "s63", "s64", "s65", "s66", "s67", "s68", "s69", "s70", "s71", "s72", "s73", "s74", "s75", "s76", "s77", "s78", "s79", "s80", "s81", "s82", "s83", "s84", "s85", "s86", "s87", "s88", "s89", "s90", "s91", "s92", "s93", "s94", "s95", "s96", "s97", "s98", "s99", "s100", "s101");
}

extern "C" void kernel_launch(void* const* d_in, const int* in_sizes, int n_in, void* d_out, int out_size,
                              void* d_ws, size_t ws_size, hipStream_t stream) {
  (void)hipFuncSetAttribute((const void*)autoint_kernel, hipFuncAttributeMaxDynamicSharedMemorySize, 163840);
  autoint_kernel<<<256, 256, 163840, stream>>>((const __bf16*)d_in[0], (const __bf16*)d_in[1], (const __bf16*)d_in[2],
                                               (const __bf16*)d_in[3], (const __bf16*)d_in[4], (__bf16*)d_out,
                                               (unsigned long long*)d_ws);
}
```
